```python
import torch

def _softmax(x, axis=-1):
    return torch.softmax(x, dim=axis)
def _gelu(x):
    return torch.nn.functional.gelu(x, approximate="tanh")
def _silu(x):
    return torch.nn.functional.silu(x)
def _sigmoid(x):
    return torch.sigmoid(x)
def _layernorm(x, axis=-1, eps=1e-5):
    m = x.mean(dim=axis, keepdim=True)
    v = x.var(dim=axis, keepdim=True, unbiased=False)
    return (x - m) / torch.sqrt(v + eps)
def _scatter_add(tgt, idx, src, axis=0):
    return tgt.clone().index_add_(axis, idx, src)
def _segment_sum(src, seg, n):
    out = torch.zeros((n,) + src.shape[1:], dtype=src.dtype)
    return out.index_add_(0, seg, src)
def _pad(x, pw):
    flat = tuple(v for pair in reversed(pw) for v in pair)
    return torch.nn.functional.pad(x, flat)

kernel_name = 'parallel_sum__softmax_full_mha__glu_gated_glu__prompt'

def setup_inputs():

    _w = (lambda a: (lambda t: t.to(torch.bfloat16) if t.is_floating_point() else t)(torch.as_tensor(a)))
    def _A_setup():
        B, S, D = 1, 2048, 512
        return {
            "x": _w((lambda *s: torch.randn(*s, dtype=torch.bfloat16) * 0.05)(B, S, D)),
            "wqkv": _w((lambda *s: torch.randn(*s, dtype=torch.bfloat16) * 0.2)(D, 3 * D)),
            "wo": _w((lambda *s: torch.randn(*s, dtype=torch.bfloat16) * 0.1)(D, D)),
        }
    _A_in = _A_setup()

    def _B_setup():
        B, S, D, F = 1, 2048, 512, 1792
        return {
            "x": _w((lambda *s: torch.randn(*s, dtype=torch.bfloat16) * 0.05)(B, S, D)),
            "w_gate": _w((lambda *s: torch.randn(*s, dtype=torch.bfloat16) * 0.8)(D, F)),
            "w_up": _w((lambda *s: torch.randn(*s, dtype=torch.bfloat16) * 0.2)(D, F)),
            "w_down": _w((lambda *s: torch.randn(*s, dtype=torch.bfloat16) * 0.2)(F, D)),
        }
    _B_in = _B_setup()

    return {
        "A_x": _A_in["x"],
        "A_wqkv": _A_in["wqkv"],
        "A_wo": _A_in["wo"],
        "B_x": _B_in["x"],
        "B_w_gate": _B_in["w_gate"],
        "B_w_up": _B_in["w_up"],
        "B_w_down": _B_in["w_down"],
    }

def reference(A_x, A_wqkv, A_wo, B_x, B_w_gate, B_w_up, B_w_down):
    def _A_ref(x, wqkv, wo):
        B, S, D = x.shape
        H = 32; Dh = D // H
        qkv = (lambda x, *d: x.permute(*d))((x @ wqkv).reshape(B, S, 3, H, Dh), 2, 0, 3, 1, 4)
        q, k, v = qkv[0], qkv[1], qkv[2]
        scale = Dh ** -0.5
        scores = torch.einsum("bhqd,bhkd->bhqk", q, k) * scale
        p = _softmax(scores, axis=-1)
        o = torch.einsum("bhqk,bhkd->bhqd", p, v)
        return (lambda x, *d: x.permute(*d))(o, 0, 2, 1, 3).reshape(B, S, D) @ wo

    def _B_ref(x, w_gate, w_up, w_down):
        g = torch.einsum("bsd,df->bsf", x, w_gate)
        u = torch.einsum("bsd,df->bsf", x, w_up)
        h = _sigmoid(g) * u
        return torch.einsum("bsf,fd->bsd", h, w_down)

    x = A_x
    xn = _layernorm(x, axis=-1)
    return x + _A_ref(xn, A_wqkv, A_wo) + _B_ref(xn, B_w_gate, B_w_up, B_w_down)
```

```cpp
#include <hip/hip_runtime.h>
#include <hip/hip_bf16.h>
typedef unsigned short u16;
__device__ __forceinline__ float b2f(u16 b){ return __uint_as_float(((unsigned)b)<<16); }
__device__ __forceinline__ u16 f2b(float f){ unsigned u=__float_as_uint(f); u += 0x7fffu + ((u>>16)&1u); return (u16)(u>>16); }
constexpr int S=2048, D=512, F=1792, H=32, DH=16;

__global__ void k_ln(const u16* x, float* xn){
  int r=blockIdx.x; int t=threadIdx.x;
  float v[8]; float s=0;
  for(int i=0;i<8;i++){ v[i]=b2f(x[r*D+t*8+i]); s+=v[i]; }
  for(int o=32;o;o>>=1) s+=__shfl_xor(s,o);
  float m=s/D; float q=0;
  for(int i=0;i<8;i++){ float d=v[i]-m; q+=d*d; }
  for(int o=32;o;o>>=1) q+=__shfl_xor(q,o);
  float rs=rsqrtf(q/D+1e-5f);
  for(int i=0;i<8;i++) xn[r*D+t*8+i]=b2f(f2b((v[i]-m)*rs));
}
__global__ void k_gemm(const float* A, const u16* B, float* C, int K, int N){
  int n=blockIdx.x*64+threadIdx.x; int m0=blockIdx.y*8;
  float acc[8]={0,0,0,0,0,0,0,0};
  for(int k=0;k<K;k++){ float b=b2f(B[(long)k*N+n]);
    for(int i=0;i<8;i++) acc[i]+=A[(long)(m0+i)*K+k]*b; }
  for(int i=0;i<8;i++) C[(long)(m0+i)*N+n]=b2f(f2b(acc[i]));
}
__global__ void k_attn(const float* qkv, float* o){
  int h=blockIdx.y; int q=blockIdx.x*64+threadIdx.x;
  float qv[DH]; for(int d=0;d<DH;d++) qv[d]=qkv[(long)q*1536+h*DH+d];
  float m=-1e30f,l=0,acc[DH]; for(int d=0;d<DH;d++) acc[d]=0;
  for(int k=0;k<S;k++){ float s=0; for(int d=0;d<DH;d++) s+=qv[d]*qkv[(long)k*1536+512+h*DH+d];
    s*=0.25f; float mn=fmaxf(m,s); float a=__expf(m-mn), p=__expf(s-mn); l=l*a+p;
    for(int d=0;d<DH;d++) acc[d]=acc[d]*a+p*qkv[(long)k*1536+1024+h*DH+d]; m=mn; }
  for(int d=0;d<DH;d++) o[(long)q*D+h*DH+d]=b2f(f2b(acc[d]/l));
}
__global__ void k_glu(const float* g,const float* u,float* h){ long i=(long)blockIdx.x*256+threadIdx.x; float s=1.f/(1.f+__expf(-g[i])); h[i]=b2f(f2b(s*u[i])); }
__global__ void k_fin(const u16* x,const float* a,const float* b,u16* out){ long i=(long)blockIdx.x*256+threadIdx.x; out[i]=f2b(b2f(x[i])+a[i]+b[i]); }

extern "C" void kernel_launch(void* const* d_in, const int* in_sizes, int n_in, void* d_out, int out_size, void* d_ws, size_t ws_size, hipStream_t stream){
  const u16* x=(const u16*)d_in[0]; const u16* wqkv=(const u16*)d_in[1]; const u16* wo=(const u16*)d_in[2];
  const u16* wg=(const u16*)d_in[4]; const u16* wu=(const u16*)d_in[5]; const u16* wd=(const u16*)d_in[6];
  float* ws=(float*)d_ws; float* xn=ws; float* qkv=xn+S*D; float* o=qkv+S*1536; float* g=o+S*D; float* u=g+S*F; float* h=u+S*F; float* a=h+S*F; float* b=a+S*D;
  k_ln<<<S,64,0,stream>>>(x,xn);
  k_gemm<<<dim3(1536/64,S/8),64,0,stream>>>(xn,wqkv,qkv,D,1536);
  k_gemm<<<dim3(F/64,S/8),64,0,stream>>>(xn,wg,g,D,F);
  k_gemm<<<dim3(F/64,S/8),64,0,stream>>>(xn,wu,u,D,F);
  k_attn<<<dim3(S/64,H),64,0,stream>>>(qkv,o);
  k_glu<<<S*F/256,256,0,stream>>>(g,u,h);
  k_gemm<<<dim3(D/64,S/8),64,0,stream>>>(o,wo,a,D,D);
  k_gemm<<<dim3(D/64,S/8),64,0,stream>>>(h,wd,b,F,D);
  k_fin<<<S*D/256,256,0,stream>>>(x,a,b,(u16*)d_out);
}
```

```cpp
#include <hip/hip_runtime.h>
#include <hip/hip_bf16.h>

typedef __bf16 bf16x8 __attribute__((ext_vector_type(8)));
typedef __bf16 bf16x16 __attribute__((ext_vector_type(16)));
typedef __bf16 bf16x2 __attribute__((ext_vector_type(2)));
typedef short s16x4 __attribute__((ext_vector_type(4)));
typedef float f32x16 __attribute__((ext_vector_type(16)));
typedef float f32x8 __attribute__((ext_vector_type(8)));
typedef float f32x4 __attribute__((ext_vector_type(4)));
typedef float f32x2 __attribute__((ext_vector_type(2)));
typedef unsigned u32x4 __attribute__((ext_vector_type(4)));
typedef unsigned u32x2 __attribute__((ext_vector_type(2)));
typedef __amdgpu_buffer_rsrc_t rsrc_t;
typedef __attribute__((address_space(3))) char* lptr;
#define DI __device__ __forceinline__
#define SBAR() __builtin_amdgcn_sched_barrier(0)
#define MFMA(a, b, c) __builtin_amdgcn_mfma_f32_32x32x16_bf16(a, b, c, 0, 0, 0)
#define SMF(a, b, c, idx) __builtin_amdgcn_smfmac_f32_32x32x32_bf16(a, __builtin_bit_cast(bf16x16, b), c, idx, 0, 0)
#define LDS8(p) (*(const __attribute__((address_space(3))) bf16x8*)(p))
#define PIN(x) asm volatile("" : "+v"(x))

#define DBG_STAMPS 0

constexpr unsigned WS_EPOCH = 0;
constexpr unsigned WS_F0 = 4096, WS_F1 = WS_F0 + 2048, WS_F2 = WS_F1 + 2048;
constexpr unsigned WS_F3 = 16384;
constexpr unsigned WS_STAT = 32768;
constexpr unsigned WS_CS = 65536;
constexpr unsigned WS_Q = 1u << 20;
constexpr unsigned WS_KV = WS_Q + (2u << 20);
constexpr unsigned WS_O = WS_KV + (4u << 20);
constexpr unsigned WS_W2 = WS_O + (2u << 20);
constexpr unsigned WS_H = WS_W2 + (3u << 20);
constexpr unsigned WS_DBG = WS_H + (7u << 20);
constexpr unsigned WS_END = WS_DBG + (1u << 20);
constexpr int SLOT = 40960, L_XCS = 122880, L_PRIV = 131072, L_ONES = 147456, LDS_BYTES = 163840;
constexpr unsigned SPIN_CAP = 1u << 15;

DI rsrc_t mk(const void* p, unsigned bytes) { return __builtin_amdgcn_make_buffer_rsrc((void*)p, (short)0, (int)bytes, 0x00020000); }
DI unsigned cvtpk(float lo, float hi) { f32x2 v = {lo, hi}; return __builtin_bit_cast(unsigned, __builtin_convertvector(v, bf16x2)); }
DI float bflo(unsigned u) { return __uint_as_float(u << 16); }
DI float bfhi(unsigned u) { return __uint_as_float(u & 0xffff0000u); }
DI float rbf(float f) { return bflo(cvtpk(f, f)); }
DI s16x4 trd(lptr p) { return __builtin_amdgcn_ds_read_tr16_b64_v4i16((__attribute__((address_space(3))) s16x4*)p); }
DI bf16x8 trfrag(lptr p, int step) { s16x4 a = trd(p), b = trd(p + step); return __builtin_bit_cast(bf16x8, __builtin_shufflevector(a, b, 0, 1, 2, 3, 4, 5, 6, 7)); }
DI void dma(unsigned m0v, unsigned voff, rsrc_t r, unsigned soff) {
  asm volatile("s_mov_b32 m0, %0\n\ts_nop 0\n\tbuffer_load_dwordx4 %1, %2, %3 offen lds" :: "s"(m0v), "v"(voff), "s"(r), "s"(soff) : "memory"); }
DI void dma_sc1(unsigned m0v, unsigned voff, rsrc_t r, unsigned soff) {
  asm volatile("s_mov_b32 m0, %0\n\ts_nop 0\n\tbuffer_load_dwordx4 %1, %2, %3 offen sc1 lds" :: "s"(m0v), "v"(voff), "s"(r), "s"(soff) : "memory"); }
#define ALD4(dst, voff, r, soff) asm volatile("buffer_load_dwordx4 %0, %1, %2, %3 offen" : "=v"(dst) : "v"(voff), "s"(r), "s"(soff) : "memory")
#define ALD4C(dst, voff, r, soff) asm volatile("buffer_load_dwordx4 %0, %1, %2, %3 offen sc1" : "=v"(dst) : "v"(voff), "s"(r), "s"(soff) : "memory")
#define ALD2C(dst, voff, r, soff) asm volatile("buffer_load_dwordx2 %0, %1, %2, %3 offen sc1" : "=v"(dst) : "v"(voff), "s"(r), "s"(soff) : "memory")
#define ALD1C(dst, voff, r, soff) asm volatile("buffer_load_dword %0, %1, %2, %3 offen sc1" : "=v"(dst) : "v"(voff), "s"(r), "s"(soff) : "memory")
#define WAITV(n) asm volatile("s_waitcnt vmcnt(%0)" :: "i"(n) : "memory")
#define WAITBAR(n) asm volatile("s_waitcnt vmcnt(%0) lgkmcnt(0)\n\ts_barrier" :: "i"(n) : "memory")
#define LBAR() asm volatile("s_waitcnt lgkmcnt(0)\n\ts_barrier" ::: "memory")
DI void st16(u32x4 v, rsrc_t r, unsigned voff, unsigned soff) {
  asm volatile("buffer_store_dwordx4 %0, %1, %2, %3 offen sc1\n\ts_nop 1" :: "v"(v), "v"(voff), "s"(r), "s"(soff) : "memory"); }
DI void st8(u32x2 v, rsrc_t r, unsigned voff, unsigned soff) { __builtin_amdgcn_raw_buffer_store_b64(v, r, voff, soff, 16); }
DI void st4(unsigned v, rsrc_t r, unsigned voff, unsigned soff) { __builtin_amdgcn_raw_buffer_store_b32(v, r, voff, soff, 16); }
DI bf16x8 ld16c(rsrc_t r, unsigned voff, unsigned soff) { return __builtin_bit_cast(bf16x8, __builtin_amdgcn_raw_buffer_load_b128(r, voff, soff, 16)); }
template <int CTRL, int RM> DI float dppadd(float v) { return v + __int_as_float(__builtin_amdgcn_update_dpp(0, __float_as_int(v), CTRL, RM, 0xf, true)); }
DI float wave_sum(float v) {
  v = dppadd<0x111, 0xf>(v); v = dppadd<0x112, 0xf>(v); v = dppadd<0x114, 0xf>(v); v = dppadd<0x118, 0xf>(v);
  v = dppadd<0x142, 0xa>(v); v = dppadd<0x143, 0xc>(v);
  return __int_as_float(__builtin_amdgcn_readlane(__float_as_int(v), 63)); }
DI void wait_flags(u32x2 f, unsigned voff, rsrc_t ws, unsigned t0, unsigned t1) {
#pragma unroll 1
  for (unsigned spins = 0; spins < SPIN_CAP; ++spins) {
    if (__all(f[0] == t0 && f[1] == t1)) break;
    __builtin_amdgcn_s_sleep(2);
    asm volatile("buffer_load_dwordx2 %0, %1, %2, 0 offen sc1\n\ts_waitcnt vmcnt(0)" : "=&v"(f) : "v"(voff), "s"(ws) : "memory");
  }
}
#define STAMP(i)

DI u32x4 pack8(const f32x16& a, int o, float s) {
  return (u32x4){cvtpk(a[o] * s, a[o + 1] * s), cvtpk(a[o + 2] * s, a[o + 3] * s), cvtpk(a[o + 4] * s, a[o + 5] * s), cvtpk(a[o + 6] * s, a[o + 7] * s)}; }
DI u32x4 glu8(const f32x16& a, float nc, float sd) {
  float h[8];
#pragma unroll
  for (int i = 0; i < 8; ++i) { const float e = __builtin_amdgcn_exp2f(a[i] * nc); h[i] = a[8 + i] * __builtin_amdgcn_rcpf(__builtin_fmaf(e, sd, sd)); }
  return (u32x4){cvtpk(h[0], h[1]), cvtpk(h[2], h[3]), cvtpk(h[4], h[5]), cvtpk(h[6], h[7])}; }

__global__ __launch_bounds__(512, 2) void fused_block(const void* __restrict__ px, const void* __restrict__ pwqkv, const void* __restrict__ pwo,
                                                      const void* __restrict__ pwg, const void* __restrict__ pwu, const void* __restrict__ pwd,
                                                      void* __restrict__ pout, void* __restrict__ pws) {
  extern __shared__ __attribute__((aligned(16))) char smem[];
  asm volatile("" :: "s"(px), "s"(pwqkv), "s"(pwo), "s"(pwg), "s"(pwu), "s"(pwd), "s"(pout), "s"(pws));
  const lptr lds = (lptr)smem;
  const unsigned lds0 = (unsigned)(size_t)lds;
  const int tid = threadIdx.x, lane = tid & 63, w = __builtin_amdgcn_readfirstlane(tid >> 6);
  const int b = blockIdx.x, xcd = b & 7, bi = b >> 3;
  const int r32 = lane & 31, hi = lane >> 5;
  const rsrc_t ws = mk(pws, WS_END), rx = mk(px, 2048 * 1024);
  STAMP(0);
  const int rb = (xcd & 1) * 8 + (bi & 7), set = (xcd >> 1) * 4 + (bi >> 3), Q4 = set >> 2, sj = set & 3;
  const bool tb_ = sj == 0;
  const int p1 = 7 * Q4 + 2 * sj - 1, p2 = 7 * Q4 + 2 * sj;
  const int rot = rb >> 1, half = rb & 1;
  const int tt = w & 3, cg = w >> 2;
  const unsigned col0 = tb_ ? 128 * Q4 : (sj - 1) * 512 + 128 * Q4 + 64;
  const unsigned col1 = tb_ ? 512 + 128 * Q4 : 1536 + 64 * p1, col2 = tb_ ? 1024 + 128 * Q4 : 3328 + 64 * p1;
  const unsigned str12 = tb_ ? 3072 : 3584;
  const rsrc_t rg0 = mk((const char*)pwqkv + col0 * 2, 512 * 3072 - col0 * 2);
  const unsigned bo1 = tb_ ? col1 * 2 : p1 * 128, bo2 = tb_ ? col2 * 2 : p1 * 128;
  const rsrc_t rg1 = mk((const char*)(tb_ ? pwqkv : pwg) + bo1, 512 * str12 - bo1), rg2 = mk((const char*)(tb_ ? pwqkv : pwu) + bo2, 512 * str12 - bo2);
  const rsrc_t rg3 = mk((const char*)pwg + p2 * 128, 512 * 3584 - p2 * 128), rg4 = mk((const char*)pwu + p2 * 128, 512 * 3584 - p2 * 128);
  const rsrc_t rge = half ? rg4 : rg3;
  const unsigned l3 = lane >> 3, l7 = lane & 7, sw = ((lane >> 4) & 1) * 4;
  const unsigned vx0 = (rb * 128 + w * 16 + l3) * 1024 + ((l7 ^ (lane >> 4)) * 16), vx1 = (rb * 128 + w * 16 + 8 + l3) * 1024 + ((l7 ^ (4 + (lane >> 4))) * 16);
  const unsigned vg0 = (w * 8 + l3) * 3072 + (l7 ^ sw) * 16, vg1 = (w * 8 + l3) * str12 + (l7 ^ sw) * 16, vg2 = (w * 8 + l3) * str12 + (l7 ^ sw ^ 2) * 16;
  const unsigned vg3 = (w * 8 + l3) * 3584 + (l7 ^ sw) * 16, vg4 = (w * 8 + l3) * 3584 + (l7 ^ sw ^ 2) * 16;
  const unsigned mx = lds0 + w * 2048, mw = lds0 + w * 1024;
#define KC(c) (unsigned)(((c) + rot) & 7)
#define DMA1(c, i) do { const unsigned sl_ = ((c) % 3) * SLOT; \
    if ((i) == 0) dma(mx + sl_, vx0, rx, KC(c) * 128); if ((i) == 1) dma(mx + sl_ + 1024, vx1, rx, KC(c) * 128); \
    if ((i) == 2) dma(mw + sl_ + 16384, vg0, rg0, KC(c) * (64 * 3072)); if ((i) == 3) dma(mw + sl_ + 24576, vg1, rg1, KC(c) * 64 * str12); \
    if ((i) == 4) dma(mw + sl_ + 32768, vg2, rg2, KC(c) * 64 * str12); } while (0)
#define DMA2(c, i) do { const unsigned sl_ = ((c) % 3) * SLOT + (i) * 8192, kc_ = KC(2 * ((c) - 8) + ((i) >> 1)) * (64 * 3584); \
    if ((i) & 1) dma(mw + sl_, vg4, rg4, kc_); else dma(mw + sl_, vg3, rg3, kc_); } while (0)
  u32x2 ep; u32x4 xr; unsigned pf;
  const unsigned zero = 0;
  ALD2C(ep, zero, ws, WS_EPOCH);
  const unsigned srow = rb * 128 + set * 8 + w;
  { const unsigned v = lane * 16; ALD4(xr, v, rx, srow * 1024); }
  { unsigned long long pc; asm volatile("s_getpc_b64 %0" : "=s"(pc));
    const rsrc_t rc = mk((const void*)(pc & ~127ull), 20480);
    const unsigned v = (w == 7 && lane < 8) ? (bi * 8 + lane) * 128 : 0x7fffff00u;
    asm volatile("buffer_load_dword %0, %1, %2, 0 offen" : "=v"(pf) : "v"(v), "s"(rc) : "memory"); }
  dma(mw + L_XCS, vg3, rge, rot * (64 * 3584));
#pragma unroll
  for (int c = 0; c < 3; ++c) { DMA1(c, 0); DMA1(c, 1); DMA1(c, 2); DMA1(c, 3); DMA1(c, 4); }
  asm volatile("s_waitcnt vmcnt(16)" : "+v"(ep), "+v"(xr), "+v"(pf) :: "memory");
  const unsigned e0 = __builtin_amdgcn_readfirstlane(ep[0]), e1 = __builtin_amdgcn_readfirstlane(ep[1]);
  const unsigned t0 = e0 * 0x9E3779B1u + 0x7F4A7C15u, t1 = (e1 ^ 0x85EBCA6Bu) + e0;
  const u32x2 tag = {t0, t1};
  {
    float v[8];
#pragma unroll
    for (int i = 0; i < 4; ++i) { v[2 * i] = bflo(xr[i]); v[2 * i + 1] = bfhi(xr[i]); }
    const float mean = wave_sum(((v[0] + v[1]) + (v[2] + v[3])) + ((v[4] + v[5]) + (v[6] + v[7]))) * (1.f / 512);
    float q = 0.f;
#pragma unroll
    for (int i = 0; i < 8; ++i) { const float d = v[i] - mean; q = __builtin_fmaf(d, d, q); }
    const float var = wave_sum(q) * (1.f / 512);
    const float sdv = __builtin_amdgcn_sqrtf(rbf(rbf(var) + 1e-5f));
    if (lane == 0) st4(cvtpk(-mean, sdv), ws, WS_STAT + srow * 4, 0);
  }
  const int q4 = (lane & 15) >> 2, p4 = lane & 3, g16 = (lane >> 4) & 1, swr = (q4 >> 1) * 4;
  const int wrow = (8 * hi + q4) * 128 + (p4 & 1) * 8;
  const int pq = 4 * cg + 2 * g16 + (p4 >> 1);
  const int gl0 = g16 * 8192 + (((4 * cg + (p4 >> 1)) ^ swr ^ (2 * g16)) * 16), gl1 = g16 * 8192 + (((4 * cg + 2 + (p4 >> 1)) ^ swr ^ (2 * g16)) * 16);
  const lptr wp0 = lds + 16384 + wrow + (pq ^ swr) * 16;
  const lptr wp1 = lds + 24576 + wrow + (tb_ ? (pq ^ swr) * 16 : gl0);
  const lptr wp2 = lds + 24576 + wrow + (tb_ ? 8192 + (pq ^ swr ^ 2) * 16 : gl1);
  const lptr wp3 = lds + wrow + gl0, wp4 = lds + wrow + gl1;
  lptr xp[4];
#pragma unroll
  for (int s = 0; s < 4; ++s) xp[s] = lds + tt * 4096 + r32 * 128 + (((2 * s + hi) ^ ((r32 >> 1) & 7)) * 16);
  STAMP(1);
  WAITBAR(10);
  STAMP(2);
  if (w < 5) {
    const int pc_ = (w < 3 ? 4 * half : 4 * (w - 3)) + 2 * g16 + (p4 >> 1);
    const lptr cp = lds + (w < 3 ? 16384 + w * 8192 : L_XCS) + wrow + ((pc_ ^ swr ^ (w == 2 ? 2 : 0)) * 16);
    const __bf16 o1 = (__bf16)1.0f; const bf16x8 ones = {o1, o1, o1, o1, o1, o1, o1, o1};
    f32x16 cs = {};
#pragma unroll
    for (int s = 0; s < 4; ++s) cs = MFMA(ones, trfrag(cp + s * 2048, 512), cs);
    const float ch = rbf(cs[0]);
    const unsigned ccol = (w == 0 ? col0 : w == 1 ? col1 : w == 2 ? col2 : (half ? 3328 : 1536) + 64 * p2) + (w < 3 ? 32 * half : 32 * (w - 3));
    if (lane < 32) st4(cvtpk(ch, cs[0] - ch), ws, WS_CS + (ccol + lane) * 4, rot * 20480);
  }
  bf16x8 xf[32], wa[3], wb[3];
  f32x16 acc0 = {}, acc1 = {}, acc2 = {};
  u32x2 pl; unsigned stat; u32x4 cs0, cs1, cs2;
  const unsigned vf0 = WS_F0 + (lane < 16 ? rb * 16 + lane : (lane & 15) * 16 + set) * 8;
  const unsigned gcol = (r32 & 15) + 32 * cg;
  const unsigned vc0 = WS_CS + hi * 81920 + (col0 + 32 * cg + r32) * 4;
  const unsigned vc1 = WS_CS + hi * 81920 + (tb_ ? col1 + 32 * cg + r32 : (r32 < 16 ? col1 : col2) + gcol) * 4;
  const unsigned vc2 = WS_CS + hi * 81920 + (tb_ ? col2 + 32 * cg + r32 : (r32 < 16 ? col1 : col2) + gcol + 16) * 4;
#define LDCS(dst, v) do { ALD1C(dst[0], v, ws, 0); ALD1C(dst[1], v, ws, 20480); ALD1C(dst[2], v, ws, 40960); ALD1C(dst[3], v, ws, 61440); } while (0)
#define RD1(W, ks) do { const int o_ = (((ks) >> 2) % 3) * SLOT; xf[ks] = LDS8(xp[(ks) & 3] + o_); W[0] = trfrag(wp0 + o_ + ((ks) & 3) * 2048, 512); } while (0)
#define RD1B(W, ks) do { const int o_ = (((ks) >> 2) % 3) * SLOT; W[1] = trfrag(wp1 + o_ + ((ks) & 3) * 2048, 512); } while (0)
#define RD1C(W, ks) do { const int o_ = (((ks) >> 2) % 3) * SLOT; W[2] = trfrag(wp2 + o_ + ((ks) & 3) * 2048, 512); } while (0)
#define RD2A(W, ks) do { const int o_ = ((8 + ((ks) >> 3)) % 3) * SLOT + (((ks) >> 2) & 1) * 16384 + ((ks) & 3) * 2048; W[0] = trfrag(wp3 + o_, 512); } while (0)
#define RD2B(W, ks) do { const int o_ = ((8 + ((ks) >> 3)) % 3) * SLOT + (((ks) >> 2) & 1) * 16384 + ((ks) & 3) * 2048; W[1] = trfrag(wp4 + o_, 512); } while (0)
  RD1(wa, 0); RD1B(wa, 0); RD1C(wa, 0);
#define STEP1(ks, WC, WN) do { constexpr int c_ = ((ks) + 1) >> 2, s_ = ((ks) + 1) & 3; \
    if (s_ == 0) { SBAR(); WAITBAR(c_ < 7 ? 5 : c_ == 7 ? 17 : 4); \
      if (c_ == 2 && tid == 0) st8(tag, ws, WS_F0 + (rb * 16 + set) * 8, 0); \
      if (c_ == 5) ALD2C(pl, vf0, ws, 0); \
      if (c_ == 6) { PIN(pl); wait_flags(pl, vf0, ws, t0, t1); { const unsigned v_ = WS_STAT + (rb * 128 + tt * 32 + r32) * 4; ALD1C(stat, v_, ws, 0); } LDCS(cs0, vc0); LDCS(cs1, vc1); LDCS(cs2, vc2); } \
      if (c_ == 8) { PIN(stat); PIN(cs0); PIN(cs1); PIN(cs2); } } \
    SBAR(); \
    if ((ks) < 31) RD1(WN, ((ks) + 1) & 31); else RD2A(WN, 0); \
    if (c_ >= 1 && c_ <= 5) DMA1(c_ + 2, s_); if (c_ == 6 || c_ == 7) DMA2(c_ + 2, s_); SBAR(); \
    acc0 = MFMA(WC[0], xf[ks], acc0); SBAR(); \
    if ((ks) < 31) RD1B(WN, ((ks) + 1) & 31); else RD2B(WN, 0); SBAR(); \
    acc1 = MFMA(WC[1], xf[ks], acc1); SBAR(); \
    if ((ks) < 31) RD1C(WN, ((ks) + 1) & 31); \
    if (c_ >= 1 && c_ <= 5 && s_ == 0) DMA1(c_ + 2, 4); SBAR(); \
    acc2 = MFMA(WC[2], xf[ks], acc2); SBAR(); } while (0)
#define STEP1x4(k) STEP1(k, wa, wb); STEP1(k + 1, wb, wa); STEP1(k + 2, wa, wb); STEP1(k + 3, wb, wa)
  STEP1x4(0); STEP1x4(4); STEP1x4(8); STEP1x4(12); STEP1x4(16); STEP1x4(20); STEP1x4(24); STEP1x4(28);
  STAMP(3);
  const unsigned nm2 = (stat & 0xffffu) * 0x10001u;
  const bf16x8 nmf = __builtin_bit_cast(bf16x8, (u32x4){nm2, nm2, nm2, nm2});
  const float sd = bfhi(stat), rstd = __builtin_amdgcn_rcpf(sd), gsc = -1.4426950408889634f * rstd;
  acc0 = MFMA(__builtin_bit_cast(bf16x8, cs0), nmf, acc0); acc1 = MFMA(__builtin_bit_cast(bf16x8, cs1), nmf, acc1); acc2 = MFMA(__builtin_bit_cast(bf16x8, cs2), nmf, acc2);
  const unsigned l16 = lane * 16;
  const int tile = rb * 4 + tt;
  {
    const int hd = (tb_ ? 8 * Q4 : 8 * Q4 + 4) + 2 * cg;
    const lptr pv = lds + L_PRIV + w * 2048;
    auto put_q = [&](const f32x16& a) { const float s = rstd * (0.25f * 1.4426950408889634f);
      st16(pack8(a, 0, s), ws, l16, WS_Q + (hd * 64 + tile) * 1024); st16(pack8(a, 8, s), ws, l16, WS_Q + (hd * 64 + 64 + tile) * 1024); };
    auto put_k = [&](const f32x16& a) {
      st16(pack8(a, 0, rstd), ws, l16, WS_KV + hd * 131072 + tile * 1024); st16(pack8(a, 8, rstd), ws, l16, WS_KV + (hd + 1) * 131072 + tile * 1024); };
    auto put_v = [&](const f32x16& a) {
      const u32x4 u = pack8(a, 0, rstd), v = pack8(a, 8, rstd);
      typedef __attribute__((address_space(3))) u32x2* l2p;
      const lptr d = pv + r32 * 64 + hi * 8;
      *(l2p)(d) = (u32x2){u[0], u[1]}; *(l2p)(d + 16) = (u32x2){u[2], u[3]}; *(l2p)(d + 32) = (u32x2){v[0], v[1]}; *(l2p)(d + 48) = (u32x2){v[2], v[3]};
      asm volatile("s_waitcnt lgkmcnt(0)" ::: "memory");
      const lptr s = pv + (8 * hi + q4) * 64 + g16 * 32 + p4 * 8;
      const unsigned vo = g16 * 131072 + (hi * 16 + (lane & 15)) * 16;
      st16(__builtin_bit_cast(u32x4, trfrag(s, 256)), ws, vo, WS_KV + hd * 131072 + 65536 + tile * 1024);
      st16(__builtin_bit_cast(u32x4, trfrag(s + 1024, 256)), ws, vo, WS_KV + hd * 131072 + 65536 + tile * 1024 + 512); };
    if (tb_) { put_k(acc1); put_v(acc2); }
    if (sj <= 1) put_q(acc0); else if (sj == 2) put_k(acc0); else put_v(acc0);
    DMA2(10, 0); DMA2(10, 1); DMA2(10, 2); DMA2(10, 3);
    if (!tb_) { st16(glu8(acc1, gsc, sd), ws, l16, WS_H + (tile * 112 + 4 * p1 + 2 * cg) * 1024); st16(glu8(acc2, gsc, sd), ws, l16, WS_H + (tile * 112 + 4 * p1 + 2 * cg + 1) * 1024); }
  }
  STAMP(4);
  const int ah = xcd * 4 + (bi & 3), aq = bi >> 2;
  const int ks2 = bi + 32 * w;
  unsigned vf1;
  { const int gq = (ah >> 3) * 4, odd = (ah >> 2) & 1;
    vf1 = WS_F1 + (lane < 16 ? lane * 16 + gq + 2 * odd : lane < 32 ? (lane - 16) * 16 + gq + 3 * odd : (2 * aq + (lane & 1)) * 16 + gq + odd) * 8; }
  const unsigned vc3 = WS_CS + hi * 81920 + ((r32 < 16 ? 1536 : 3328) + 64 * p2 + gcol) * 4, vc4 = vc3 + 64;
  u32x4 qf4;
  acc0 = (f32x16){}; acc1 = (f32x16){};
#define KVD(i) dma_sc1(mw + (i) * 8192, l16, ws, WS_KV + ah * 131072 + (i) * 8192 + w * 1024)
#define STEP2(ks, WC, WN) do { constexpr int c_ = 8 + (((ks) + 1) >> 3), s_ = ((ks) + 1) & 7; \
    if (s_ == 0) { SBAR(); if (c_ == 9) WAITBAR(4); else if (c_ == 10) WAITBAR(14); else if (c_ == 11) WAITBAR(0); else LBAR(); \
      if (c_ == 9 && tid == 0) st8(tag, ws, WS_F1 + (rb * 16 + set) * 8, 0); \
      if (c_ == 10) ALD2C(pl, vf1, ws, 0); \
      if (c_ == 11) { PIN(pl); PIN(cs0); PIN(cs1); wait_flags(pl, vf1, ws, t0, t1); ALD4C(qf4, l16, ws, WS_Q + (ah * 64 + aq * 8 + w) * 1024); } } \
    SBAR(); \
    if ((ks) < 31) RD2A(WN, ((ks) + 1) & 31); \
    if (c_ == 9 && s_ < 4) DMA2(11, s_); \
    if (c_ == 9 && s_ == 4) { const rsrc_t r_ = ks2 < 112 ? mk(pwd, 1792 * 1024) : mk(pwo, 512 * 1024); const unsigned so_ = (ks2 < 112 ? ks2 : ks2 < 144 ? ks2 - 112 : 0) * 16384 + xcd * 128; \
      const unsigned v_ = ks2 < 144 ? l3 * 1024 + (l7 ^ sw) * 16 : 0x7fffff00u; dma(lds0 + L_PRIV + w * 2048, v_, r_, so_); dma(lds0 + L_PRIV + w * 2048 + 1024, v_, r_, so_ + 8192); } \
    if (c_ == 9 && s_ == 5) LDCS(cs0, vc3); if (c_ == 9 && s_ == 6) LDCS(cs1, vc4); \
    if (c_ == 11) KVD(s_); if (c_ == 12) { KVD(10); KVD(11); KVD(12); } SBAR(); \
    acc0 = MFMA(WC[0], xf[ks], acc0); SBAR(); \
    if ((ks) < 31) RD2B(WN, ((ks) + 1) & 31); \
    if (c_ == 11 && s_ < 2) KVD(8 + s_); if (c_ == 12) { KVD(13); KVD(14); KVD(15); } SBAR(); \
    acc1 = MFMA(WC[1], xf[ks], acc1); SBAR(); } while (0)
#define STEP2x4(k) STEP2(k, wa, wb); STEP2(k + 1, wb, wa); STEP2(k + 2, wa, wb); STEP2(k + 3, wb, wa)
  STEP2x4(0); STEP2x4(4); STEP2x4(8); STEP2x4(12); STEP2x4(16); STEP2x4(20); STEP2x4(24); STEP2x4(28);
  STAMP(5);
  acc0 = MFMA(__builtin_bit_cast(bf16x8, cs0), nmf, acc0); acc1 = MFMA(__builtin_bit_cast(bf16x8, cs1), nmf, acc1);
  if (ks2 < 144) {
    const lptr s = lds + L_PRIV + w * 2048 + (4 * hi + q4) * 128 + (p4 & 1) * 8;
    const int pc_ = 2 * g16 + (p4 >> 1);
    st16(__builtin_bit_cast(u32x4, trfrag(s + (pc_ ^ swr) * 16, 1024)), ws, l16, WS_W2 + (2 * xcd * 144 + ks2) * 1024);
    st16(__builtin_bit_cast(u32x4, trfrag(s + ((pc_ + 4) ^ swr) * 16, 1024)), ws, l16, WS_W2 + ((2 * xcd + 1) * 144 + ks2) * 1024);
  }
  if (tid < 288) *(__attribute__((address_space(3))) u32x4*)(lds + L_ONES + tid * 16) = (u32x4){0x3f803f80u, 0x3f803f80u, 0x3f803f80u, 0x3f803f80u};
  st16(glu8(acc0, gsc, sd), ws, l16, WS_H + (tile * 112 + 4 * p2 + 2 * cg) * 1024);
  st16(glu8(acc1, gsc, sd), ws, l16, WS_H + (tile * 112 + 4 * p2 + 2 * cg + 1) * 1024);
  STAMP(6);
  WAITBAR(0);
  PIN(qf4);
  if (tid == 0) { st8(tag, ws, WS_F2 + (rb * 16 + set) * 8, 0);
    if (b == 0) st8((u32x2){e0 + 1, e1}, ws, WS_EPOCH, 0); }
  STAMP(7);
  const int tb = xcd * 4 + (bi & 3), n64 = bi >> 2;
  const bf16x8 qf = __builtin_bit_cast(bf16x8, qf4);
  lptr kp = lds + lane * 16;
  const lptr vp0 = lds + (r32 < 16 ? 65536 + (hi * 16 + r32) * 16 : L_ONES + (r32 - 16) * 16);
  lptr vp = vp0;
  const int vstep = r32 < 16 ? 4096 : 0;
  int idx = 0xDDDDDDDD; PIN(idx);
  f32x16 negm, o = {}, sA, sB;
  auto hmax = [&](float m) { auto rr = __builtin_amdgcn_permlane32_swap(__float_as_uint(m), __float_as_uint(m), false, false);
    return __builtin_fmaxf(__uint_as_float(rr[0]), __uint_as_float(rr[1])); };
  auto max16 = [&](float m, const f32x16& s) {
#pragma unroll
    for (int i = 0; i < 16; i += 2) m = __builtin_fmaxf(__builtin_fmaxf(m, s[i]), s[i + 1]);
    return m; };
  bf16x8 k1 = LDS8(kp + 1024), va, vb;
  {
    const bf16x8 k0 = LDS8(kp);
    sA = MFMA(k0, qf, (f32x16){}); sB = MFMA(k1, qf, (f32x16){});
    const float m = -48.f - hmax(max16(max16(-INFINITY, sA), sB));
#pragma unroll
    for (int i = 0; i < 16; ++i) negm[i] = m;
    PIN(negm);
    sA = MFMA(k0, qf, negm);
  }
  va = LDS8(vp); vb = va;
  f32x8 pa, pb = {};
  f32x16 g00 = {}, g01 = {}, g10 = {}, g11 = {};
  bf16x8 fa[4], fb[4];
  const unsigned vf2 = WS_F2 + (lane < 16 ? (tb >> 1) * 16 + lane : (((n64 & 1) * 8 + (lane & 7)) * 16 + (n64 >> 1) * 4 + ((lane >> 3) & 3))) * 8;
  unsigned sow = WS_W2 + (2 * n64 * 144 + 14 * w) * 1024, soh = WS_H + (2 * tb * 112 + 14 * w) * 1024;
#define EX(P, S, i) P[(i) & 7] = __builtin_amdgcn_exp2f(S[i])
#define TILE(SC, SN, u, G2A, G2B) do { \
    EX(pa, SC, 0); EX(pa, SC, 1); SBAR(); \
    o = SMF(vb, pb, o, idx); SBAR(); \
    vb = LDS8(vp + (u) * 1024 + 512); SBAR(); \
    EX(pa, SC, 2); EX(pa, SC, 3); EX(pa, SC, 4); EX(pa, SC, 5); SBAR(); \
    SN = MFMA(k1, qf, negm); SBAR(); \
    k1 = LDS8(kp + ((u) + 2) * 1024); SBAR(); \
    EX(pa, SC, 6); EX(pa, SC, 7); EX(pb, SC, 8); EX(pb, SC, 9); SBAR(); \
    o = SMF(va, pa, o, idx); SBAR(); \
    va = LDS8(vp + ((u) + 1) * 1024); SBAR(); \
    EX(pb, SC, 10); EX(pb, SC, 11); EX(pb, SC, 12); EX(pb, SC, 13); SBAR(); \
    G2A; G2B; SBAR(); \
    EX(pb, SC, 14); EX(pb, SC, 15); SBAR(); } while (0)
#define NOP_ do {} while (0)
#define ADV() do { kp += 4096; vp += vstep; } while (0)
#define GROUP(C, N, LD) do { \
    TILE(sA, sB, 0, g00 = MFMA(C[0], C[2], g00), if (LD) N[0] = ld16c(ws, l16, sow)); \
    TILE(sB, sA, 1, g10 = MFMA(C[1], C[2], g10), if (LD) N[2] = ld16c(ws, l16, soh)); \
    TILE(sA, sB, 2, g11 = MFMA(C[1], C[3], g11), if (LD) N[1] = ld16c(ws, l16, sow + 144 * 1024)); \
    TILE(sB, sA, 3, g01 = MFMA(C[0], C[3], g01), if (LD) N[3] = ld16c(ws, l16, soh + 112 * 1024)); \
    ADV(); sow += 1024; soh += 1024; } while (0)
  TILE(sA, sB, 0, NOP_, NOP_); TILE(sB, sA, 1, ALD2C(pl, vf2, ws, 0), NOP_); TILE(sA, sB, 2, NOP_, NOP_); TILE(sB, sA, 3, NOP_, NOP_); ADV();
  asm volatile("s_waitcnt vmcnt(0)" : "+v"(pl) :: "memory");
  wait_flags(pl, vf2, ws, t0, t1);
  STAMP(8);
  TILE(sA, sB, 0, fa[0] = ld16c(ws, l16, sow), NOP_); TILE(sB, sA, 1, fa[2] = ld16c(ws, l16, soh), NOP_);
  TILE(sA, sB, 2, fa[1] = ld16c(ws, l16, sow + 144 * 1024), NOP_); TILE(sB, sA, 3, fa[3] = ld16c(ws, l16, soh + 112 * 1024), NOP_); ADV();
  sow += 1024; soh += 1024;
  for (int it = 0; it < 7; ++it) { GROUP(fa, fb, true); GROUP(fb, fa, true); }
  o = SMF(vb, pb, o, idx);
  STAMP(9);
  bf16x8 wo0[4], wo1[4];
#pragma unroll
  for (int i = 0; i < 4; ++i) { wo0[i] = ld16c(ws, l16, WS_W2 + (2 * n64 * 144 + 112 + 4 * w + i) * 1024); wo1[i] = ld16c(ws, l16, WS_W2 + ((2 * n64 + 1) * 144 + 112 + 4 * w + i) * 1024); }
  const unsigned vout = (tb * 64 + ((w >> 1) & 1) * 32 + r32) * 1024 + n64 * 128 + (w >> 2) * 64 + (w & 1) * 32 + hi * 16;
  const u32x4 xres = __builtin_amdgcn_raw_buffer_load_b128(rx, vout, 0, 0);
  {
    float am = 0.f;
#pragma unroll
    for (int i = 0; i < 8; ++i) am = __builtin_fmaxf(am, __builtin_fabsf(o[i]));
    float sm = 0.f;
#pragma unroll
    for (int i = 0; i < 8; ++i) sm += __builtin_fabsf(o[i]);
    const bool ok = o[8] < 0x1p110f && o[8] > 0x1p-60f && sm < INFINITY && am > 0x1p-90f;
    if (__builtin_expect(!__all(ok), 0)) {
      float m = -INFINITY;
      for (int t = 0; t < 64; ++t) m = max16(m, MFMA(LDS8(lds + lane * 16 + t * 1024), qf, (f32x16){}));
      m = -hmax(m);
#pragma unroll
      for (int i = 0; i < 16; ++i) negm[i] = m;
      o = (f32x16){};
      for (int t = 0; t < 64; ++t) {
        const f32x16 s = MFMA(LDS8(lds + lane * 16 + t * 1024), qf, negm);
        const lptr v = vp0 + t * (vstep >> 2);
#pragma unroll
        for (int i = 0; i < 8; ++i) { pa[i] = __builtin_amdgcn_exp2f(s[i]); pb[i] = __builtin_amdgcn_exp2f(s[8 + i]); }
        o = SMF(LDS8(v), pa, o, idx); o = SMF(LDS8(v + 512), pb, o, idx);
      }
    }
  }
  st16(pack8(o, 0, __builtin_amdgcn_rcpf(o[8])), ws, l16, WS_O + ((aq * 8 + w) * 32 + ah) * 1024);
  WAITV(0);
  if (lane == 0) st8(tag, ws, WS_F3 + ((aq * 8 + w) * 32 + ah) * 8, 0);
  STAMP(10);
  { const unsigned vf3 = WS_F3 + ((2 * tb + ((lane >> 2) & 1)) * 32 + 4 * w + (lane & 3)) * 8;
    wait_flags((u32x2){~t0, 0u}, vf3, ws, t0, t1); }
  STAMP(11);
  {
    bf16x8 of0[4], of1[4]; SBAR();
#pragma unroll
    for (int i = 0; i < 4; ++i) { of0[i] = ld16c(ws, l16, WS_O + (2 * tb * 32 + 4 * w + i) * 1024); of1[i] = ld16c(ws, l16, WS_O + ((2 * tb + 1) * 32 + 4 * w + i) * 1024); }
    SBAR();
#pragma unroll
    for (int i = 0; i < 4; ++i) { g00 = MFMA(wo0[i], of0[i], g00); g10 = MFMA(wo1[i], of0[i], g10); g01 = MFMA(wo0[i], of1[i], g01); g11 = MFMA(wo1[i], of1[i], g11); }
  }
  STAMP(12);
  LBAR();
  typedef __attribute__((address_space(3))) f32x4* l4p;
  { const lptr d = lds + w * 16384 + lane * 16;
#pragma unroll
    for (int i = 0; i < 4; ++i) {
      *(l4p)(d + i * 1024) = (f32x4){g00[4 * i], g00[4 * i + 1], g00[4 * i + 2], g00[4 * i + 3]};
      *(l4p)(d + (4 + i) * 1024) = (f32x4){g01[4 * i], g01[4 * i + 1], g01[4 * i + 2], g01[4 * i + 3]};
      *(l4p)(d + (8 + i) * 1024) = (f32x4){g10[4 * i], g10[4 * i + 1], g10[4 * i + 2], g10[4 * i + 3]};
      *(l4p)(d + (12 + i) * 1024) = (f32x4){g11[4 * i], g11[4 * i + 1], g11[4 * i + 2], g11[4 * i + 3]}; } }
  LBAR();
  STAMP(13);
  {
    const lptr s = lds + w * 2048 + lane * 16;
    f32x4 a = *(l4p)(s), c = *(l4p)(s + 1024);
#pragma unroll
    for (int i = 1; i < 8; ++i) { a += *(l4p)(s + i * 16384); c += *(l4p)(s + i * 16384 + 1024); }
    float f[8];
#pragma unroll
    for (int i = 0; i < 4; ++i) { auto rr = __builtin_amdgcn_permlane32_swap(__float_as_uint(a[i]), __float_as_uint(c[i]), false, false);
      f[i] = __uint_as_float(rr[0]); f[4 + i] = __uint_as_float(rr[1]); }
    u32x4 r;
#pragma unroll
    for (int i = 0; i < 4; ++i) r[i] = cvtpk(f[2 * i] + bflo(xres[i]), f[2 * i + 1] + bfhi(xres[i]));
    __builtin_amdgcn_raw_buffer_store_b128(r, mk(pout, 2048 * 1024), vout, 0, 0);
  }
  STAMP(14);
}

extern "C" void kernel_launch(void* const* d_in, const int* in_sizes, int n_in, void* d_out, int out_size, void* d_ws, size_t ws_size, hipStream_t stream) {
  (void)hipFuncSetAttribute((const void*)fused_block, hipFuncAttributeMaxDynamicSharedMemorySize, LDS_BYTES);
  hipLaunchKernelGGL(fused_block, dim3(256), dim3(512), LDS_BYTES, stream, d_in[0], d_in[1], d_in[2], d_in[4], d_in[5], d_in[6], d_out, d_ws);
}
```

```cpp
#include <hip/hip_runtime.h>
#include <hip/hip_bf16.h>

typedef __bf16 bf16x8 __attribute__((ext_vector_type(8)));
typedef __bf16 bf16x16 __attribute__((ext_vector_type(16)));
typedef __bf16 bf16x2 __attribute__((ext_vector_type(2)));
typedef short s16x4 __attribute__((ext_vector_type(4)));
typedef float f32x16 __attribute__((ext_vector_type(16)));
typedef float f32x8 __attribute__((ext_vector_type(8)));
typedef float f32x4 __attribute__((ext_vector_type(4)));
typedef float f32x2 __attribute__((ext_vector_type(2)));
typedef unsigned u32x4 __attribute__((ext_vector_type(4)));
typedef unsigned u32x2 __attribute__((ext_vector_type(2)));
typedef unsigned u32x16 __attribute__((ext_vector_type(16)));
typedef __amdgpu_buffer_rsrc_t rsrc_t;
typedef __attribute__((address_space(3))) char* lptr;
#define DI __device__ __forceinline__
#define SBAR() __builtin_amdgcn_sched_barrier(0)
#define MFMA(a, b, c) __builtin_amdgcn_mfma_f32_32x32x16_bf16(a, b, c, 0, 0, 0)
#define SMF(a, b, c, idx) __builtin_amdgcn_smfmac_f32_32x32x32_bf16(a, __builtin_bit_cast(bf16x16, b), c, idx, 0, 0)
#define LDS8(p) (*(const __attribute__((address_space(3))) bf16x8*)(p))
#define PIN(x) asm volatile("" : "+v"(x))

#define DBG_STAMPS 0

constexpr unsigned WS_EPOCH = 0;
constexpr unsigned FS = 128;
constexpr unsigned WS_F0 = 262144, WS_F1 = WS_F0 + 256 * FS, WS_F2 = WS_F1 + 256 * FS;
constexpr unsigned WS_STAT = 32768;
constexpr unsigned WS_CS = 65536;
constexpr unsigned WS_Q = 1u << 20;
constexpr unsigned WS_KV = WS_Q + (2u << 20);
constexpr unsigned WS_O = WS_KV + (4u << 20);
constexpr unsigned WS_W2 = WS_O + (2u << 20);
constexpr unsigned WS_H = WS_W2 + (3u << 20);
constexpr unsigned WS_DBG = WS_H + (7u << 20);
constexpr unsigned WS_END = WS_DBG + (1u << 20);
constexpr int U_XA = 0, U_G1 = 24576, U_XB = 49152, U_G2 = 73728, U_G0 = 98304, UD = 49152;
constexpr int L_XCS = 122880, L_V = 73728, L_PRIV = 139264, L_ONES = 155648, LDS_BYTES = 163840;
constexpr unsigned SPIN_CAP = 1u << 15, CODE_PF = 24576;
constexpr float REF_OFF = 90.f;

DI rsrc_t mk(const void* p, unsigned bytes) { return __builtin_amdgcn_make_buffer_rsrc((void*)p, (short)0, (int)bytes, 0x00020000); }
DI unsigned cvtpk(float lo, float hi) { f32x2 v = {lo, hi}; return __builtin_bit_cast(unsigned, __builtin_convertvector(v, bf16x2)); }
DI float bflo(unsigned u) { return __uint_as_float(u << 16); }
DI float bfhi(unsigned u) { return __uint_as_float(u & 0xffff0000u); }
DI float rbf(float f) { return bflo(cvtpk(f, f)); }
DI float max3(float a, float b, float c) { return __builtin_fmaxf(__builtin_fmaxf(a, b), c); }
DI s16x4 trd(lptr p) { return __builtin_amdgcn_ds_read_tr16_b64_v4i16((__attribute__((address_space(3))) s16x4*)p); }
DI bf16x8 trfrag(lptr p, int step) { s16x4 a = trd(p), b = trd(p + step); return __builtin_bit_cast(bf16x8, __builtin_shufflevector(a, b, 0, 1, 2, 3, 4, 5, 6, 7)); }
#define dma(base, imm, voff, r, soff) asm volatile("s_add_i32 m0, %0, %4\n\ts_nop 0\n\tbuffer_load_dwordx4 %1, %2, %3 offen lds" :: "s"(base), "v"(voff), "s"(r), "s"(soff), "i"(imm) : "memory", "scc")
#define dma_sc1(base, imm, voff, r, soff) asm volatile("s_add_i32 m0, %0, %4\n\ts_nop 0\n\tbuffer_load_dwordx4 %1, %2, %3 offen sc1 lds" :: "s"(base), "v"(voff), "s"(r), "s"(soff), "i"(imm) : "memory", "scc")
#define ALD4(dst, voff, r, soff) asm volatile("buffer_load_dwordx4 %0, %1, %2, %3 offen" : "=v"(dst) : "v"(voff), "s"(r), "s"(soff) : "memory")
#define ALD4C(dst, voff, r, soff) asm volatile("buffer_load_dwordx4 %0, %1, %2, %3 offen sc1" : "=v"(dst) : "v"(voff), "s"(r), "s"(soff) : "memory")
#define ALD2C(dst, voff, r, soff) asm volatile("buffer_load_dwordx2 %0, %1, %2, %3 offen sc1" : "=v"(dst) : "v"(voff), "s"(r), "s"(soff) : "memory")
#define ALD1C(dst, voff, r, soff) asm volatile("buffer_load_dword %0, %1, %2, %3 offen sc1" : "=v"(dst) : "v"(voff), "s"(r), "s"(soff) : "memory")
#define CLD(op, dst, voff, r, soff, lim) asm volatile("s_cmp_ge_u32 %4, %5\n\ts_cbranch_scc1 1f\n\t" op "\n1:" : "=v"(dst) : "v"(voff), "s"(r), "s"(soff), "s"(w), "i"(lim) : "memory", "scc")
#define POLL(voff) CLD("buffer_load_dwordx2 %0, %1, %2, %3 offen sc1", pl, voff, ws, 0, 1)
#define WAITV(n) asm volatile("s_waitcnt vmcnt(%0)" :: "i"(n) : "memory")
#define WAITBAR(n) asm volatile("s_waitcnt vmcnt(%0) lgkmcnt(0)\n\ts_barrier" :: "i"(n) : "memory")
#define LBAR() asm volatile("s_waitcnt lgkmcnt(0)\n\ts_barrier" ::: "memory")
DI void st16(u32x4 v, rsrc_t r, unsigned voff, unsigned soff) {
  asm volatile("buffer_store_dwordx4 %0, %1, %2, %3 offen sc1\n\ts_nop 1" :: "v"(v), "v"(voff), "s"(r), "s"(soff) : "memory"); }
DI void st8(u32x2 v, rsrc_t r, unsigned voff, unsigned soff) { __builtin_amdgcn_raw_buffer_store_b64(v, r, voff, soff, 16); }
DI void st4(unsigned v, rsrc_t r, unsigned voff, unsigned soff) { __builtin_amdgcn_raw_buffer_store_b32(v, r, voff, soff, 16); }
DI bf16x8 ld16c(rsrc_t r, unsigned voff, unsigned soff) { return __builtin_bit_cast(bf16x8, __builtin_amdgcn_raw_buffer_load_b128(r, voff, soff, 16)); }
template <int CTRL, int RM> DI float dppadd(float v) { return v + __int_as_float(__builtin_amdgcn_update_dpp(0, __float_as_int(v), CTRL, RM, 0xf, true)); }
DI float wave_sum(float v) {
  v = dppadd<0x111, 0xf>(v); v = dppadd<0x112, 0xf>(v); v = dppadd<0x114, 0xf>(v); v = dppadd<0x118, 0xf>(v);
  v = dppadd<0x142, 0xa>(v); v = dppadd<0x143, 0xc>(v);
  return __int_as_float(__builtin_amdgcn_readlane(__float_as_int(v), 63)); }
DI void wait_flags(u32x2 f, unsigned voff, rsrc_t ws, unsigned t0, unsigned t1) {
#pragma unroll 1
  for (unsigned spins = 0; spins < SPIN_CAP; ++spins) {
    if (__all(f[0] == t0 && f[1] == t1)) break;
    __builtin_amdgcn_s_sleep(2);
    asm volatile("buffer_load_dwordx2 %0, %1, %2, 0 offen sc1\n\ts_waitcnt vmcnt(0)" : "=&v"(f) : "v"(voff), "s"(ws) : "memory");
  }
}
#define STAMP(i)
#define STAMP_END()

DI u32x4 pack8(const f32x16& a, int o, float s) {
  return (u32x4){cvtpk(a[o] * s, a[o + 1] * s), cvtpk(a[o + 2] * s, a[o + 3] * s), cvtpk(a[o + 4] * s, a[o + 5] * s), cvtpk(a[o + 6] * s, a[o + 7] * s)}; }
DI u32x4 glu8(const f32x16& a, float nc, float sd) {
  float h[8];
#pragma unroll
  for (int i = 0; i < 8; ++i) { const float e = __builtin_amdgcn_exp2f(a[i] * nc); h[i] = a[8 + i] * __builtin_amdgcn_rcpf(__builtin_fmaf(e, sd, sd)); }
  return (u32x4){cvtpk(h[0], h[1]), cvtpk(h[2], h[3]), cvtpk(h[4], h[5]), cvtpk(h[6], h[7])}; }

__global__ __launch_bounds__(512, 2) void fused_block(const void* __restrict__ px, const void* __restrict__ pwqkv, const void* __restrict__ pwo,
                                                      const void* __restrict__ pwg, const void* __restrict__ pwu, const void* __restrict__ pwd,
                                                      void* __restrict__ pout, void* __restrict__ pws) {
  extern __shared__ __attribute__((aligned(16))) char smem[];
  asm volatile("" :: "s"(px), "s"(pwqkv), "s"(pwo), "s"(pwg), "s"(pwu), "s"(pwd), "s"(pout), "s"(pws));
  const lptr lds = (lptr)smem;
  const unsigned lds0 = (unsigned)(size_t)lds;
  const int tid = threadIdx.x, lane = tid & 63, w = __builtin_amdgcn_readfirstlane(tid >> 6);
  const int b = blockIdx.x, xcd = b & 7, bi = b >> 3;
  const int r32 = lane & 31, hi = lane >> 5;
  const rsrc_t ws = mk(pws, WS_END), rx = mk(px, 2048 * 1024);
  STAMP(0);
  const int rb = (xcd & 1) * 8 + (bi & 7), set = (xcd >> 1) * 4 + (bi >> 3), Q4 = set >> 2, sj = set & 3;
  const bool tb_ = sj == 0;
  const int p1 = 7 * Q4 + 2 * sj - 1, p2 = 7 * Q4 + 2 * sj;
  const int rot = 2 * ((rb >> 3) + 2 * ((rb >> 2) & 1)), pos = (rb >> 1) & 1, half = rb & 1, kpart = rot + pos;
  const int tt = w & 3, cg = w >> 2;
  const unsigned col0 = tb_ ? 128 * Q4 : (sj - 1) * 512 + 128 * Q4 + 64;
  const unsigned col1 = tb_ ? 512 + 128 * Q4 : 1536 + 64 * p1, col2 = tb_ ? 1024 + 128 * Q4 : 3328 + 64 * p1;
  const unsigned str12 = tb_ ? 3072 : 3584;
  const rsrc_t rg0 = mk((const char*)pwqkv + col0 * 2, 512 * 3072 - col0 * 2);
  const unsigned bo1 = tb_ ? col1 * 2 : p1 * 128, bo2 = tb_ ? col2 * 2 : p1 * 128;
  const rsrc_t rg1 = mk((const char*)(tb_ ? pwqkv : pwg) + bo1, 512 * str12 - bo1), rg2 = mk((const char*)(tb_ ? pwqkv : pwu) + bo2, 512 * str12 - bo2);
  const rsrc_t rg3 = mk((const char*)pwg + p2 * 128, 512 * 3584 - p2 * 128), rg4 = mk((const char*)pwu + p2 * 128, 512 * 3584 - p2 * 128);
  const rsrc_t rge = half ? rg4 : rg3;
  const unsigned l3 = lane >> 3, l7 = lane & 7, sw = ((lane >> 4) & 1) * 4;
  const unsigned vx0 = (rb * 128 + w * 16 + l3) * 1024 + ((l7 ^ (lane >> 4)) * 16), vx1 = (rb * 128 + w * 16 + 8 + l3) * 1024 + ((l7 ^ (4 + (lane >> 4))) * 16);
  const unsigned vg0 = (w * 8 + l3) * 3072 + (l7 ^ sw) * 16, vg1 = (w * 8 + l3) * str12 + (l7 ^ sw) * 16, vg2 = (w * 8 + l3) * str12 + (l7 ^ sw ^ 2) * 16;
  const unsigned vg3 = (w * 8 + l3) * 3584 + (l7 ^ sw) * 16, vg4 = (w * 8 + l3) * 3584 + (l7 ^ sw ^ 2) * 16;
  const unsigned mx = lds0 + (w >> 2) * UD + (w & 3) * 2048, mw = lds0 + w * 1024;
#define KC(c) (unsigned)(((c) + rot) & 7)
#define SL(c) (((c) % 3) * 8192)
#define DMA1(c, i) do { \
    if ((i) == 0) dma(mx, SL(c), vx0, rx, KC(c) * 128); if ((i) == 1) dma(mx, SL(c) + 1024, vx1, rx, KC(c) * 128); \
    if ((i) == 2) dma(mw, SL(c) + U_G0, vg0, rg0, KC(c) * (64 * 3072)); if ((i) == 3) dma(mw, SL(c) + U_G1, vg1, rg1, KC(c) * 64 * str12); \
    if ((i) == 4) dma(mw, SL(c) + U_G2, vg2, rg2, KC(c) * 64 * str12); } while (0)
#define DMA2(c, i) do { constexpr int sl_ = SL(c) + ((i) & 2 ? U_XA : U_G1); const unsigned kc_ = KC(2 * ((c) - 8) + ((i) >> 1)) * (64 * 3584); \
    if ((i) & 1) dma(mw, sl_ + UD, vg4, rg4, kc_); else dma(mw, sl_, vg3, rg3, kc_); } while (0)
  u32x2 ep; u32x4 xr0, xr1;
  const unsigned zero = 0, l16 = lane * 16;
  const unsigned srow = rb * 128 + set * 8 + 2 * w;
  STAMP(46);
  if (w == 7) {
    unsigned long long pc; asm volatile("s_getpc_b64 %0" : "=s"(pc));
    const rsrc_t rc = mk((const void*)(pc & ~127ull), CODE_PF);
    const unsigned v = lane < 8 ? (bi * 8 + lane) * 128 : 0x7fffff00u;
    asm volatile("s_mov_b32 m0, %0\n\ts_nop 0\n\tbuffer_load_dword %1, %2, 0 offen lds" :: "s"(lds0 + LDS_BYTES - 256), "v"(v), "s"(rc) : "memory"); }
  CLD("buffer_load_dwordx4 %0, %1, %2, %3 offen", xr0, l16, rx, srow * 1024, 4);
  CLD("buffer_load_dwordx4 %0, %1, %2, %3 offen offset:1024", xr1, l16, rx, srow * 1024, 4);
  CLD("buffer_load_dwordx2 %0, %1, %2, %3 offen sc1", ep, zero, ws, WS_EPOCH, 1);
  DMA1(0, 0); DMA1(0, 1); DMA1(0, 2); DMA1(0, 3); DMA1(0, 4);
  STAMP(47);
  asm volatile("s_barrier" ::: "memory");
  STAMP(48);
  DMA1(1, 0); DMA1(1, 1); DMA1(1, 2); DMA1(1, 3); DMA1(1, 4);
  dma(mw, L_XCS, vg3, rge, kpart * (64 * 3584));
  STAMP(49);
  const int q4 = (lane & 15) >> 2, p4 = lane & 3, g16 = (lane >> 4) & 1, swr = (q4 >> 1) * 4;
  const int wrow = (8 * hi + q4) * 128 + (p4 & 1) * 8;
  const int pq = 4 * cg + 2 * g16 + (p4 >> 1);
  const int gl0 = g16 * UD + (((4 * cg + (p4 >> 1)) ^ swr ^ (2 * g16)) * 16), gl1 = g16 * UD + (((4 * cg + 2 + (p4 >> 1)) ^ swr ^ (2 * g16)) * 16);
  lptr wp0 = lds + U_G0 + wrow + (pq ^ swr) * 16;
  lptr wp1 = lds + wrow + (tb_ ? (pq ^ swr) * 16 : gl0);
  lptr wp2 = lds + wrow + (tb_ ? UD + (pq ^ swr ^ 2) * 16 : gl1);
  lptr xp[4];
#pragma unroll
  for (int s = 0; s < 4; ++s) { xp[s] = lds + (tt >> 1) * UD + (tt & 1) * 4096 + r32 * 128 + (((2 * s + hi) ^ ((r32 >> 1) & 7)) * 16); PIN(xp[s]); }
  PIN(wp0); PIN(wp1); PIN(wp2);
  if (tid < 272) { const unsigned o_ = (tid & 11) == 0 ? 0x3f803f80u : 0u;
    *(__attribute__((address_space(3))) u32x4*)(lds + L_ONES + tid * 16) = (u32x4){o_, o_, o_, o_}; }
  asm volatile("s_waitcnt vmcnt(11)" : "+v"(ep), "+v"(xr0), "+v"(xr1) :: "memory");
  STAMP(50);
  const unsigned e0 = __builtin_amdgcn_readfirstlane(ep[0]), e1 = __builtin_amdgcn_readfirstlane(ep[1]);
  const unsigned t0 = e0 * 0x9E3779B1u + 0x7F4A7C15u, t1 = (e1 ^ 0x85EBCA6Bu) + e0;
  const u32x2 tag = {t0, t1};
  if (w < 4) {
    float v[8], u[8];
#pragma unroll
    for (int i = 0; i < 4; ++i) { v[2 * i] = bflo(xr0[i]); v[2 * i + 1] = bfhi(xr0[i]); u[2 * i] = bflo(xr1[i]); u[2 * i + 1] = bfhi(xr1[i]); }
    const float mv = wave_sum(((v[0] + v[1]) + (v[2] + v[3])) + ((v[4] + v[5]) + (v[6] + v[7]))) * (1.f / 512);
    const float mu = wave_sum(((u[0] + u[1]) + (u[2] + u[3])) + ((u[4] + u[5]) + (u[6] + u[7]))) * (1.f / 512);
    float qv = 0.f, qu = 0.f;
#pragma unroll
    for (int i = 0; i < 8; ++i) { const float d = v[i] - mv, e = u[i] - mu; qv = __builtin_fmaf(d, d, qv); qu = __builtin_fmaf(e, e, qu); }
    const float vv = wave_sum(qv) * (1.f / 512), vu = wave_sum(qu) * (1.f / 512);
    const float sv = __builtin_amdgcn_sqrtf(rbf(rbf(vv) + 1e-5f)), su = __builtin_amdgcn_sqrtf(rbf(rbf(vu) + 1e-5f));
    if (lane == 0) st8((u32x2){cvtpk(-mv, sv), cvtpk(-mu, su)}, ws, WS_STAT + srow * 4, 0);
  }
  const int ah = xcd * 4 + (bi & 3), aq = bi >> 2, kh = w >> 2;
  const int qown = aq * 8 + 2 * (w & 3) + kh;
  STAMP(1);
  WAITBAR(6);
  STAMP(2);
  auto colsum = [&](int j, int sl) {
    const int pc_ = (j < 3 ? 4 * half : 4 * (j - 3)) + 2 * g16 + (p4 >> 1);
    const lptr cp = lds + (j == 0 ? U_G0 + sl : j == 1 ? U_G1 + sl : j == 2 ? U_G2 + sl : L_XCS) + wrow + ((pc_ ^ swr ^ (j == 2 ? 2 : 0)) * 16);
    const __bf16 o1 = (__bf16)1.0f; const bf16x8 ones = {o1, o1, o1, o1, o1, o1, o1, o1};
    const bf16x8 f0 = trfrag(cp, 512), f1 = trfrag(cp + 2048, 512), f2 = trfrag(cp + 4096, 512), f3 = trfrag(cp + 6144, 512);
    const f32x16 cs = MFMA(ones, f3, MFMA(ones, f2, MFMA(ones, f1, MFMA(ones, f0, (f32x16){}))));
    const float ch = rbf(cs[0]);
    const unsigned ccol = (j == 0 ? col0 : j == 1 ? col1 : j == 2 ? col2 : (half ? 3328 : 1536) + 64 * p2) + (j < 3 ? 32 * half : 32 * (j - 3));
    if (lane < 32) st4(cvtpk(ch, cs[0] - ch), ws, l16, WS_CS + ((kpart >> 2) * 5120 + ccol) * 16 + (kpart & 3) * 4);
  };
  bf16x8 xf[32], wa[3], wb[3];
  f32x16 acc0 = {}, acc1 = {}, acc2 = {};
  u32x2 pl; unsigned stat; u32x4 cs0, cs1, cs2;
  const unsigned gcol = (r32 & 15) + 32 * cg;
  unsigned vf0 = WS_F0 + (lane < 16 ? rb * 16 + lane : (lane & 15) * 16 + set) * FS, vst = (rb * 128 + tt * 32 + r32) * 4;
  unsigned vc0, vc1, vc2;
  { const unsigned vb_ = hi * 5120 + 32 * cg + r32, vg_ = hi * 5120 + (r32 < 16 ? col1 : col2) + gcol;
    vc0 = (col0 + vb_) * 16; vc1 = (tb_ ? col1 + vb_ : vg_) * 16; vc2 = (tb_ ? col2 + vb_ : vg_ + 16) * 16; }
#define RD1(W, ks) do { xf[ks] = LDS8(xp[(ks) & 3] + SL((ks) >> 2)); W[0] = trfrag(wp0 + SL((ks) >> 2) + ((ks) & 3) * 2048, 512); } while (0)
#define RD1B(W, ks) W[1] = trfrag(wp1 + U_G1 + SL((ks) >> 2) + ((ks) & 3) * 2048, 512)
#define RD1C(W, ks) W[2] = trfrag(wp2 + U_G1 + SL((ks) >> 2) + ((ks) & 3) * 2048, 512)
#define RD2A(W, ks) W[0] = trfrag(wp1 + SL(8 + ((ks) >> 3)) + ((ks) & 4 ? U_XA : U_G1) + ((ks) & 3) * 2048, 512)
#define RD2B(W, ks) W[1] = trfrag(wp2 + SL(8 + ((ks) >> 3)) + ((ks) & 4 ? U_XA : U_G1) + ((ks) & 3) * 2048, 512)
  RD1(wa, 0); RD1B(wa, 0); RD1C(wa, 0);
  DMA1(2, 0); DMA1(2, 4);
#define STEP1(ks, WC, WN) do { constexpr int c_ = ((ks) + 1) >> 2, s_ = ((ks) + 1) & 3; \
    if (s_ == 0) { SBAR(); \
      if (c_ == 1 && w < 3 && pos == 0) colsum(w, SL(0)); \
      if (c_ == 2 && w < 4) { _Pragma("unroll 1") for (int j = pos ? w : w + 3; j < 5; j += (j == 3 && pos ? 1 : 5)) colsum(j, SL(1)); } \
      if (c_ == 7 && w == 0) { WAITV(4); PIN(pl); wait_flags(pl, vf0, ws, t0, t1); } \
      STAMP(16 + 2 * c_); WAITBAR(c_ < 7 ? 5 : c_ == 7 ? 4 : 8); STAMP(17 + 2 * c_); \
      if (c_ == 1) st16((u32x4){~0u, ~0u, ~0u, ~0u}, ws, l16, WS_O + (qown * 32 + ah) * 1024);     \
      if (c_ == 3 && tid == 0) st8(tag, ws, WS_F0 + (rb * 16 + set) * FS, 0); \
      if (c_ == 6) POLL(vf0); \
      if (c_ == 7) { ALD1C(stat, vst, ws, WS_STAT); ALD4C(cs0, vc0, ws, WS_CS); ALD4C(cs1, vc1, ws, WS_CS); ALD4C(cs2, vc2, ws, WS_CS); } \
      if (c_ == 8) { wp1 = lds + wrow + gl0; wp2 = lds + wrow + gl1; PIN(wp1); PIN(wp2); } } \
    SBAR(); \
    if ((ks) < 31) RD1(WN, ((ks) + 1) & 31); else RD2A(WN, 0); \
    if (c_ <= 5) DMA1(c_ + 2, s_); if (c_ == 6 || c_ == 7) DMA2(c_ + 2, s_); SBAR(); \
    acc0 = MFMA(WC[0], xf[ks], acc0); SBAR(); \
    if ((ks) < 31) RD1B(WN, ((ks) + 1) & 31); else RD2B(WN, 0); SBAR(); \
    acc1 = MFMA(WC[1], xf[ks], acc1); SBAR(); \
    if ((ks) < 31) RD1C(WN, ((ks) + 1) & 31); \
    if (c_ >= 1 && c_ <= 5 && s_ == 0) DMA1(c_ + 2, 4); SBAR(); \
    acc2 = MFMA(WC[2], xf[ks], acc2); SBAR(); } while (0)
#define STEP1x4(k) STEP1(k, wa, wb); STEP1(k + 1, wb, wa); STEP1(k + 2, wa, wb); STEP1(k + 3, wb, wa)
  STEP1x4(0); STEP1x4(4); STEP1x4(8); STEP1x4(12); STEP1x4(16); STEP1x4(20); STEP1x4(24); STEP1x4(28);
  asm volatile("s_waitcnt vmcnt(4)" : "+v"(stat), "+v"(cs0), "+v"(cs1), "+v"(cs2) :: "memory");
  STAMP(3);
  const unsigned nm2 = (stat & 0xffffu) * 0x10001u;
  const bf16x8 nmf = __builtin_bit_cast(bf16x8, (u32x4){nm2, nm2, nm2, nm2});
  const float sd = bfhi(stat), rstd = __builtin_amdgcn_rcpf(sd), gsc = -1.4426950408889634f * rstd;
  acc0 = MFMA(__builtin_bit_cast(bf16x8, cs0), nmf, acc0); acc1 = MFMA(__builtin_bit_cast(bf16x8, cs1), nmf, acc1); acc2 = MFMA(__builtin_bit_cast(bf16x8, cs2), nmf, acc2);
  const int tile = rb * 4 + tt;
  {
    const int hd = (tb_ ? 8 * Q4 : 8 * Q4 + 4) + 2 * cg;
    const lptr pv = lds + L_PRIV + w * 2048;
    auto put_qk = [&](const f32x16& a, float s, unsigned so, unsigned hs) { st16(pack8(a, 0, s), ws, l16, so); st16(pack8(a, 8, s), ws, l16, so + hs); };
    auto put_v = [&](const f32x16& a) {
      const u32x4 u = pack8(a, 0, rstd), v = pack8(a, 8, rstd);
      typedef __attribute__((address_space(3))) u32x2* l2p;
      const lptr d = pv + r32 * 64 + hi * 8;
      *(l2p)(d) = (u32x2){u[0], u[1]}; *(l2p)(d + 16) = (u32x2){u[2], u[3]}; *(l2p)(d + 32) = (u32x2){v[0], v[1]}; *(l2p)(d + 48) = (u32x2){v[2], v[3]};
      asm volatile("s_waitcnt lgkmcnt(0)" ::: "memory");
      const lptr s = pv + (8 * hi + q4) * 64 + g16 * 32 + p4 * 8;
      const unsigned vo = g16 * 131072 + (hi * 16 + (lane & 15)) * 16;
      const u32x4 f0 = __builtin_bit_cast(u32x4, trfrag(s, 256)), f1 = __builtin_bit_cast(u32x4, trfrag(s + 1024, 256));
      st16(f0, ws, vo, WS_KV + hd * 131072 + 65536 + tile * 1024); st16(f1, ws, vo, WS_KV + hd * 131072 + 65536 + tile * 1024 + 512); };
    const unsigned sok = WS_KV + hd * 131072 + tile * 1024;
    if (sj != 3) put_qk(acc0, sj == 2 ? rstd : rstd * (0.25f * 1.4426950408889634f), sj == 2 ? sok : WS_Q + (hd * 64 + tile) * 1024, sj == 2 ? 131072 : 65536);
    if (tb_) { put_qk(acc1, rstd, sok, 131072); acc0 = acc2; }
    if (tb_ || sj == 3) put_v(acc0);
  }
  STAMP(4);
  const int ks2 = bi + 32 * w;
  u32x4 qf0, qf1, hp, w2a, w2b; unsigned vf1;
  f32x16 acc3 = {}, acc4 = {};
  auto glu2 = [&](const f32x16& a, int i) {
    const float e0_ = __builtin_amdgcn_exp2f(a[i] * gsc), e1_ = __builtin_amdgcn_exp2f(a[i + 1] * gsc);
    return cvtpk(a[8 + i] * __builtin_amdgcn_rcpf(__builtin_fmaf(e0_, sd, sd)), a[9 + i] * __builtin_amdgcn_rcpf(__builtin_fmaf(e1_, sd, sd))); };
#define KVD(i) dma_sc1(mw, (i) * 8192 + ((i) >= 8 ? L_V - 65536 : 0), l16, ws, WS_KV + ah * 131072 + (i) * 8192 + w * 1024)
  const rsrc_t rw2 = mk(ks2 < 112 ? pwd : pwo, ks2 < 112 ? 1792 * 1024 : 512 * 1024);
  const unsigned sw2 = (ks2 < 112 ? ks2 : ks2 - 112) * 16384 + xcd * 128;
#define STEP2(ks, WC, WN) do { constexpr int c_ = 8 + (((ks) + 1) >> 3), s_ = ((ks) + 1) & 7; \
    if (s_ == 0 && c_ < 12) { SBAR(); \
      if (c_ == 9 && ks2 < 144) { const unsigned v_ = l3 * 1024 + (l7 ^ sw) * 16; dma(lds0 + w * 2048, L_PRIV, v_, rw2, sw2); dma(lds0 + w * 2048, L_PRIV + 1024, v_, rw2, sw2 + 8192); } \
      if (c_ == 11 && ks2 < 144) {     \
        const lptr s2_ = lds + L_PRIV + w * 2048 + (4 * hi + q4) * 128 + (p4 & 1) * 8; const int pc_ = 2 * g16 + (p4 >> 1); \
        w2a = __builtin_bit_cast(u32x4, trfrag(s2_ + (pc_ ^ swr) * 16, 1024)); w2b = __builtin_bit_cast(u32x4, trfrag(s2_ + ((pc_ + 4) ^ swr) * 16, 1024)); } \
      if (c_ == 11 && w == 0) { WAITV(0); PIN(pl); wait_flags(pl, vf1, ws, t0, t1); } \
      STAMP(16 + 2 * c_); if (c_ == 9) { if (ks2 < 144) WAITBAR(6); else WAITBAR(4); } else WAITBAR(c_ == 10 ? 6 : 0); STAMP(17 + 2 * c_); \
      if (c_ == 9 && tid == 0) st8(tag, ws, WS_F1 + (rb * 16 + set) * FS, 0); \
      if (c_ == 11) { PIN(cs0); PIN(cs1); } } \
    if (c_ == 10 && s_ == 2) POLL(vf1); \
    SBAR(); \
    if ((ks) < 31) RD2A(WN, ((ks) + 1) & 31); \
    if (c_ == 8 && s_ >= 1 && s_ <= 4) DMA2(10, s_ - 1); \
    if (c_ == 9 && s_ < 4) DMA2(11, s_); \
    if (c_ == 9 && (s_ == 4 || s_ == 5)) { if (s_ == 4) ALD4C(cs0, vc0, ws, WS_CS); else ALD4C(cs1, vc0, ws, WS_CS + 256); } \
    if (c_ == 11 && s_ < 4) KVD(4 * s_); if (c_ == 11 && s_ == 4) ALD4C(qf0, l16, ws, WS_Q + (ah * 64 + qown) * 1024); \
    if (c_ == 11 && s_ == 5) ALD4C(qf1, l16, ws, WS_Q + (ah * 64 + (qown ^ 1)) * 1024); SBAR(); \
    acc3 = MFMA(WC[0], xf[ks], acc3); SBAR(); \
    if ((ks) < 31) RD2B(WN, ((ks) + 1) & 31); SBAR(); \
    acc4 = MFMA(WC[1], xf[ks], acc4); SBAR(); \
    if ((ks) == 0) { vc0 = (hi * 5120 + (r32 < 16 ? 1536 : 3328) + 64 * p2 + gcol) * 16; const int gq = (ah >> 3) * 4, odd = (ah >> 2) & 1; \
      vf1 = WS_F1 + (lane < 16 ? lane * 16 + gq + 2 * odd : lane < 32 ? (lane - 16) * 16 + gq + 3 * odd : (2 * aq + (lane & 1)) * 16 + gq + odd) * FS; } \
    if ((ks) <= 16 && !((ks) & 1) && !tb_) { \
      if ((ks) == 8 || (ks) == 16) st16(hp, ws, l16, WS_H + (tile * 112 + 4 * p1 + 2 * cg + ((ks) >> 4)) * 1024); \
      if ((ks) < 16) { hp[((ks) >> 1) & 3] = glu2((ks) < 8 ? acc1 : acc2, (ks) & 6); PIN(hp); } } SBAR(); } while (0)
#define STEP2x4(k) STEP2(k, wa, wb); STEP2(k + 1, wb, wa); STEP2(k + 2, wa, wb); STEP2(k + 3, wb, wa)
  STEP2x4(0); STEP2x4(4); STEP2x4(8); STEP2x4(12); STEP2x4(16); STEP2x4(20); STEP2x4(24); STEP2x4(28);
  STAMP(5);
  acc3 = MFMA(__builtin_bit_cast(bf16x8, cs0), nmf, acc3); acc4 = MFMA(__builtin_bit_cast(bf16x8, cs1), nmf, acc4);
  st16(glu8(acc3, gsc, sd), ws, l16, WS_H + (tile * 112 + 4 * p2 + 2 * cg) * 1024);
  st16(glu8(acc4, gsc, sd), ws, l16, WS_H + (tile * 112 + 4 * p2 + 2 * cg + 1) * 1024);
  STAMP(6);
  WAITBAR(2);
  PIN(qf0); PIN(qf1);
  if (ks2 < 144) { st16(w2a, ws, l16, WS_W2 + (2 * xcd * 144 + ks2) * 1024); st16(w2b, ws, l16, WS_W2 + ((2 * xcd + 1) * 144 + ks2) * 1024); }
  STAMP(7);
  const int tb = xcd * 4 + (bi & 3), n64 = bi >> 2;
  const bf16x8 q0 = __builtin_bit_cast(bf16x8, qf0), q1 = __builtin_bit_cast(bf16x8, qf1);
  lptr kp = lds + kh * 32768 + lane * 16;
  const lptr vp0 = lds + (r32 < 16 ? L_V + (hi * 16 + r32) * 16 : L_ONES + (r32 - 16) * 16);
  const int vstep = r32 < 16 ? 4096 : 0;
  lptr vp = vp0 + kh * 8 * vstep;
  int idx = 0xDDDDDDDD; PIN(idx);
  f32x16 nm0, nm1, o0 = {}, o1 = {}, sA, sB;
  auto hmax = [&](float m) { auto rr = __builtin_amdgcn_permlane32_swap(__float_as_uint(m), __float_as_uint(m), false, false);
    return __builtin_fmaxf(__uint_as_float(rr[0]), __uint_as_float(rr[1])); };
  auto max16 = [&](float m, const f32x16& s) {
#pragma unroll
    for (int i = 0; i < 16; i += 2) m = __builtin_fmaxf(__builtin_fmaxf(m, s[i]), s[i + 1]);
    return m; };
  __builtin_amdgcn_s_setprio(1);
  bf16x8 k1 = LDS8(kp), va, vb;
  {
    const bf16x8 k0 = LDS8(lds + (kh ^ 1) * 32768 + lane * 16);
    const f32x16 z = {};
    f32x16 a0 = MFMA(k0, q0, z), a1 = MFMA(k0, q1, z), a2 = MFMA(k1, q0, z), a3 = MFMA(k1, q1, z);
    PIN(o0); PIN(o1); SBAR();
    float x0 = -INFINITY, x1 = -INFINITY, x2 = -INFINITY, x3 = -INFINITY;
#pragma unroll
    for (int i = 0; i < 16; i += 2) { x0 = max3(x0, a0[i], a0[i + 1]); x1 = max3(x1, a1[i], a1[i + 1]); }
#pragma unroll
    for (int i = 0; i < 16; i += 2) { x2 = max3(x2, a2[i], a2[i + 1]); x3 = max3(x3, a3[i], a3[i + 1]); }
    const float m0 = -REF_OFF - hmax(__builtin_fmaxf(x0, x2)), m1 = -REF_OFF - hmax(__builtin_fmaxf(x1, x3));
#pragma unroll
    for (int i = 0; i < 16; ++i) { nm0[i] = m0; nm1[i] = m1; }
    PIN(nm0); PIN(nm1);
    sA = MFMA(k1, q0, nm0);
  }
  va = LDS8(vp); vb = va;
  f32x8 pa, pb = {};
  f32x16 g00, g01, g10, g11;
  bf16x8 fa[4], fb[4];
  unsigned sow = WS_W2 + (2 * n64 * 144 + 14 * w) * 1024, soh = WS_H + (2 * tb * 112 + 14 * w) * 1024;
#define EX(P, S, i) P[(i) & 7] = __builtin_amdgcn_exp2f(S[i])
#define T2(u, A0, A1, B0, B1) do { \
    EX(pa, sA, 0); EX(pa, sA, 1); SBAR(); o1 = SMF(vb, pb, o1, idx); SBAR(); vb = LDS8(vp + (u) * 1024 + 512); SBAR(); \
    EX(pa, sA, 2); EX(pa, sA, 3); EX(pa, sA, 4); EX(pa, sA, 5); SBAR(); sB = MFMA(k1, q1, nm1); SBAR(); k1 = LDS8(kp + ((u) + 1) * 1024); SBAR(); \
    EX(pa, sA, 6); EX(pa, sA, 7); EX(pb, sA, 8); EX(pb, sA, 9); SBAR(); o0 = SMF(va, pa, o0, idx); SBAR(); \
    EX(pb, sA, 10); EX(pb, sA, 11); EX(pb, sA, 12); EX(pb, sA, 13); SBAR(); A0; A1; SBAR(); EX(pb, sA, 14); EX(pb, sA, 15); SBAR(); \
    EX(pa, sB, 0); EX(pa, sB, 1); SBAR(); o0 = SMF(vb, pb, o0, idx); SBAR(); \
    EX(pa, sB, 2); EX(pa, sB, 3); EX(pa, sB, 4); EX(pa, sB, 5); SBAR(); sA = MFMA(k1, q0, nm0); SBAR(); \
    EX(pa, sB, 6); EX(pa, sB, 7); EX(pb, sB, 8); EX(pb, sB, 9); SBAR(); o1 = SMF(va, pa, o1, idx); SBAR(); va = LDS8(vp + ((u) + 1) * 1024); SBAR(); \
    EX(pb, sB, 10); EX(pb, sB, 11); EX(pb, sB, 12); EX(pb, sB, 13); SBAR(); B0; B1; SBAR(); EX(pb, sB, 14); EX(pb, sB, 15); SBAR(); } while (0)
#define NOP_ do {} while (0)
#define ADV() do { kp += 4096; vp += vstep; } while (0)
#define G2P(u, C, N) do { \
    T2(u, g00 = MFMA(C[0], C[2], g00), (N[0] = ld16c(ws, l16, sow), N[2] = ld16c(ws, l16, soh)), g10 = MFMA(C[1], C[2], g10), N[1] = ld16c(ws, l16, sow + 144 * 1024)); \
    T2(u + 1, g11 = MFMA(C[1], C[3], g11), N[3] = ld16c(ws, l16, soh + 112 * 1024), g01 = MFMA(C[0], C[3], g01), NOP_); \
    sow += 1024; soh += 1024; } while (0)
#define PRIO_AT(k, n) asm volatile("s_cmp_lg_u32 %0, " #k "\n\ts_cbranch_scc1 1f\n\ts_setprio " #n "\n1:" :: "s"(it) : "scc")
#define PRIO(n) __builtin_amdgcn_s_setprio(n)
  T2(0, KVD(1), KVD(5), KVD(9), KVD(13)); PRIO(0); T2(1, KVD(2), KVD(6), KVD(10), KVD(14));
  STAMP(41);
  asm volatile("s_waitcnt vmcnt(8)\n\ts_barrier" ::: "memory");
  STAMP(42);
  if (tid == 0) { st8(tag, ws, WS_F2 + (rb * 16 + set) * FS, 0);
    if (b == 0) st8((u32x2){e0 + 1, e1}, ws, WS_EPOCH, 0); }
  const unsigned vf2 = WS_F2 + (lane < 16 ? (tb >> 1) * 16 + lane : (((n64 & 1) * 8 + (lane & 7)) * 16 + (n64 >> 1) * 4 + ((lane >> 3) & 3))) * FS;
  PRIO(2); T2(2, KVD(3), KVD(7), KVD(11), KVD(15)); T2(3, NOP_, NOP_, NOP_, NOP_); ADV();
  PRIO(1); T2(0, NOP_, NOP_, POLL(vf2), NOP_); PRIO(0); T2(1, NOP_, NOP_, NOP_, NOP_);
  STAMP(43);
  if (w == 0) { WAITV(0); PIN(pl); wait_flags(pl, vf2, ws, t0, t1); }
  STAMP(44);
  asm volatile("s_waitcnt vmcnt(0)\n\ts_barrier" ::: "memory");
  STAMP(8);
  const unsigned vout = (tb * 64 + ((w >> 1) & 1) * 32 + r32) * 1024 + n64 * 128 + (w >> 2) * 64 + (w & 1) * 32 + hi * 16;
  u32x4 xres;
  PRIO(3);
  fb[0] = ld16c(ws, l16, sow); fb[2] = ld16c(ws, l16, soh); fb[1] = ld16c(ws, l16, sow + 144 * 1024); fb[3] = ld16c(ws, l16, soh + 112 * 1024); SBAR();
  T2(2, fa[0] = ld16c(ws, l16, sow + 1024), fa[2] = ld16c(ws, l16, soh + 1024), fa[1] = ld16c(ws, l16, sow + 145 * 1024), fa[3] = ld16c(ws, l16, soh + 113 * 1024));
  { const f32x16 z = {};
    T2(3, g00 = MFMA(fb[0], fb[2], z), g10 = MFMA(fb[1], fb[2], z), g11 = MFMA(fb[1], fb[3], z), g01 = MFMA(fb[0], fb[3], z)); ADV(); }
  xres = __builtin_amdgcn_raw_buffer_load_b128(rx, vout, 0, 0);
  sow += 2048; soh += 2048;
#pragma unroll 1
  for (int it = 0; it < 6; ++it) { PRIO_AT(3, 2); PRIO_AT(5, 1); G2P(0, fa, fb); PRIO_AT(5, 0); G2P(2, fb, fa); ADV(); }
  o1 = SMF(vb, pb, o1, idx);
  STAMP(9);
  typedef __attribute__((address_space(3))) f32x4* l4p;
  { const lptr d = lds + L_PRIV + w * 2048 + lane * 16;
    *(l4p)(d) = (f32x4){o1[0], o1[1], o1[2], o1[3]}; *(l4p)(d + 1024) = (f32x4){o1[4], o1[5], o1[6], o1[7]}; }
  { *(__attribute__((address_space(3))) float*)(lds + 65536 + w * 256 + lane * 4) = o1[8]; }
  const unsigned swo = WS_W2 + (2 * n64 * 144 + 112 + 4 * w) * 1024;
#define WLOADS \
    "buffer_load_dwordx4 v[48:51], %[l], %[r], %[a] offen sc1\n\tbuffer_load_dwordx4 v[64:67], %[l], %[r], %[b] offen sc1\n\t" \
    "buffer_load_dwordx4 v[52:55], %[l], %[r], %[a] offen offset:1024 sc1\n\tbuffer_load_dwordx4 v[68:71], %[l], %[r], %[b] offen offset:1024 sc1\n\t" \
    "buffer_load_dwordx4 v[56:59], %[l], %[r], %[a] offen offset:2048 sc1\n\tbuffer_load_dwordx4 v[72:75], %[l], %[r], %[b] offen offset:2048 sc1\n\t" \
    "buffer_load_dwordx4 v[60:63], %[l], %[r], %[a] offen offset:3072 sc1\n\tbuffer_load_dwordx4 v[76:79], %[l], %[r], %[b] offen offset:3072 sc1\n\t"
#define WOPS [l] "v"(l16), [r] "s"(ws), [a] "s"(swo), [b] "s"(swo + 144 * 1024)
  g00 = MFMA(fa[0], fa[2], g00); g10 = MFMA(fa[1], fa[2], g10); g11 = MFMA(fa[1], fa[3], g11); g01 = MFMA(fa[0], fa[3], g01);
  LBAR();
  {
    const lptr s = lds + L_PRIV + (w ^ 4) * 2048 + lane * 16;
    const f32x4 a = *(l4p)(s), c = *(l4p)(s + 1024);
#pragma unroll
    for (int i = 0; i < 4; ++i) { o0[i] += a[i]; o0[4 + i] += c[i]; }
    o0[8] += *(__attribute__((address_space(3))) float*)(lds + 65536 + (w ^ 4) * 256 + lane * 4);
    float am = 0.f, sm = 0.f;
#pragma unroll
    for (int i = 0; i < 8; ++i) { am = __builtin_fmaxf(am, __builtin_fabsf(o0[i])); sm += __builtin_fabsf(o0[i]); }
    const bool ok = o0[8] < 0x1p120f && o0[8] > 0x1p-100f && sm < INFINITY && am > 0x1p-104f;
    if (__builtin_expect(!__all(ok), 0)) {
      float m = -INFINITY;
#pragma unroll 1
      for (int t = 0; t < 64; ++t) m = max16(m, MFMA(LDS8(lds + lane * 16 + t * 1024), q0, (f32x16){}));
      m = -hmax(m);
#pragma unroll
      for (int i = 0; i < 16; ++i) nm0[i] = m;
      o0 = (f32x16){};
#pragma unroll 1
      for (int t = 0; t < 64; ++t) {
        const f32x16 s_ = MFMA(LDS8(lds + lane * 16 + t * 1024), q0, nm0);
        const lptr v = vp0 + t * (vstep >> 2);
#pragma unroll
        for (int i = 0; i < 8; ++i) { pa[i] = __builtin_amdgcn_exp2f(s_[i]); pb[i] = __builtin_amdgcn_exp2f(s_[8 + i]); }
        o0 = SMF(LDS8(v), pa, o0, idx); o0 = SMF(LDS8(v + 512), pb, o0, idx);
      }
    }
  }
  { u32x4 ov = pack8(o0, 0, __builtin_amdgcn_rcpf(o0[8]));
#pragma unroll
    for (int i = 0; i < 4; ++i) ov[i] = ov[i] < 0xfffffffeu ? ov[i] : 0xfffffffeu;
    st16(ov, ws, l16, WS_O + (qown * 32 + ah) * 1024); }
  STAMP(45);
  {
    const unsigned soo = WS_O + (2 * tb * 32 + 4 * w) * 1024; unsigned spins;
#define OL0 "buffer_load_dwordx4 v[0:3], %[l], %[r], %[o0] offen sc1\n\t"
#define OL1 "buffer_load_dwordx4 v[4:7], %[l], %[r], %[o1] offen sc1\n\t"
#define OL2 "buffer_load_dwordx4 v[8:11], %[l], %[r], %[o0] offen offset:1024 sc1\n\t"
#define OL3 "buffer_load_dwordx4 v[12:15], %[l], %[r], %[o1] offen offset:1024 sc1\n\t"
#define OL4 "buffer_load_dwordx4 v[16:19], %[l], %[r], %[o0] offen offset:2048 sc1\n\t"
#define OL5 "buffer_load_dwordx4 v[20:23], %[l], %[r], %[o1] offen offset:2048 sc1\n\t"
#define OL6 "buffer_load_dwordx4 v[24:27], %[l], %[r], %[o0] offen offset:3072 sc1\n\t"
#define OL7 "buffer_load_dwordx4 v[28:31], %[l], %[r], %[o1] offen offset:3072 sc1\n\t"
#define OCHK(r0, r1, r2, r3) "v_max3_u32 v80, v" #r0 ", v" #r1 ", v" #r2 "\n\tv_max_u32 v80, v80, v" #r3 "\n\tv_cmp_eq_u32 vcc, -1, v80\n\t"
#define OSTEP(n, r0, r1, r2, r3, ga, gb, wa_, wb_) \
    "s_getpc_b64 %[ret]\n\ts_waitcnt vmcnt(" #n ")\n\t" OCHK(r0, r1, r2, r3) "s_cbranch_vccnz 3f\n\t" \
    "v_mfma_f32_32x32x16_bf16 " ga ", " wa_ ", v[" #r0 ":" #r3 "], " ga "\n\tv_mfma_f32_32x32x16_bf16 " gb ", " wb_ ", v[" #r0 ":" #r3 "], " gb "\n\t"
#define ORE(r0, r1, r2, r3, load) OCHK(r0, r1, r2, r3) "s_cbranch_vccz 4f\n\t" load "4:\n\t"
    unsigned long long ret;
    asm volatile("s_barrier\n\ts_mov_b32 %[sp], 0\n\t" WLOADS OL0 OL1 OL2 OL3 OL4 OL5 OL6 OL7
                 OSTEP(7, 0, 1, 2, 3, "%[g00]", "%[g10]", "v[48:51]", "v[64:67]") OSTEP(6, 4, 5, 6, 7, "%[g01]", "%[g11]", "v[48:51]", "v[64:67]")
                 OSTEP(5, 8, 9, 10, 11, "%[g00]", "%[g10]", "v[52:55]", "v[68:71]") OSTEP(4, 12, 13, 14, 15, "%[g01]", "%[g11]", "v[52:55]", "v[68:71]")
                 OSTEP(3, 16, 17, 18, 19, "%[g00]", "%[g10]", "v[56:59]", "v[72:75]") OSTEP(2, 20, 21, 22, 23, "%[g01]", "%[g11]", "v[56:59]", "v[72:75]")
                 OSTEP(1, 24, 25, 26, 27, "%[g00]", "%[g10]", "v[60:63]", "v[76:79]") OSTEP(0, 28, 29, 30, 31, "%[g01]", "%[g11]", "v[60:63]", "v[76:79]")
                 "s_branch 2f\n"
                 "3:\n\ts_add_u32 %[sp], %[sp], 1\n\ts_cmp_gt_u32 %[sp], %[cap]\n\ts_cbranch_scc1 2f\n\ts_waitcnt vmcnt(0)\n\t"
                 ORE(0, 1, 2, 3, OL0) ORE(4, 5, 6, 7, OL1) ORE(8, 9, 10, 11, OL2) ORE(12, 13, 14, 15, OL3)
                 ORE(16, 17, 18, 19, OL4) ORE(20, 21, 22, 23, OL5) ORE(24, 25, 26, 27, OL6) ORE(28, 29, 30, 31, OL7)
                 "s_waitcnt vmcnt(0)\n\ts_setpc_b64 %[ret]\n"
                 "2:\n\ts_waitcnt vmcnt(0)\n\ts_nop 15"
                 : [g00] "+v"(g00), [g01] "+v"(g01), [g10] "+v"(g10), [g11] "+v"(g11), [sp] "=&s"(spins), [ret] "=&s"(ret)
                 : WOPS, [o0] "s"(soo), [o1] "s"(soo + 32768), [cap] "i"(SPIN_CAP)
                 : "memory", "scc", "vcc", "v0", "v1", "v2", "v3", "v4", "v5", "v6", "v7", "v8", "v9", "v10", "v11", "v12", "v13", "v14", "v15", "v16", "v17", "v18", "v19",
                   "v20", "v21", "v22", "v23", "v24", "v25", "v26", "v27", "v28", "v29", "v30", "v31",
                   "v48", "v49", "v50", "v51", "v52", "v53", "v54", "v55", "v56", "v57", "v58", "v59",
                   "v60", "v61", "v62", "v63", "v64", "v65", "v66", "v67", "v68", "v69", "v70", "v71", "v72", "v73", "v74", "v75", "v76", "v77", "v78", "v79", "v80");
  }
  STAMP(12);
  { const lptr d = lds + w * 16384 + lane * 16;
#pragma unroll
    for (int i = 0; i < 4; ++i) {
      *(l4p)(d + i * 1024) = (f32x4){g00[4 * i], g00[4 * i + 1], g00[4 * i + 2], g00[4 * i + 3]};
      *(l4p)(d + (4 + i) * 1024) = (f32x4){g01[4 * i], g01[4 * i + 1], g01[4 * i + 2], g01[4 * i + 3]};
      *(l4p)(d + (8 + i) * 1024) = (f32x4){g10[4 * i], g10[4 * i + 1], g10[4 * i + 2], g10[4 * i + 3]};
      *(l4p)(d + (12 + i) * 1024) = (f32x4){g11[4 * i], g11[4 * i + 1], g11[4 * i + 2], g11[4 * i + 3]}; } }
  lptr rs0 = lds + (2 * w + hi) * 1024 + r32 * 16, rs1 = rs0 + 65536; PIN(rs0); PIN(rs1);
  f32x4 ra = {bflo(xres[0]), bfhi(xres[0]), bflo(xres[1]), bfhi(xres[1])}, rc = {bflo(xres[2]), bfhi(xres[2]), bflo(xres[3]), bfhi(xres[3])}; PIN(ra); PIN(rc);
  LBAR();
  STAMP(13);
  {
    f32x4 pa_[8], pc_[8];
#pragma unroll
    for (int i = 0; i < 8; ++i) { pa_[i] = *(l4p)((i < 4 ? rs0 : rs1) + (i & 3) * 16384); pc_[i] = *(l4p)((i < 4 ? rs0 : rs1) + (i & 3) * 16384 + 512); }
    SBAR();
#pragma unroll
    for (int i = 0; i < 8; ++i) { ra += pa_[i]; rc += pc_[i]; PIN(ra); PIN(rc); }
    const u32x4 r = {cvtpk(ra[0], ra[1]), cvtpk(ra[2], ra[3]), cvtpk(rc[0], rc[1]), cvtpk(rc[2], rc[3])};
    __builtin_amdgcn_raw_buffer_store_b128(r, mk(pout, 2048 * 1024), vout, 0, 0);
  }
  STAMP(14);
  STAMP_END();
}

extern "C" void kernel_launch(void* const* d_in, const int* in_sizes, int n_in, void* d_out, int out_size, void* d_ws, size_t ws_size, hipStream_t stream) {
  (void)hipFuncSetAttribute((const void*)fused_block, hipFuncAttributeMaxDynamicSharedMemorySize, LDS_BYTES);
  hipLaunchKernelGGL(fused_block, dim3(256), dim3(512), LDS_BYTES, stream, d_in[0], d_in[1], d_in[2], d_in[4], d_in[5], d_in[6], d_out, d_ws);
}
```

```cpp
#include <hip/hip_runtime.h>
#include <hip/hip_bf16.h>

typedef __bf16 bf16x8 __attribute__((ext_vector_type(8)));
typedef __bf16 bf16x16 __attribute__((ext_vector_type(16)));
typedef __bf16 bf16x2 __attribute__((ext_vector_type(2)));
typedef short s16x4 __attribute__((ext_vector_type(4)));
typedef float f32x16 __attribute__((ext_vector_type(16)));
typedef float f32x8 __attribute__((ext_vector_type(8)));
typedef float f32x4 __attribute__((ext_vector_type(4)));
typedef float f32x2 __attribute__((ext_vector_type(2)));
typedef unsigned u32x4 __attribute__((ext_vector_type(4)));
typedef unsigned u32x2 __attribute__((ext_vector_type(2)));
typedef unsigned u32x16 __attribute__((ext_vector_type(16)));
typedef __amdgpu_buffer_rsrc_t rsrc_t;
typedef __attribute__((address_space(3))) char* lptr;
#define DI __device__ __forceinline__
#define SBAR() __builtin_amdgcn_sched_barrier(0)
#define MFMA(a, b, c) __builtin_amdgcn_mfma_f32_32x32x16_bf16(a, b, c, 0, 0, 0)
#define SMF(a, b, c, idx) __builtin_amdgcn_smfmac_f32_32x32x32_bf16(a, __builtin_bit_cast(bf16x16, b), c, idx, 0, 0)
#define LDS8(p) (*(const __attribute__((address_space(3))) bf16x8*)(p))
#define PIN(x) asm volatile("" : "+v"(x))
#define LANE(n) unsigned n = l16; PIN(n); n >>= 4

#define FORCE_FB 0
#define DBG_STAMPS 0

constexpr unsigned WS_EPOCH = 0;
constexpr unsigned FS = 128;
constexpr unsigned WS_F0 = 262144, WS_F1 = WS_F0 + 256 * FS, WS_F2 = WS_F1 + 256 * FS, WS_F3 = WS_F2 + 256 * FS;
constexpr unsigned WS_STAT = 32768;
constexpr unsigned WS_CS = 65536;
constexpr unsigned WS_Q = 1u << 20;
constexpr unsigned WS_KV = WS_Q + (2u << 20);
constexpr unsigned WS_O = WS_KV + (4u << 20);
constexpr unsigned WS_W2 = WS_O + (2u << 20);
constexpr unsigned WS_H = WS_W2 + (3u << 20);
constexpr unsigned WS_DBG = WS_H + (7u << 20);
constexpr unsigned WS_END = WS_DBG + (1u << 20);
constexpr int U_XA = 0, U_G1 = 24576, U_XB = 49152, U_G2 = 73728, U_G0 = 98304, UD = 49152;
constexpr int L_XCS = 122880, L_V = 73728, L_PRIV = 139264, L_ONES = 155648, LDS_BYTES = 163840;
constexpr unsigned SPIN_CAP = 1u << 15, CODE_PF = 26112;
constexpr float REF_OFF = 90.f;

DI rsrc_t mk(const void* p, unsigned bytes) { return __builtin_amdgcn_make_buffer_rsrc((void*)p, (short)0, (int)bytes, 0x00020000); }
DI unsigned cvtpk(float lo, float hi) { f32x2 v = {lo, hi}; return __builtin_bit_cast(unsigned, __builtin_convertvector(v, bf16x2)); }
DI unsigned cvtpa(float lo, float hi) { unsigned r; asm("v_cvt_pk_bf16_f32 %0, %1, %2" : "=v"(r) : "v"(lo), "v"(hi)); return r; }
DI float bflo(unsigned u) { return __uint_as_float(u << 16); }
DI float bfhi(unsigned u) { return __uint_as_float(u & 0xffff0000u); }
DI float rbf(float f) { return bflo(cvtpk(f, f)); }
DI float max3(float a, float b, float c) { return __builtin_fmaxf(__builtin_fmaxf(a, b), c); }
DI s16x4 trd(lptr p) { return __builtin_amdgcn_ds_read_tr16_b64_v4i16((__attribute__((address_space(3))) s16x4*)p); }
DI bf16x8 trfrag(lptr p, int step) { s16x4 a = trd(p), b = trd(p + step); return __builtin_bit_cast(bf16x8, __builtin_shufflevector(a, b, 0, 1, 2, 3, 4, 5, 6, 7)); }
#define dma(base, imm, voff, r, soff) asm volatile("s_add_i32 m0, %0, %4\n\ts_nop 0\n\tbuffer_load_dwordx4 %1, %2, %3 offen lds" :: "s"(base), "v"(voff), "s"(r), "s"(soff), "i"(imm) : "memory", "scc")
#define dma_sc1(base, imm, voff, r, soff) asm volatile("s_add_i32 m0, %0, %4\n\ts_nop 0\n\tbuffer_load_dwordx4 %1, %2, %3 offen sc1 lds" :: "s"(base), "v"(voff), "s"(r), "s"(soff), "i"(imm) : "memory", "scc")
#define ALD4(dst, voff, r, soff) asm volatile("buffer_load_dwordx4 %0, %1, %2, %3 offen" : "=v"(dst) : "v"(voff), "s"(r), "s"(soff) : "memory")
#define ALD4C(dst, voff, r, soff) asm volatile("buffer_load_dwordx4 %0, %1, %2, %3 offen sc1" : "=v"(dst) : "v"(voff), "s"(r), "s"(soff) : "memory")
#define ALD2C(dst, voff, r, soff) asm volatile("buffer_load_dwordx2 %0, %1, %2, %3 offen sc1" : "=v"(dst) : "v"(voff), "s"(r), "s"(soff) : "memory")
#define ALD1C(dst, voff, r, soff) asm volatile("buffer_load_dword %0, %1, %2, %3 offen sc1" : "=v"(dst) : "v"(voff), "s"(r), "s"(soff) : "memory")
#define CLD(op, dst, voff, r, soff, lim) asm volatile("s_cmp_ge_u32 %4, %5\n\ts_cbranch_scc1 1f\n\t" op "\n1:" : "=v"(dst) : "v"(voff), "s"(r), "s"(soff), "s"(w), "i"(lim) : "memory", "scc")
#define POLL(voff) CLD("buffer_load_dwordx2 %0, %1, %2, %3 offen sc1", pl, voff, ws, 0, 1)
#define WAITV(n) asm volatile("s_waitcnt vmcnt(%0)" :: "i"(n) : "memory")
#define WAITBAR(n) asm volatile("s_waitcnt vmcnt(%0) lgkmcnt(0)\n\ts_barrier" :: "i"(n) : "memory")
#define LBAR() asm volatile("s_waitcnt lgkmcnt(0)\n\ts_barrier" ::: "memory")
DI void st16(u32x4 v, rsrc_t r, unsigned voff, unsigned soff) {
  asm volatile("buffer_store_dwordx4 %0, %1, %2, %3 offen sc1\n\ts_nop 1" :: "v"(v), "v"(voff), "s"(r), "s"(soff) : "memory"); }
DI void st8(u32x2 v, rsrc_t r, unsigned voff, unsigned soff) { __builtin_amdgcn_raw_buffer_store_b64(v, r, voff, soff, 16); }
DI void st4(unsigned v, rsrc_t r, unsigned voff, unsigned soff) { __builtin_amdgcn_raw_buffer_store_b32(v, r, voff, soff, 16); }
DI bf16x8 ld16c(rsrc_t r, unsigned voff, unsigned soff) { return __builtin_bit_cast(bf16x8, __builtin_amdgcn_raw_buffer_load_b128(r, voff, soff, 16)); }
template <int CTRL> DI float dppadd(float v) { return v + __int_as_float(__builtin_amdgcn_update_dpp(0, __float_as_int(v), CTRL, 0xf, 0xf, true)); }
DI void wait_flags(u32x2 f, unsigned voff, rsrc_t ws, unsigned t0, unsigned t1) {
#pragma unroll 1
  for (unsigned spins = 0; spins < SPIN_CAP; ++spins) {
    if (__all(f[0] == t0 && f[1] == t1)) break;
    __builtin_amdgcn_s_sleep(2);
    asm volatile("buffer_load_dwordx2 %0, %1, %2, 0 offen sc1\n\ts_waitcnt vmcnt(0)" : "=&v"(f) : "v"(voff), "s"(ws) : "memory");
  }
}
#define STAMP(i)
#define STAMP_END()

DI u32x4 pack8(const f32x16& a, int o, float s) {
  return (u32x4){cvtpa(a[o] * s, a[o + 1] * s), cvtpa(a[o + 2] * s, a[o + 3] * s), cvtpa(a[o + 4] * s, a[o + 5] * s), cvtpa(a[o + 6] * s, a[o + 7] * s)}; }
DI u32x4 glu8(const f32x16& a, float nc, float sd) {
  float h[8];
#pragma unroll
  for (int i = 0; i < 8; ++i) { const float e = __builtin_amdgcn_exp2f(a[i] * nc); h[i] = a[8 + i] * __builtin_amdgcn_rcpf(__builtin_fmaf(e, sd, sd)); }
  return (u32x4){cvtpa(h[0], h[1]), cvtpa(h[2], h[3]), cvtpa(h[4], h[5]), cvtpa(h[6], h[7])}; }

__global__ __launch_bounds__(512, 2) void fused_block(const void* __restrict__ px, const void* __restrict__ pwqkv, const void* __restrict__ pwo,
                                                      const void* __restrict__ pwg, const void* __restrict__ pwu, const void* __restrict__ pwd,
                                                      void* __restrict__ pout, void* __restrict__ pws) {
  extern __shared__ __attribute__((aligned(16))) char smem[];
  asm volatile("" :: "s"(px), "s"(pwqkv), "s"(pwo), "s"(pwg), "s"(pwu), "s"(pwd), "s"(pout), "s"(pws));
  const lptr lds = (lptr)smem;
  const unsigned lds0 = (unsigned)(size_t)lds;
  const int tid = threadIdx.x, lane = tid & 63, w = __builtin_amdgcn_readfirstlane(tid >> 6);
  const int b = blockIdx.x, xcd = b & 7, bi = b >> 3;
  const int r32 = lane & 31, hi = lane >> 5;
  const rsrc_t ws = mk(pws, WS_END), rx = mk(px, 2048 * 1024);
  STAMP(0);
  const int rb = (xcd & 1) * 8 + (bi & 7), Q4 = xcd >> 1, sj = bi >> 3, set = Q4 * 4 + sj;
  const int rot = 2 * (xcd & 1) + (bi & 4), pos = (bi >> 1) & 1, half = bi & 1, kpart = rot + pos;
  const unsigned l3 = lane >> 3, l7 = lane & 7, l16 = lane * 16;
  const int wx = (w + 2 * sj) & 7, ww = (w + 2 * bi) & 7;
  const unsigned vx0 = (rb * 128 + wx * 16 + l3) * 1024 + ((l7 ^ (lane >> 4)) * 16), vx1 = (vx0 + 8192) ^ 64;
  const unsigned mx = lds0 + (wx >> 2) * UD + (wx & 3) * 2048, mw = lds0 + ww * 1024;
#define KC(c) (unsigned)(((c) + rot) & 7)
#define SL(c) (((c) % 3) * 8192)
  dma(mx, 0, vx0, rx, rot * 128); dma(mx, 1024, vx1, rx, rot * 128); SBAR();
  const bool tb_ = sj == 0;
  const int p1 = 7 * Q4 + 2 * sj - 1, p2 = p1 + 1;
  const unsigned col0 = 128 * Q4 + (tb_ ? 1024 : sj * 512 - 448);
  const unsigned col1 = tb_ ? 512 + 128 * Q4 : 1536 + 64 * p1, col2 = tb_ ? 128 * Q4 : 3328 + 64 * p1;
  const unsigned str12 = tb_ ? 3072 : 3584, wr = ww * 8 + l3, wpc = (l7 ^ ((lane >> 2) & 4)) * 16;
  const rsrc_t rg0 = mk(pwqkv, 512 * 3072);
  const unsigned vg0 = wr * 3072 + wpc + col0 * 2;
  dma(mw, U_G0, vg0, rg0, rot * (64 * 3072)); SBAR();
  const rsrc_t rg1 = mk(tb_ ? pwqkv : pwg, 512 * str12), rg2 = mk(tb_ ? pwqkv : pwu, 512 * str12);
  const unsigned vg1 = wr * str12 + wpc + (tb_ ? col1 * 2 : p1 * 128), vg2 = (vg1 - (tb_ ? 1024 : 0)) ^ 32;
  dma(mw, U_G1, vg1, rg1, rot * 64 * str12); dma(mw, U_G2, vg2, rg2, rot * 64 * str12); SBAR();
  if (w == 0) {
    unsigned long long pc; asm volatile("s_getpc_b64 %0" : "=s"(pc));
    const rsrc_t rc = mk((const void*)(pc & ~127ull), CODE_PF);
    const unsigned v = lane < 8 ? (bi * 8 + lane) * 128 : 0x7fffff00u;
    asm volatile("s_mov_b32 m0, %0\n\ts_nop 0\n\tbuffer_load_dword %1, %2, 0 offen lds" :: "s"(lds0 + LDS_BYTES - 256), "v"(v), "s"(rc) : "memory"); }
  STAMP(47);
#define DMA1(c, i) do { \
    if ((i) == 0) dma(mx, SL(c), vx0, rx, KC(c) * 128); if ((i) == 1) dma(mx, SL(c) + 1024, vx1, rx, KC(c) * 128); \
    if ((i) == 2) dma(mw, SL(c) + U_G0, vg0, rg0, KC(c) * (64 * 3072)); if ((i) == 3) dma(mw, SL(c) + U_G1, vg1, rg1, KC(c) * 64 * str12); \
    if ((i) == 4) dma(mw, SL(c) + U_G2, vg2, rg2, KC(c) * 64 * str12); } while (0)
#define DMA2(c, i) do { constexpr int sl_ = SL(c) + ((i) & 2 ? U_XA : U_G1); const unsigned kc_ = KC(2 * ((c) - 8) + ((i) >> 1)) * (64 * 3584); \
    if ((i) & 1) dma(mw, sl_ + UD, vg4, rg4, kc_); else dma(mw, sl_, vg3, rg3, kc_); } while (0)
  const int tt = w & 3, cg = w >> 2;
  const int q4 = (lane & 15) >> 2, p4 = lane & 3, g16 = (lane >> 4) & 1, swr = (q4 >> 1) * 4;
  const int wrow = (8 * hi + q4) * 128 + (p4 & 1) * 8;
  const int pq = 4 * cg + 2 * g16 + (p4 >> 1);
  const int gl0 = g16 * UD + (pq ^ swr) * 16, gl1 = gl0 ^ 32;
  lptr wp0 = lds + U_G0 + wrow + (pq ^ swr) * 16;
  lptr wp1 = lds + wrow + (tb_ ? (pq ^ swr) * 16 : gl0);
  lptr wp2 = lds + wrow + (tb_ ? UD + (pq ^ swr ^ 2) * 16 : gl1);
  lptr xp[4];
#pragma unroll
  for (int s = 0; s < 4; ++s) { xp[s] = lds + (tt >> 1) * UD + (tt & 1) * 4096 + r32 * 128 + (((2 * s + hi) ^ ((r32 >> 1) & 7)) * 16); PIN(xp[s]); }
  PIN(wp0); PIN(wp1); PIN(wp2);
  if (tid < 272) { const unsigned o_ = (tid & 11) == 0 ? 0x3f803f80u : 0u;
    *(__attribute__((address_space(3))) u32x4*)(lds + L_ONES + tid * 16) = (u32x4){o_, o_, o_, o_}; }
  asm volatile("s_barrier" ::: "memory");
  STAMP(48);
  DMA1(1, 0); DMA1(1, 1); DMA1(1, 2); DMA1(1, 3); DMA1(1, 4);
  STAMP(49);
  const rsrc_t rg3 = mk(pwg, 512 * 3584), rg4 = mk(pwu, 512 * 3584);
  unsigned vg3 = wr * 3584 + wpc + p2 * 128, vg4 = vg3 ^ 32; PIN(vg3); PIN(vg4);
  const int ah = xcd * 4 + (bi & 3), aq = bi >> 2, kh = w >> 2;
  const int qown = aq * 8 + 2 * (w & 3) + kh;
  const unsigned gcol = (r32 & 15) + 32 * cg;
  unsigned vf0 = WS_F0 + (lane < 16 ? rb * 16 + lane : (lane & 15) * 16 + set) * FS, vst = (rb * 128 + tt * 32 + r32) * 4, vc0, vc1, vc2;
  { const unsigned vb_ = hi * 5120 + 32 * cg + r32, vg_ = hi * 5120 + (r32 < 16 ? col1 : col2) + gcol;
    vc0 = (col0 + vb_) * 16; vc1 = (tb_ ? col1 + vb_ : vg_) * 16; vc2 = (tb_ ? col2 + vb_ : vg_ + 16) * 16; }
  PIN(vf0); PIN(vst); PIN(vc0); PIN(vc1); PIN(vc2);
  lptr cp1 = lds + (w == 0 ? U_G0 : w == 1 ? U_G1 : U_G2) + pos * 8192 + wrow + (((4 * half + 2 * g16 + (p4 >> 1)) ^ swr ^ (w & 2)) * 16); PIN(cp1);
  const unsigned csb = WS_CS + (kpart >> 2) * 81920 + (kpart & 3) * 4;
  auto colsum = [&](lptr cp, unsigned ccol) {
    const __bf16 o1 = (__bf16)1.0f; const bf16x8 ones = {o1, o1, o1, o1, o1, o1, o1, o1};
    const bf16x8 f0 = trfrag(cp, 512), f1 = trfrag(cp + 2048, 512), f2 = trfrag(cp + 4096, 512), f3 = trfrag(cp + 6144, 512);
    const f32x16 cs = MFMA(ones, f3, MFMA(ones, f2, MFMA(ones, f1, MFMA(ones, f0, (f32x16){}))));
    const float ch = rbf(cs[0]);
    st4(cvtpk(ch, cs[0] - ch), ws, hi ? 0x7fffff00u : l16, csb + ccol * 16);
  };
  u32x2 ep, tag; u32x4 xr; unsigned e0, e1, t0, t1;
  const unsigned srow = rb * 128 + set * 8 + w;
  float sv[8], sa, sb, smean;
  auto swapadd16 = [](float v) { auto r = __builtin_amdgcn_permlane16_swap(__float_as_uint(v), __float_as_uint(v), false, false); return __uint_as_float(r[0]) + __uint_as_float(r[1]); };
  auto swapadd32 = [](float v) { auto r = __builtin_amdgcn_permlane32_swap(__float_as_uint(v), __float_as_uint(v), false, false); return __uint_as_float(r[0]) + __uint_as_float(r[1]); };
#define STATS(i) do { \
    if ((i) == 0 || (i) == 1) { sv[4 * (i)] = bflo(xr[2 * (i)]); sv[4 * (i) + 1] = bfhi(xr[2 * (i)]); sv[4 * (i) + 2] = bflo(xr[2 * (i) + 1]); sv[4 * (i) + 3] = bfhi(xr[2 * (i) + 1]); \
      PIN(sv[4 * (i)]); PIN(sv[4 * (i) + 1]); PIN(sv[4 * (i) + 2]); PIN(sv[4 * (i) + 3]); } \
    if ((i) == 2) { sa = (sv[0] + sv[1]) + sv[2]; PIN(sa); sb = (sv[3] + sv[4]) + sv[5]; PIN(sb); } \
    if ((i) == 3) { sa = ((sa + sv[6]) + sv[7]) + sb; PIN(sa); } \
    if ((i) == 4 || (i) == 14) { sa = dppadd<0x121>(sa); PIN(sa); } if ((i) == 5 || (i) == 15) { sa = dppadd<0x122>(sa); PIN(sa); } \
    if ((i) == 6 || (i) == 16) { sa = dppadd<0x124>(sa); PIN(sa); } if ((i) == 7 || (i) == 17) { sa = dppadd<0x128>(sa); PIN(sa); } \
    if ((i) == 8 || (i) == 18) { sa = swapadd16(sa); PIN(sa); } \
    if ((i) == 9) { smean = swapadd32(sa) * (1.f / 512); PIN(smean); sa = 0.f; } \
    if ((i) >= 10 && (i) <= 13) { const float d_ = sv[2 * (i) - 20] - smean, e_ = sv[2 * (i) - 19] - smean; sa = __builtin_fmaf(e_, e_, __builtin_fmaf(d_, d_, sa)); PIN(sa); } \
    if ((i) == 19) { sa = rbf(swapadd32(sa) * (1.f / 512)); PIN(sa); } \
    if ((i) == 20) { sa = __builtin_amdgcn_sqrtf(rbf(sa + 1e-5f)); PIN(sa); } \
    if ((i) == 21) st4(cvtpk(-smean, sa), ws, lane ? 0x7fffff00u : 0u, WS_STAT + srow * 4); } while (0)
  bf16x8 xf[32], wa[3], wb[3];
  f32x16 acc0 = {}, acc1 = {}, acc2 = {};
  u32x2 pl; unsigned stat; u32x4 cs0, cs1, cs2;
#define RD1(W, ks) do { xf[ks] = LDS8(xp[(ks) & 3] + SL((ks) >> 2)); W[0] = trfrag(wp0 + SL((ks) >> 2) + ((ks) & 3) * 2048, 512); } while (0)
#define RD1B(W, ks) W[1] = trfrag(wp1 + U_G1 + SL((ks) >> 2) + ((ks) & 3) * 2048, 512)
#define RD1C(W, ks) W[2] = trfrag(wp2 + U_G1 + SL((ks) >> 2) + ((ks) & 3) * 2048, 512)
#define RD2A(W, ks) W[0] = trfrag(wp1 + SL(8 + ((ks) >> 3)) + ((ks) & 4 ? U_XA : U_G1) + ((ks) & 3) * 2048, 512)
#define RD2B(W, ks) W[1] = trfrag(wp2 + SL(8 + ((ks) >> 3)) + ((ks) & 4 ? U_XA : U_G1) + ((ks) & 3) * 2048, 512)
#define FLAG(f) do { if (w == 0) st8(tag, ws, l16 ? 0x7fffff00u : 0u, (f) + (rb * 16 + set) * FS); } while (0)
  STAMP(1);
  WAITBAR(5);
  STAMP(2);
  RD1(wa, 0); RD1B(wa, 0); RD1C(wa, 0);
  { const unsigned z_ = 0; ALD4(xr, l16, rx, srow * 1024); CLD("buffer_load_dwordx2 %0, %1, %2, %3 offen sc1", ep, z_, ws, WS_EPOCH, 1); }
  DMA1(2, 0); DMA1(2, 4);
#define STEP1(ks, WC, WN) do { constexpr int c_ = ((ks) + 1) >> 2, s_ = ((ks) + 1) & 3; \
    if (s_ == 0) { SBAR(); \
      if ((c_ == 1 || c_ == 2) && w < 3 && pos == c_ - 1) colsum(cp1, (w == 0 ? col0 : w == 1 ? col1 : col2) + 32 * half); \
      if (c_ == 2) st16((u32x4){~0u, ~0u, ~0u, ~0u}, ws, l16, WS_O + (qown * 32 + ah) * 1024);     \
      if (c_ == 5 && (w == 1 || w == 2)) colsum(lds + L_XCS + wrow + (((4 * (w - 1) + 2 * g16 + (p4 >> 1)) ^ swr) * 16), (half ? 3328 : 1536) + 64 * p2 + 32 * (w - 1)); \
      if (c_ == 7 && w == 0) { WAITV(4); PIN(pl); wait_flags(pl, vf0, ws, t0, t1); } \
      STAMP(16 + 2 * c_); WAITBAR(c_ == 2 ? 6 : c_ < 7 ? 5 : c_ == 7 ? 4 : 8); STAMP(17 + 2 * c_); \
      if (c_ == 1) { PIN(ep); PIN(xr); e0 = __builtin_amdgcn_readfirstlane(ep[0]); e1 = __builtin_amdgcn_readfirstlane(ep[1]);     \
        t0 = e0 * 0x9E3779B1u + 0x7F4A7C15u; t1 = (e1 ^ 0x85EBCA6Bu) + e0; tag = (u32x2){t0, t1}; }     \
      if (c_ == 3) dma(mw, L_XCS, vg3, (half ? rg4 : rg3), kpart * (64 * 3584)); \
      if (c_ == 4) FLAG(WS_F0); \
      if (c_ == 6) { FLAG(WS_F3); POLL(vf0); } \
      if (c_ == 7) { ALD1C(stat, vst, ws, WS_STAT); ALD4C(cs0, vc0, ws, WS_CS); ALD4C(cs1, vc1, ws, WS_CS); ALD4C(cs2, vc2, ws, WS_CS); } \
      if (c_ == 8) { wp1 = lds + wrow + gl0; wp2 = lds + wrow + gl1; PIN(wp1); PIN(wp2); vf0 = WS_F3 + ((lane & 15) * 16 + set) * FS; POLL(vf0); } } \
    SBAR(); \
    if ((ks) < 31) RD1(WN, ((ks) + 1) & 31); else RD2A(WN, 0); \
    if (s_ == 3) { RD1B(WN, ((ks) + 1) & 31); RD1C(WN, ((ks) + 1) & 31); } \
    if (s_ != 3) { if (c_ <= 5) DMA1(c_ + 2, s_); else DMA2(c_ + 2, s_); } SBAR(); \
    acc0 = MFMA(WC[0], xf[ks], acc0); SBAR(); STATS(3 * ((ks) - 3)); \
    if (s_ != 3) { if ((ks) < 31) RD1B(WN, ((ks) + 1) & 31); else RD2B(WN, 0); } \
    if (s_ == 3) { if (c_ <= 5) DMA1(c_ + 2, s_); else DMA2(c_ + 2, s_); } if (c_ == 8) DMA2(10, 1); SBAR(); \
    acc1 = MFMA(WC[1], xf[ks], acc1); SBAR(); STATS(3 * ((ks) - 3) + 1); \
    if ((ks) < 31 && s_ != 3) RD1C(WN, ((ks) + 1) & 31); \
    if (c_ >= 1 && c_ <= 5 && s_ == 0) DMA1(c_ + 2, 4); if (c_ == 8) DMA2(10, 2); SBAR(); \
    acc2 = MFMA(WC[2], xf[ks], acc2); SBAR(); STATS(3 * ((ks) - 3) + 2); SBAR(); } while (0)
#define STEP1x4(k) STEP1(k, wa, wb); STEP1(k + 1, wb, wa); STEP1(k + 2, wa, wb); STEP1(k + 3, wb, wa)
  STEP1x4(0); STEP1x4(4); STEP1x4(8); STEP1x4(12); STEP1x4(16); STEP1x4(20); STEP1x4(24); STEP1x4(28);
  DMA2(10, 3);
  asm volatile("s_waitcnt vmcnt(8)" : "+v"(stat), "+v"(cs0), "+v"(cs1), "+v"(cs2) :: "memory");
  STAMP(3);
  unsigned nm2 = (stat & 0xffffu) * 0x10001u;
  bf16x8 nmf = __builtin_bit_cast(bf16x8, (u32x4){nm2, nm2, nm2, nm2});
  acc0 = MFMA(__builtin_bit_cast(bf16x8, cs0), nmf, acc0); acc1 = MFMA(__builtin_bit_cast(bf16x8, cs1), nmf, acc1); acc2 = MFMA(__builtin_bit_cast(bf16x8, cs2), nmf, acc2);
  const float sd = bfhi(stat), rstd = __builtin_amdgcn_rcpf(sd), gsc = -1.4426950408889634f * rstd, qsc = rstd * (0.25f * 1.4426950408889634f), sc0 = sj == 1 ? qsc : rstd;
  const int tile = rb * 4 + tt, hd = 8 * Q4 + (tb_ ? 0 : 4) + 2 * cg;
  const unsigned sok = WS_KV + hd * 131072 + tile * 1024, soq = WS_Q + (hd * 64 + tile) * 1024, isv = (sj + 1) & 2;
  unsigned l16k = (l16 & 0x330) | (l16 & 64) * 2 | (l16 & 128) / 2; if (sj == 1) l16k = l16; PIN(l16k);
  SBAR();
  STAMP(4);
  const int ks2 = bi + 32 * w;
  u32x4 qf0, qf1, hp, hq;
  f32x16 acc3 = {}, acc4 = {};
  auto glu2 = [&](const f32x16& a, int i) {
    const float e0_ = __builtin_amdgcn_exp2f(a[i] * gsc), e1_ = __builtin_amdgcn_exp2f(a[i + 1] * gsc);
    return cvtpa(a[8 + i] * __builtin_amdgcn_rcpf(__builtin_fmaf(e0_, sd, sd)), a[9 + i] * __builtin_amdgcn_rcpf(__builtin_fmaf(e1_, sd, sd))); };
  const unsigned mk_ = lds0 + ((w + aq) & 7) * 1024, skv = WS_KV + ah * 131072 + ((w + aq) & 7) * 1024;
#define KVD(i) dma_sc1(mk_, (i) * 8192 + ((i) >= 8 ? L_V - 65536 : 0), l16, ws, skv + (i) * 8192)
#define STEP2(ks, WC, WN) do { constexpr int c_ = 8 + (((ks) + 1) >> 3), s_ = ((ks) + 1) & 7; \
    if (s_ == 0 && c_ < 12) { SBAR(); \
      if (c_ == 9 && w == 0) { WAITV(6); PIN(pl); wait_flags(pl, vf0, ws, t0, t1); } \
      STAMP(16 + 2 * c_); if (c_ == 9) WAITBAR(6); else if (c_ == 11) LBAR(); else if (w == 0) WAITBAR(3); else WAITBAR(2); STAMP(17 + 2 * c_); } \
    if ((ks) == 12) { asm volatile("s_waitcnt vmcnt(4)\n\ts_barrier" ::: "memory"); FLAG(WS_F1); STAMP(51); } \
    if ((ks) == 21) POLL(vf0); \
    if ((ks) == 27) { \
      if (w == 0) { WAITV(0); PIN(pl); wait_flags(pl, vf0, ws, t0, t1); } \
      STAMP(52); asm volatile("s_barrier" ::: "memory"); STAMP(53); } \
    SBAR(); \
    if ((ks) < 31) RD2A(WN, ((ks) + 1) & 31); \
    if (s_ == 7) RD2B(WN, ((ks) + 1) & 31); \
    if (c_ == 9 && s_ < 4) DMA2(11, s_); \
    if ((ks) == 13) ALD4C(cs0, vc0, ws, WS_CS); if ((ks) == 14) ALD4C(cs1, vc0, ws, WS_CS + 256); \
    if ((ks) == 27) KVD(0); if ((ks) == 28) KVD(8); if ((ks) == 29) ALD4C(qf0, l16, ws, WS_Q + (ah * 64 + qown) * 1024); SBAR(); \
    acc3 = MFMA(WC[0], xf[ks], acc3); SBAR(); \
    if ((ks) < 31 && s_ != 7) RD2B(WN, ((ks) + 1) & 31); \
    if ((ks) == 27) KVD(4); if ((ks) == 28) KVD(12); if ((ks) == 29) ALD4C(qf1, l16, ws, WS_Q + (ah * 64 + (qown ^ 1)) * 1024); SBAR(); \
    acc4 = MFMA(WC[1], xf[ks], acc4); SBAR(); \
    if ((ks) < 2) { const u32x4 p_ = pack8(acc0, ((ks) & 1) * 8, sc0); if ((ks) == 0) hp = p_; else hq = p_; \
      if (isv) st16(p_, ws, l16k, sj == 1 ? soq + ((ks) & 1) * 65536 : sok + ((ks) & 1) * 131072); } \
    if ((ks) == 2 && !isv) { LANE(n_); const unsigned one_ = (n_ >> 5) == ((n_ >> 2) & 1) ? 0x3f80u << ((n_ & 1) * 16) : 0u, dw_ = ((n_ >> 1) & 1) + ((n_ >> 2) & 6); \
      u32x4 i0_, i1_; _Pragma("unroll") for (int i_ = 0; i_ < 4; ++i_) { i0_[i_] = dw_ == i_ ? one_ : 0u; i1_[i_] = dw_ == 4 + i_ ? one_ : 0u; } \
      acc0 = MFMA(__builtin_bit_cast(bf16x8, hq), __builtin_bit_cast(bf16x8, i1_), MFMA(__builtin_bit_cast(bf16x8, hp), __builtin_bit_cast(bf16x8, i0_), (f32x16){})); } \
    if ((ks) == 3 && !isv) { LANE(n_); const unsigned vvo = (n_ & 16) * 8192 + ((n_ >> 5) * 16 + (n_ & 15)) * 16; \
      st16((u32x4){cvtpk(acc0[0], acc0[1]), cvtpk(acc0[2], acc0[3]), cvtpk(acc0[4], acc0[5]), cvtpk(acc0[6], acc0[7])}, ws, vvo, sok + 65536); \
      st16((u32x4){cvtpk(acc0[8], acc0[9]), cvtpk(acc0[10], acc0[11]), cvtpk(acc0[12], acc0[13]), cvtpk(acc0[14], acc0[15])}, ws, vvo, sok + 65536 + 512); } \
    if ((ks) >= 4 && (ks) <= 6 && tb_) st16(pack8((ks) < 6 ? acc1 : acc2, ((ks) & 1) * 8, (ks) < 6 ? rstd : qsc), ws, (ks) < 6 ? l16k : l16, (ks) < 6 ? sok + ((ks) & 1) * 131072 : soq); \
    if ((ks) == 6 && tb_) st16(pack8(acc2, 8, qsc), ws, l16, soq + 65536); \
    if ((ks) == 7) { LANE(n_); vc0 = ((n_ >> 5) * 5120 + (n_ & 16) * 112 + 1536 + 64 * p2 + 32 * cg + (n_ & 15)) * 16; PIN(vc0); } \
    if ((ks) == 9) { LANE(n_); const int odd = xcd & 1, j_ = n_ >> 4; \
      vf0 = WS_F1 + ((j_ < 2 ? n_ & 15 : 2 * aq + (n_ & 1)) * 16 + Q4 * 4 + odd * (j_ < 2 ? 2 + j_ : 1)) * FS; PIN(vf0); } \
    if ((ks) == 15 && ks2 < 144) { const rsrc_t r_ = mk(ks2 < 112 ? pwd : pwo, ks2 < 112 ? 1792 * 1024 : 512 * 1024); const unsigned so_ = (ks2 < 112 ? ks2 : ks2 - 112) * 16384 + xcd * 128; LANE(n_); const unsigned v_ = (n_ >> 3) * 1024 + ((n_ & 7) ^ ((n_ >> 2) & 4)) * 16; \
      dma(lds0 + w * 2048, L_PRIV, v_, r_, so_); dma(lds0 + w * 2048, L_PRIV + 1024, v_, r_, so_ + 8192); } \
    if ((ks) >= 4 && (ks) <= 20 && !((ks) & 1) && !tb_) { \
      if ((ks) == 16) st16(hp, ws, l16, WS_H + (tile * 112 + 4 * p1 + 2 * cg) * 1024); \
      if ((ks) == 20) st16(hq, ws, l16, WS_H + (tile * 112 + 4 * p1 + 2 * cg + 1) * 1024); \
      else if ((ks) < 12) { hp[(((ks) >> 1) + 2) & 3] = glu2(acc1, ((ks) + 4) & 6); PIN(hp); } \
      else { hq[(((ks) >> 1) + 2) & 3] = glu2(acc2, ((ks) + 4) & 6); PIN(hq); } } SBAR(); } while (0)
#define STEP2x4(k) STEP2(k, wa, wb); STEP2(k + 1, wb, wa); STEP2(k + 2, wa, wb); STEP2(k + 3, wb, wa)
  STEP2x4(0); STEP2x4(4); STEP2x4(8); STEP2x4(12); STEP2x4(16); STEP2x4(20); STEP2x4(24); STEP2x4(28);
  asm volatile("s_waitcnt vmcnt(6)" : "+v"(cs0), "+v"(cs1) :: "memory");
  STAMP(5);
  PIN(nm2); nmf = __builtin_bit_cast(bf16x8, (u32x4){nm2, nm2, nm2, nm2});
  acc3 = MFMA(__builtin_bit_cast(bf16x8, cs0), nmf, acc3); acc4 = MFMA(__builtin_bit_cast(bf16x8, cs1), nmf, acc4);
  {
    LANE(n_); const lptr s2_ = lds + L_PRIV + w * 2048 + (4 * (n_ >> 5) + ((n_ >> 2) & 3)) * 128 + (n_ & 1) * 8; const int pc_ = (((n_ >> 3) & 2) + ((n_ >> 1) & 1)) ^ ((n_ >> 1) & 4);
    hp = __builtin_bit_cast(u32x4, trfrag(s2_ + pc_ * 16, 1024)); hq = __builtin_bit_cast(u32x4, trfrag(s2_ + (pc_ ^ 4) * 16, 1024)); }
  st16(glu8(acc3, gsc, sd), ws, l16, WS_H + (tile * 112 + 4 * p2 + 2 * cg) * 1024);
  LANE(ln); const int r3 = ln & 31, hf = ln >> 5;
  const int tb = xcd * 4 + (bi & 3), n64 = bi >> 2;
  lptr kp = lds + kh * 32768 + l16, kq = lds + (kh ^ 1) * 32768 + l16;
  const bool vrow = r3 < 16;
  int vstep = vrow ? 4096 : 0;
  lptr vp = lds + (vrow ? L_V + kh * 32768 + hf * 256 : L_ONES - 256) + r3 * 16;
  int idx = 0xDDDDDDDD;
  float aone = hf ? 0.f : 1.f;
  PIN(kp); PIN(kq); PIN(vstep); PIN(vp); PIN(idx); PIN(aone);
  f32x16 nm0, nm1, o0, o1, sA, sB;
  const f32x16 z = {};
  { u32x4 zz = {}; PIN(zz); o0 = MFMA(__builtin_bit_cast(bf16x8, zz), __builtin_bit_cast(bf16x8, zz), z); PIN(o0); o1 = MFMA(__builtin_bit_cast(bf16x8, zz), __builtin_bit_cast(bf16x8, zz), o0); PIN(o1); }
  STAMP(6);
  WAITBAR(1);
  PIN(qf0); PIN(qf1);
  STAMP(7);
  const bf16x8 q0 = __builtin_bit_cast(bf16x8, qf0), q1 = __builtin_bit_cast(bf16x8, qf1);
  auto hmax = [&](float m) { auto rr = __builtin_amdgcn_permlane32_swap(__float_as_uint(m), __float_as_uint(m), false, false);
    return __builtin_fmaxf(__uint_as_float(rr[0]), __uint_as_float(rr[1])); };
  auto max16 = [&](float m, const f32x16& s) {
#pragma unroll
    for (int i = 0; i < 16; i += 2) m = __builtin_fmaxf(__builtin_fmaxf(m, s[i]), s[i + 1]);
    return m; };
  __builtin_amdgcn_s_setprio(1);
  bf16x8 k1 = LDS8(kp), va, vb;
  {
    const bf16x8 k0 = LDS8(kq);
    va = LDS8(vp); SBAR();
    if (ks2 < 144) { st16(hp, ws, l16, WS_W2 + (2 * xcd * 144 + ks2) * 1024); st16(hq, ws, l16, WS_W2 + ((2 * xcd + 1) * 144 + ks2) * 1024); }
    hp[0] = glu2(acc4, 0); PIN(hp); SBAR();
    const f32x16 a2 = MFMA(k1, q0, z), a0 = MFMA(k0, q0, z); SBAR();
    hp[1] = glu2(acc4, 2); PIN(hp); SBAR();
    const f32x16 a3 = MFMA(k1, q1, z), a1 = MFMA(k0, q1, z); SBAR();
    float x0 = a0[0], x2 = a2[0];
#pragma unroll
    for (int i = 1; i < 15; i += 2) { x2 = max3(x2, a2[i], a2[i + 1]); x0 = max3(x0, a0[i], a0[i + 1]); }
    nm0 = __builtin_amdgcn_mfma_f32_32x32x2f32(aone, -REF_OFF - hmax(max3(max3(x0, x2, a0[15]), a2[15], a2[15])), z, 0, 0, 0); SBAR();
    float x1 = a1[0], x3 = a3[0];
#pragma unroll
    for (int i = 1; i < 15; i += 2) { x3 = max3(x3, a3[i], a3[i + 1]); x1 = max3(x1, a1[i], a1[i + 1]); }
    float m1 = -REF_OFF - hmax(max3(max3(x1, x3, a1[15]), a3[15], a3[15])); PIN(m1); SBAR();
    sA = MFMA(k1, q0, nm0); SBAR();
    hp[2] = glu2(acc4, 4); PIN(hp); SBAR();
    nm1 = __builtin_amdgcn_mfma_f32_32x32x2f32(aone, m1, z, 0, 0, 0); SBAR();
    hp[3] = glu2(acc4, 6); PIN(hp);
    st16(hp, ws, l16, WS_H + (tile * 112 + 4 * p2 + 2 * cg + 1) * 1024); SBAR();
  }
  f32x8 pa, pb;
  f32x16 g00, g01, g10, g11;
  bf16x8 fa[4], fb[4];
  unsigned sow = WS_W2 + (2 * n64 * 144 + 14 * w) * 1024, soh = WS_H + (2 * tb * 112 + 14 * w) * 1024;
#define EX(P, S, i) P[(i) & 7] = __builtin_amdgcn_exp2f(S[i])
#define T2(u, A0, A1, B0, B1) T2X(o1 = SMF(vb, pb, o1, idx), u, A0, A1, B0, B1)
#define T2X(FIN, u, A0, A1, B0, B1) do { \
    EX(pa, sA, 0); EX(pa, sA, 1); SBAR(); FIN; SBAR(); vb = LDS8(vp + (u) * 1024 + 512); SBAR(); \
    EX(pa, sA, 2); EX(pa, sA, 3); EX(pa, sA, 4); EX(pa, sA, 5); SBAR(); sB = MFMA(k1, q1, nm1); SBAR(); k1 = LDS8(kp + ((u) + 1) * 1024); SBAR(); \
    EX(pa, sA, 6); EX(pa, sA, 7); EX(pb, sA, 8); EX(pb, sA, 9); SBAR(); o0 = SMF(va, pa, o0, idx); SBAR(); \
    EX(pb, sA, 10); EX(pb, sA, 11); EX(pb, sA, 12); EX(pb, sA, 13); SBAR(); A0; A1; SBAR(); EX(pb, sA, 14); EX(pb, sA, 15); SBAR(); \
    EX(pa, sB, 0); EX(pa, sB, 1); SBAR(); o0 = SMF(vb, pb, o0, idx); SBAR(); \
    EX(pa, sB, 2); EX(pa, sB, 3); EX(pa, sB, 4); EX(pa, sB, 5); SBAR(); sA = MFMA(k1, q0, nm0); SBAR(); \
    EX(pa, sB, 6); EX(pa, sB, 7); EX(pb, sB, 8); EX(pb, sB, 9); SBAR(); o1 = SMF(va, pa, o1, idx); SBAR(); va = LDS8(vp + ((u) + 1) * 1024); SBAR(); \
    EX(pb, sB, 10); EX(pb, sB, 11); EX(pb, sB, 12); EX(pb, sB, 13); SBAR(); B0; B1; SBAR(); EX(pb, sB, 14); EX(pb, sB, 15); SBAR(); } while (0)
#define NOP_ do {} while (0)
#define ADV() do { kp += 4096; vp += vstep; } while (0)
#define G2P(u, C, N) do { \
    T2(u, g00 = MFMA(C[0], C[2], g00), (N[0] = ld16c(ws, l16, sow), N[2] = ld16c(ws, l16, soh)), g10 = MFMA(C[1], C[2], g10), N[1] = ld16c(ws, l16, sow + 144 * 1024)); \
    T2(u + 1, g11 = MFMA(C[1], C[3], g11), N[3] = ld16c(ws, l16, soh + 112 * 1024), g01 = MFMA(C[0], C[3], g01), NOP_); \
    sow += 1024; soh += 1024; } while (0)
#define PRIO_AT(k, n) asm volatile("s_cmp_lg_u32 %0, " #k "\n\ts_cbranch_scc1 1f\n\ts_setprio " #n "\n1:" :: "s"(it) : "scc")
#define PRIO(n) __builtin_amdgcn_s_setprio(n)
  T2X(NOP_, 0, KVD(1), KVD(5), KVD(9), NOP_); PRIO(0); T2(1, KVD(13), KVD(2), KVD(6), NOP_);
  STAMP(41);
  asm volatile("s_waitcnt vmcnt(6)\n\ts_barrier" ::: "memory");
  STAMP(42);
  FLAG(WS_F2);
  if (w == 0 && b == 0) st8((u32x2){e0 + 1, e1}, ws, l16 ? 0x7fffff00u : 0u, WS_EPOCH);
  const unsigned vf2 = WS_F2 + (ln < 16 ? (tb >> 1) * 16 + ln : (((n64 & 1) * 8 + (ln & 7)) * 16 + (n64 >> 1) * 4 + ((ln >> 3) & 3))) * FS;
  PRIO(2); T2(2, KVD(10), KVD(14), KVD(3), NOP_); T2(3, KVD(7), KVD(11), KVD(15), NOP_); ADV();
  PRIO(1); T2(0, NOP_, NOP_, POLL(vf2), NOP_); PRIO(0); T2(1, NOP_, NOP_, NOP_, NOP_);
  STAMP(43);
  if (w == 0) { WAITV(0); PIN(pl); wait_flags(pl, vf2, ws, t0, t1); }
  STAMP(44);
  asm volatile("s_waitcnt vmcnt(0)\n\ts_barrier" ::: "memory");
  STAMP(8);
  const unsigned vout = (tb * 64 + ((w >> 1) & 1) * 32 + r3) * 1024 + n64 * 128 + (w >> 2) * 64 + (w & 1) * 32 + hf * 16;
  u32x4 xres;
  PRIO(3);
  fb[0] = ld16c(ws, l16, sow); fb[2] = ld16c(ws, l16, soh); fb[1] = ld16c(ws, l16, sow + 144 * 1024); fb[3] = ld16c(ws, l16, soh + 112 * 1024); SBAR();
  T2(2, fa[0] = ld16c(ws, l16, sow + 1024), fa[2] = ld16c(ws, l16, soh + 1024), fa[1] = ld16c(ws, l16, sow + 145 * 1024), fa[3] = ld16c(ws, l16, soh + 113 * 1024));
  {
    T2(3, g00 = MFMA(fb[0], fb[2], z), g10 = MFMA(fb[1], fb[2], z), g11 = MFMA(fb[1], fb[3], z), g01 = MFMA(fb[0], fb[3], z)); ADV(); }
  xres = __builtin_amdgcn_raw_buffer_load_b128(rx, vout, 0, 0);
  if (w < 2) {
    const unsigned v = ln * 128;
    asm volatile("s_mov_b32 m0, %0\n\ts_nop 0\n\tbuffer_load_dword %1, %2, %3 offen sc1 lds" :: "s"(lds0 + LDS_BYTES - 256), "v"(v), "s"(ws), "s"(WS_W2 + ((2 * n64 + w) * 144 + 112 + 8 * (bi & 3)) * 1024) : "memory"); }
  sow += 2048; soh += 2048;
#pragma unroll 1
  for (int it = 0; it < 5; ++it) { PRIO_AT(3, 2); G2P(0, fa, fb); G2P(2, fb, fa); ADV(); }
  const int hg = (w + n64) & 7;
  const unsigned swo = WS_W2 + (2 * n64 * 144 + 112 + 4 * hg) * 1024, lwo = (w < 3 ? w : w < 6 ? w + 1 : w + 3) * 8192;
#define WOD(i) dma_sc1(lds0 + lwo, (i) * 1024, l16, ws, swo + ((i) & 3) * 1024 + ((i) >> 2) * (144 * 1024))
  asm volatile("s_barrier" ::: "memory");
  PRIO(2);
  T2(0, g00 = MFMA(fa[0], fa[2], g00), (fb[0] = ld16c(ws, l16, sow), fb[2] = ld16c(ws, l16, soh)), g10 = MFMA(fa[1], fa[2], g10), (fb[1] = ld16c(ws, l16, sow + 144 * 1024), fb[3] = ld16c(ws, l16, soh + 112 * 1024)));
  T2(1, g11 = MFMA(fa[1], fa[3], g11), { SBAR(); WOD(0); WOD(1); WOD(2); WOD(3); }, g01 = MFMA(fa[0], fa[3], g01), { WOD(4); WOD(5); WOD(6); WOD(7); });
  PRIO(1);
  T2(2, g00 = MFMA(fb[0], fb[2], g00), (fa[0] = ld16c(ws, l16, sow + 1024), fa[2] = ld16c(ws, l16, soh + 1024)), g10 = MFMA(fb[1], fb[2], g10), (fa[1] = ld16c(ws, l16, sow + 145 * 1024), fa[3] = ld16c(ws, l16, soh + 113 * 1024)));
  PRIO(0);
  T2(3, g11 = MFMA(fb[1], fb[3], g11), NOP_, g01 = MFMA(fb[0], fb[3], g01), NOP_);
  o1 = SMF(vb, pb, o1, idx);
  STAMP(9);
  typedef __attribute__((address_space(3))) f32x4* l4p;
  { const lptr d = lds + L_PRIV + w * 2048 + l16;
    *(l4p)(d) = (f32x4){o1[0], o1[1], o1[2], o1[3]}; *(l4p)(d + 1024) = (f32x4){o1[4], o1[5], o1[6], o1[7]}; }
  { *(__attribute__((address_space(3))) float*)(lds + 65536 + w * 256 + ln * 4) = o1[8]; }
  LBAR();
  {
    const lptr s = lds + L_PRIV + (w ^ 4) * 2048 + l16;
    const f32x4 a = *(l4p)(s), c = *(l4p)(s + 1024);
#pragma unroll
    for (int i = 0; i < 4; ++i) { o0[i] += a[i]; o0[4 + i] += c[i]; }
    o0[8] += *(__attribute__((address_space(3))) float*)(lds + 65536 + (w ^ 4) * 256 + ln * 4);
    float am = 0.f, sm = 0.f;
#pragma unroll
    for (int i = 0; i < 8; ++i) { am = __builtin_fmaxf(am, __builtin_fabsf(o0[i])); sm += __builtin_fabsf(o0[i]); }
    const bool ok = !FORCE_FB && o0[8] < 0x1p120f && o0[8] > 0x1p-100f && sm < INFINITY && am > 0x1p-104f;
    if (__builtin_expect(!__all(ok), 0)) {
      const unsigned sk = WS_KV + ah * 131072, vv = vrow ? (hf * 16 + r3) * 16 : 0x7fffff00u, one = (r3 & 27) == 16 ? 0x3f803f80u : 0u;
      float m = -INFINITY;
#pragma unroll 1
      for (int t = 0; t < 64; ++t) m = max16(m, MFMA(ld16c(ws, l16, sk + t * 1024), q0, z));
      m = -hmax(m);
#pragma unroll
      for (int i = 0; i < 16; ++i) nm0[i] = m;
      o0 = z;
#pragma unroll 1
      for (int t = 0; t < 64; ++t) {
        const f32x16 s_ = MFMA(ld16c(ws, l16, sk + t * 1024), q0, nm0);
        const u32x4 v0 = __builtin_bit_cast(u32x4, ld16c(ws, vv, sk + 65536 + t * 1024)) | one, v1 = __builtin_bit_cast(u32x4, ld16c(ws, vv, sk + 65536 + t * 1024 + 512)) | one;
#pragma unroll
        for (int i = 0; i < 8; ++i) { pa[i] = __builtin_amdgcn_exp2f(s_[i]); pb[i] = __builtin_amdgcn_exp2f(s_[8 + i]); }
        o0 = SMF(__builtin_bit_cast(bf16x8, v0), pa, o0, idx); o0 = SMF(__builtin_bit_cast(bf16x8, v1), pb, o0, idx);
      }
    }
  }
  PIN(fa[0]); PIN(fa[1]); PIN(fa[2]); PIN(fa[3]);
  WAITV(0);
  { u32x4 ov = pack8(o0, 0, __builtin_amdgcn_rcpf(o0[8]));
#pragma unroll
    for (int i = 0; i < 4; ++i) ov[i] = ov[i] < 0xfffffffeu ? ov[i] : 0xfffffffeu;
    st16(ov, ws, l16, WS_O + (qown * 32 + ah) * 1024); }
  STAMP(45);
  {
    const unsigned soo = WS_O + (2 * tb * 32 + 4 * hg) * 1024; unsigned spins;
#define OL0 "buffer_load_dwordx4 v[0:3], %[l], %[r], %[o0] offen sc1\n\t"
#define OL1 "buffer_load_dwordx4 v[4:7], %[l], %[r], %[o1] offen sc1\n\t"
#define OL2 "buffer_load_dwordx4 v[8:11], %[l], %[r], %[o0] offen offset:1024 sc1\n\t"
#define OL3 "buffer_load_dwordx4 v[12:15], %[l], %[r], %[o1] offen offset:1024 sc1\n\t"
#define OL4 "buffer_load_dwordx4 v[16:19], %[l], %[r], %[o0] offen offset:2048 sc1\n\t"
#define OL5 "buffer_load_dwordx4 v[20:23], %[l], %[r], %[o1] offen offset:2048 sc1\n\t"
#define OL6 "buffer_load_dwordx4 v[24:27], %[l], %[r], %[o0] offen offset:3072 sc1\n\t"
#define OL7 "buffer_load_dwordx4 v[28:31], %[l], %[r], %[o1] offen offset:3072 sc1\n\t"
#define OCHK(r0, r1, r2, r3) "v_max3_u32 v80, v" #r0 ", v" #r1 ", v" #r2 "\n\tv_max_u32 v80, v80, v" #r3 "\n\tv_cmp_eq_u32 vcc, -1, v80\n\t"
#define OSTEP(n, r0, r1, r2, r3, ga, gb, wa_, wb_) \
    "s_getpc_b64 %[ret]\n\ts_waitcnt vmcnt(" #n ")\n\t" OCHK(r0, r1, r2, r3) "s_cbranch_vccnz 3f\n\t" \
    "v_mfma_f32_32x32x16_bf16 " ga ", " wa_ ", v[" #r0 ":" #r3 "], " ga "\n\tv_mfma_f32_32x32x16_bf16 " gb ", " wb_ ", v[" #r0 ":" #r3 "], " gb "\n\t"
#define ORE(r0, r1, r2, r3, load) OCHK(r0, r1, r2, r3) "s_cbranch_vccz 4f\n\t" load "4:\n\t"
    unsigned long long ret;
        const lptr lw = lds + lwo + l16;
    asm volatile("ds_read_b128 v[48:51], %[lw]\n\tds_read_b128 v[52:55], %[lw] offset:1024\n\tds_read_b128 v[56:59], %[lw] offset:2048\n\tds_read_b128 v[60:63], %[lw] offset:3072\n\t"
                 "ds_read_b128 v[64:67], %[lw] offset:4096\n\tds_read_b128 v[68:71], %[lw] offset:5120\n\tds_read_b128 v[72:75], %[lw] offset:6144\n\tds_read_b128 v[76:79], %[lw] offset:7168\n\t"
                 "s_mov_b32 %[sp], 0\n\ts_waitcnt lgkmcnt(0)\n\ts_barrier\n\t" OL0 OL1 OL2 OL3 OL4 OL5 OL6 OL7
                 "v_mfma_f32_32x32x16_bf16 %[g00], %[f0], %[f2], %[g00]\n\tv_mfma_f32_32x32x16_bf16 %[g10], %[f1], %[f2], %[g10]\n\t"
                 "v_mfma_f32_32x32x16_bf16 %[g01], %[f0], %[f3], %[g01]\n\tv_mfma_f32_32x32x16_bf16 %[g11], %[f1], %[f3], %[g11]\n\t"
                 OSTEP(7, 0, 1, 2, 3, "%[g00]", "%[g10]", "v[48:51]", "v[64:67]") OSTEP(6, 4, 5, 6, 7, "%[g01]", "%[g11]", "v[48:51]", "v[64:67]")
                 OSTEP(5, 8, 9, 10, 11, "%[g00]", "%[g10]", "v[52:55]", "v[68:71]") OSTEP(4, 12, 13, 14, 15, "%[g01]", "%[g11]", "v[52:55]", "v[68:71]")
                 OSTEP(3, 16, 17, 18, 19, "%[g00]", "%[g10]", "v[56:59]", "v[72:75]") OSTEP(2, 20, 21, 22, 23, "%[g01]", "%[g11]", "v[56:59]", "v[72:75]")
                 OSTEP(1, 24, 25, 26, 27, "%[g00]", "%[g10]", "v[60:63]", "v[76:79]") OSTEP(0, 28, 29, 30, 31, "%[g01]", "%[g11]", "v[60:63]", "v[76:79]")
                 "s_branch 2f\n"
                 "3:\n\ts_add_u32 %[sp], %[sp], 1\n\ts_cmp_gt_u32 %[sp], %[cap]\n\ts_cbranch_scc1 2f\n\ts_waitcnt vmcnt(0)\n\t"
                 ORE(0, 1, 2, 3, OL0) ORE(4, 5, 6, 7, OL1) ORE(8, 9, 10, 11, OL2) ORE(12, 13, 14, 15, OL3)
                 ORE(16, 17, 18, 19, OL4) ORE(20, 21, 22, 23, OL5) ORE(24, 25, 26, 27, OL6) ORE(28, 29, 30, 31, OL7)
                 "s_waitcnt vmcnt(0)\n\ts_setpc_b64 %[ret]\n"
                 "2:\n\ts_waitcnt vmcnt(0) lgkmcnt(0)\n\ts_nop 15"
                 : [g00] "+v"(g00), [g01] "+v"(g01), [g10] "+v"(g10), [g11] "+v"(g11), [sp] "=&s"(spins), [ret] "=&s"(ret)
                 : [l] "v"(l16), [r] "s"(ws), [lw] "v"(lw), [f0] "v"(fa[0]), [f1] "v"(fa[1]), [f2] "v"(fa[2]), [f3] "v"(fa[3]), [o0] "s"(soo), [o1] "s"(soo + 32768), [cap] "i"(SPIN_CAP)
                 : "memory", "scc", "vcc", "v0", "v1", "v2", "v3", "v4", "v5", "v6", "v7", "v8", "v9", "v10", "v11", "v12", "v13", "v14", "v15", "v16", "v17", "v18", "v19",
                   "v20", "v21", "v22", "v23", "v24", "v25", "v26", "v27", "v28", "v29", "v30", "v31",
                   "v48", "v49", "v50", "v51", "v52", "v53", "v54", "v55", "v56", "v57", "v58", "v59",
                   "v60", "v61", "v62", "v63", "v64", "v65", "v66", "v67", "v68", "v69", "v70", "v71", "v72", "v73", "v74", "v75", "v76", "v77", "v78", "v79", "v80");
  }
  STAMP(12);
  { const lptr d = lds + w * 16384 + l16;
#pragma unroll
    for (int i = 0; i < 4; ++i) {
      *(l4p)(d + i * 1024) = (f32x4){g00[4 * i], g00[4 * i + 1], g00[4 * i + 2], g00[4 * i + 3]};
      *(l4p)(d + (4 + i) * 1024) = (f32x4){g01[4 * i], g01[4 * i + 1], g01[4 * i + 2], g01[4 * i + 3]};
      *(l4p)(d + (8 + i) * 1024) = (f32x4){g10[4 * i], g10[4 * i + 1], g10[4 * i + 2], g10[4 * i + 3]};
      *(l4p)(d + (12 + i) * 1024) = (f32x4){g11[4 * i], g11[4 * i + 1], g11[4 * i + 2], g11[4 * i + 3]}; } }
  lptr rs0 = lds + (2 * w + hf) * 1024 + r3 * 16, rs1 = rs0 + 65536; PIN(rs0); PIN(rs1);
  f32x4 ra = {bflo(xres[0]), bfhi(xres[0]), bflo(xres[1]), bfhi(xres[1])}, rc = {bflo(xres[2]), bfhi(xres[2]), bflo(xres[3]), bfhi(xres[3])}; PIN(ra); PIN(rc);
  LBAR();
  STAMP(13);
  {
    f32x4 pa_[8], pc_[8];
#pragma unroll
    for (int i = 0; i < 8; ++i) { pa_[i] = *(l4p)((i < 4 ? rs0 : rs1) + (i & 3) * 16384); pc_[i] = *(l4p)((i < 4 ? rs0 : rs1) + (i & 3) * 16384 + 512); }
    SBAR();
#pragma unroll
    for (int i = 0; i < 8; ++i) { ra += pa_[i]; rc += pc_[i]; PIN(ra); PIN(rc); }
    const u32x4 r = {cvtpk(ra[0], ra[1]), cvtpk(ra[2], ra[3]), cvtpk(rc[0], rc[1]), cvtpk(rc[2], rc[3])};
    __builtin_amdgcn_raw_buffer_store_b128(r, mk(pout, 2048 * 1024), vout, 0, 0);
  }
  STAMP(14);
  STAMP_END();
}

extern "C" void kernel_launch(void* const* d_in, const int* in_sizes, int n_in, void* d_out, int out_size, void* d_ws, size_t ws_size, hipStream_t stream) {
  (void)hipFuncSetAttribute((const void*)fused_block, hipFuncAttributeMaxDynamicSharedMemorySize, LDS_BYTES);
  hipLaunchKernelGGL(fused_block, dim3(256), dim3(512), LDS_BYTES, stream, d_in[0], d_in[1], d_in[2], d_in[4], d_in[5], d_in[6], d_out, d_ws);
}
```

```cpp
#include <hip/hip_runtime.h>
#include <hip/hip_bf16.h>

typedef __bf16 bf16x8 __attribute__((ext_vector_type(8)));
typedef __bf16 bf16x16 __attribute__((ext_vector_type(16)));
typedef __bf16 bf16x2 __attribute__((ext_vector_type(2)));
typedef short s16x4 __attribute__((ext_vector_type(4)));
typedef float f32x16 __attribute__((ext_vector_type(16)));
typedef float f32x8 __attribute__((ext_vector_type(8)));
typedef float f32x4 __attribute__((ext_vector_type(4)));
typedef float f32x2 __attribute__((ext_vector_type(2)));
typedef unsigned u32x4 __attribute__((ext_vector_type(4)));
typedef unsigned u32x2 __attribute__((ext_vector_type(2)));
typedef unsigned u32x16 __attribute__((ext_vector_type(16)));
typedef __amdgpu_buffer_rsrc_t rsrc_t;
typedef __attribute__((address_space(3))) char* lptr;
#define DI __device__ __forceinline__
#define SBAR() __builtin_amdgcn_sched_barrier(0)
#define MFMA(a, b, c) __builtin_amdgcn_mfma_f32_32x32x16_bf16(a, b, c, 0, 0, 0)
#define SMF(a, b, c, idx) __builtin_amdgcn_smfmac_f32_32x32x32_bf16(a, __builtin_bit_cast(bf16x16, b), c, idx, 0, 0)
#define LDS8(p) (*(const __attribute__((address_space(3))) bf16x8*)(p))
#define PIN(x) asm volatile("" : "+v"(x))
#define LANE(n) unsigned n = l16; PIN(n); n >>= 4

#define FORCE_FB 0
#define DBG_STAMPS 0

constexpr unsigned WS_EPOCH = 0;
constexpr unsigned FS = 128;
constexpr unsigned WS_F0 = 262144, WS_F1 = WS_F0 + 256 * FS, WS_F2 = WS_F1 + 256 * FS, WS_F3 = WS_F2 + 256 * FS;
constexpr unsigned WS_STAT = 32768;
constexpr unsigned WS_CS = 65536;
constexpr unsigned WS_Q = 1u << 20;
constexpr unsigned WS_KV = WS_Q + (2u << 20);
constexpr unsigned WS_O = WS_KV + (4u << 20);
constexpr unsigned WS_W2 = WS_O + (2u << 20);
constexpr unsigned WS_H = WS_W2 + (3u << 20);
constexpr unsigned WS_DBG = WS_H + (7u << 20);
constexpr unsigned WS_END = WS_DBG + (1u << 20);
constexpr int U_XA = 0, U_G1 = 24576, U_XB = 49152, U_G2 = 73728, U_G0 = 98304, UD = 49152;
constexpr int L_XCS = 122880, L_V = 73728, L_PRIV = 139264, L_ONES = 155648, LDS_BYTES = 163840;
constexpr unsigned SPIN_CAP = 1u << 15, CODE_PF = 26112;
constexpr float REF_OFF = 90.f;

DI rsrc_t mk(const void* p, unsigned bytes) { return __builtin_amdgcn_make_buffer_rsrc((void*)p, (short)0, (int)bytes, 0x00020000); }
DI unsigned cvtpk(float lo, float hi) { f32x2 v = {lo, hi}; return __builtin_bit_cast(unsigned, __builtin_convertvector(v, bf16x2)); }
DI unsigned cvtpa(float lo, float hi) { unsigned r; asm("v_cvt_pk_bf16_f32 %0, %1, %2" : "=v"(r) : "v"(lo), "v"(hi)); return r; }
DI float bflo(unsigned u) { return __uint_as_float(u << 16); }
DI float bfhi(unsigned u) { return __uint_as_float(u & 0xffff0000u); }
DI float rbf(float f) { return bflo(cvtpk(f, f)); }
DI float max3(float a, float b, float c) { return __builtin_fmaxf(__builtin_fmaxf(a, b), c); }
DI s16x4 trd(lptr p) { return __builtin_amdgcn_ds_read_tr16_b64_v4i16((__attribute__((address_space(3))) s16x4*)p); }
DI bf16x8 trfrag(lptr p, int step) { s16x4 a = trd(p), b = trd(p + step); return __builtin_bit_cast(bf16x8, __builtin_shufflevector(a, b, 0, 1, 2, 3, 4, 5, 6, 7)); }
#define dma(base, imm, voff, r, soff) asm volatile("s_add_i32 m0, %0, %4\n\ts_nop 0\n\tbuffer_load_dwordx4 %1, %2, %3 offen lds" :: "s"(base), "v"(voff), "s"(r), "s"(soff), "i"(imm) : "memory", "scc")
#define dma_sc1(base, imm, voff, r, soff) asm volatile("s_add_i32 m0, %0, %4\n\ts_nop 0\n\tbuffer_load_dwordx4 %1, %2, %3 offen sc1 lds" :: "s"(base), "v"(voff), "s"(r), "s"(soff), "i"(imm) : "memory", "scc")
#define ALD4(dst, voff, r, soff) asm volatile("buffer_load_dwordx4 %0, %1, %2, %3 offen" : "=v"(dst) : "v"(voff), "s"(r), "s"(soff) : "memory")
#define ALD4C(dst, voff, r, soff) asm volatile("buffer_load_dwordx4 %0, %1, %2, %3 offen sc1" : "=v"(dst) : "v"(voff), "s"(r), "s"(soff) : "memory")
#define ALD2C(dst, voff, r, soff) asm volatile("buffer_load_dwordx2 %0, %1, %2, %3 offen sc1" : "=v"(dst) : "v"(voff), "s"(r), "s"(soff) : "memory")
#define ALD1C(dst, voff, r, soff) asm volatile("buffer_load_dword %0, %1, %2, %3 offen sc1" : "=v"(dst) : "v"(voff), "s"(r), "s"(soff) : "memory")
#define CLD(op, dst, voff, r, soff, lim) asm volatile("s_cmp_ge_u32 %4, %5\n\ts_cbranch_scc1 1f\n\t" op "\n1:" : "=v"(dst) : "v"(voff), "s"(r), "s"(soff), "s"(w), "i"(lim) : "memory", "scc")
#define POLL(voff) CLD("buffer_load_dwordx2 %0, %1, %2, %3 offen sc1", pl, voff, ws, 0, 1)
#define WAITV(n) asm volatile("s_waitcnt vmcnt(%0)" :: "i"(n) : "memory")
#define WAITBAR(n) asm volatile("s_waitcnt vmcnt(%0) lgkmcnt(0)\n\ts_barrier" :: "i"(n) : "memory")
#define LBAR() asm volatile("s_waitcnt lgkmcnt(0)\n\ts_barrier" ::: "memory")
DI void st16(u32x4 v, rsrc_t r, unsigned voff, unsigned soff) {
  asm volatile("buffer_store_dwordx4 %0, %1, %2, %3 offen sc1\n\ts_nop 1" :: "v"(v), "v"(voff), "s"(r), "s"(soff) : "memory"); }
DI void st8(u32x2 v, rsrc_t r, unsigned voff, unsigned soff) { __builtin_amdgcn_raw_buffer_store_b64(v, r, voff, soff, 16); }
DI void st4(unsigned v, rsrc_t r, unsigned voff, unsigned soff) { __builtin_amdgcn_raw_buffer_store_b32(v, r, voff, soff, 16); }
DI bf16x8 ld16c(rsrc_t r, unsigned voff, unsigned soff) { return __builtin_bit_cast(bf16x8, __builtin_amdgcn_raw_buffer_load_b128(r, voff, soff, 16)); }
template <int CTRL> DI float dppadd(float v) { return v + __int_as_float(__builtin_amdgcn_update_dpp(0, __float_as_int(v), CTRL, 0xf, 0xf, true)); }
DI void wait_flags(u32x2 f, unsigned voff, rsrc_t ws, unsigned t0, unsigned t1) {
#pragma unroll 1
  for (unsigned spins = 0; spins < SPIN_CAP; ++spins) {
    if (__all(f[0] == t0 && f[1] == t1)) break;
    __builtin_amdgcn_s_sleep(2);
    asm volatile("buffer_load_dwordx2 %0, %1, %2, 0 offen sc1\n\ts_waitcnt vmcnt(0)" : "=&v"(f) : "v"(voff), "s"(ws) : "memory");
  }
}
#define STAMP(i)
#define STAMP_END()

DI u32x4 pack8(const f32x16& a, int o, float s) {
  return (u32x4){cvtpa(a[o] * s, a[o + 1] * s), cvtpa(a[o + 2] * s, a[o + 3] * s), cvtpa(a[o + 4] * s, a[o + 5] * s), cvtpa(a[o + 6] * s, a[o + 7] * s)}; }
DI u32x4 glu8(const f32x16& a, float nc, float sd) {
  float h[8];
#pragma unroll
  for (int i = 0; i < 8; ++i) { const float e = __builtin_amdgcn_exp2f(a[i] * nc); h[i] = a[8 + i] * __builtin_amdgcn_rcpf(__builtin_fmaf(e, sd, sd)); }
  return (u32x4){cvtpa(h[0], h[1]), cvtpa(h[2], h[3]), cvtpa(h[4], h[5]), cvtpa(h[6], h[7])}; }

__global__ __launch_bounds__(512, 2) void fused_block(const void* __restrict__ px, const void* __restrict__ pwqkv, const void* __restrict__ pwo,
                                                      const void* __restrict__ pwg, const void* __restrict__ pwu, const void* __restrict__ pwd,
                                                      void* __restrict__ pout, void* __restrict__ pws) {
  extern __shared__ __attribute__((aligned(16))) char smem[];
  asm volatile("" :: "s"(px), "s"(pwqkv), "s"(pwo), "s"(pwg), "s"(pwu), "s"(pwd), "s"(pout), "s"(pws));
  const lptr lds = (lptr)smem;
  const unsigned lds0 = (unsigned)(size_t)lds;
  const int tid = threadIdx.x, lane = tid & 63, w = __builtin_amdgcn_readfirstlane(tid >> 6);
  const int b = blockIdx.x, xcd = b & 7, bi = b >> 3;
  const int r32 = lane & 31, hi = lane >> 5;
  const rsrc_t ws = mk(pws, WS_END), rx = mk(px, 2048 * 1024);
  STAMP(0);
  const int rb = (xcd & 1) * 8 + (bi & 7), Q4 = xcd >> 1, sj = bi >> 3, set = Q4 * 4 + sj;
  const int rot = 2 * (xcd & 1) + (bi & 4), pos = (bi >> 1) & 1, half = bi & 1, kpart = rot + pos;
  const unsigned l3 = lane >> 3, l7 = lane & 7, l16 = lane * 16;
  const int wx = (w + 2 * sj) & 7, ww = (w + 2 * bi) & 7;
  const unsigned vx0 = (rb * 128 + wx * 16 + l3) * 1024 + ((l7 ^ (lane >> 4)) * 16), vx1 = (vx0 + 8192) ^ 64;
  const unsigned mx = lds0 + (wx >> 2) * UD + (wx & 3) * 2048, mw = lds0 + ww * 1024;
#define KC(c) (unsigned)(((c) + rot) & 7)
#define SL(c) (((c) % 3) * 8192)
  dma(mx, 0, vx0, rx, rot * 128); dma(mx, 1024, vx1, rx, rot * 128); SBAR();
  const bool tb_ = sj == 0;
  const int p1 = 7 * Q4 + 2 * sj - 1, p2 = p1 + 1;
  const unsigned col0 = 128 * Q4 + (tb_ ? 1024 : sj * 512 - 448);
  const unsigned col1 = tb_ ? 512 + 128 * Q4 : 1536 + 64 * p1, col2 = tb_ ? 128 * Q4 : 3328 + 64 * p1;
  const unsigned str12 = tb_ ? 3072 : 3584, wr = ww * 8 + l3, wpc = (l7 ^ ((lane >> 2) & 4)) * 16;
  const rsrc_t rg0 = mk(pwqkv, 512 * 3072);
  const unsigned vg0 = wr * 3072 + wpc + col0 * 2;
  dma(mw, U_G0, vg0, rg0, rot * (64 * 3072)); SBAR();
  const rsrc_t rg1 = mk(tb_ ? pwqkv : pwg, 512 * str12), rg2 = mk(tb_ ? pwqkv : pwu, 512 * str12);
  const unsigned vg1 = wr * str12 + wpc + (tb_ ? col1 * 2 : p1 * 128), vg2 = (vg1 - (tb_ ? 1024 : 0)) ^ 32;
  dma(mw, U_G1, vg1, rg1, rot * 64 * str12); dma(mw, U_G2, vg2, rg2, rot * 64 * str12); SBAR();
  if (w == 0) {
    unsigned long long pc; asm volatile("s_getpc_b64 %0" : "=s"(pc));
    const rsrc_t rc = mk((const void*)(pc & ~127ull), CODE_PF);
    const unsigned v = lane < 8 ? (bi * 8 + lane) * 128 : 0x7fffff00u;
    asm volatile("s_mov_b32 m0, %0\n\ts_nop 0\n\tbuffer_load_dword %1, %2, 0 offen lds" :: "s"(lds0 + LDS_BYTES - 256), "v"(v), "s"(rc) : "memory"); }
  STAMP(47);
#define DMA1(c, i) do { \
    if ((i) == 0) dma(mx, SL(c), vx0, rx, KC(c) * 128); if ((i) == 1) dma(mx, SL(c) + 1024, vx1, rx, KC(c) * 128); \
    if ((i) == 2) dma(mw, SL(c) + U_G0, vg0, rg0, KC(c) * (64 * 3072)); if ((i) == 3) dma(mw, SL(c) + U_G1, vg1, rg1, KC(c) * 64 * str12); \
    if ((i) == 4) dma(mw, SL(c) + U_G2, vg2, rg2, KC(c) * 64 * str12); } while (0)
#define DMA2(c, i) do { constexpr int sl_ = SL(c) + ((i) & 2 ? U_XA : U_G1); const unsigned kc_ = KC(2 * ((c) - 8) + ((i) >> 1)) * (64 * 3584); \
    if ((i) & 1) dma(mw, sl_ + UD, vg4, rg4, kc_); else dma(mw, sl_, vg3, rg3, kc_); } while (0)
  const int tt = w & 3, cg = w >> 2;
  const int q4 = (lane & 15) >> 2, p4 = lane & 3, g16 = (lane >> 4) & 1, swr = (q4 >> 1) * 4;
  const int wrow = (8 * hi + q4) * 128 + (p4 & 1) * 8;
  const int pq = 4 * cg + 2 * g16 + (p4 >> 1);
  const int gl0 = g16 * UD + (pq ^ swr) * 16, gl1 = gl0 ^ 32;
  lptr wp0 = lds + U_G0 + wrow + (pq ^ swr) * 16;
  lptr wp1 = lds + wrow + (tb_ ? (pq ^ swr) * 16 : gl0);
  lptr wp2 = lds + wrow + (tb_ ? UD + (pq ^ swr ^ 2) * 16 : gl1);
  lptr xp[4];
#pragma unroll
  for (int s = 0; s < 4; ++s) { xp[s] = lds + (tt >> 1) * UD + (tt & 1) * 4096 + r32 * 128 + (((2 * s + hi) ^ ((r32 >> 1) & 7)) * 16); PIN(xp[s]); }
  PIN(wp0); PIN(wp1); PIN(wp2);
  if (tid < 272) { const unsigned o_ = (tid & 11) == 0 ? 0x3f803f80u : 0u;
    *(__attribute__((address_space(3))) u32x4*)(lds + L_ONES + tid * 16) = (u32x4){o_, o_, o_, o_}; }
  asm volatile("s_barrier" ::: "memory");
  STAMP(48);
  DMA1(1, 0); DMA1(1, 1); DMA1(1, 2); DMA1(1, 3); DMA1(1, 4);
  STAMP(49);
  const rsrc_t rg3 = mk(pwg, 512 * 3584), rg4 = mk(pwu, 512 * 3584);
  unsigned vg3 = wr * 3584 + wpc + p2 * 128, vg4 = vg3 ^ 32; PIN(vg3); PIN(vg4);
  const int ah = xcd * 4 + (bi & 3), aq = bi >> 2, kh = w >> 2;
  const int qown = aq * 8 + 2 * (w & 3) + kh;
  const unsigned gcol = (r32 & 15) + 32 * cg;
  unsigned vf0 = WS_F0 + (lane < 16 ? rb * 16 + lane : (lane & 15) * 16 + set) * FS, vst = (rb * 128 + tt * 32 + r32) * 4, vc0, vc1, vc2;
  { const unsigned vb_ = hi * 5120 + 32 * cg + r32, vg_ = hi * 5120 + (r32 < 16 ? col1 : col2) + gcol;
    vc0 = (col0 + vb_) * 16; vc1 = (tb_ ? col1 + vb_ : vg_) * 16; vc2 = (tb_ ? col2 + vb_ : vg_ + 16) * 16; }
  PIN(vf0); PIN(vst); PIN(vc0); PIN(vc1); PIN(vc2);
  lptr cp1 = lds + (w == 0 ? U_G0 : w == 1 ? U_G1 : U_G2) + pos * 8192 + wrow + (((4 * half + 2 * g16 + (p4 >> 1)) ^ swr ^ (w & 2)) * 16); PIN(cp1);
  const unsigned csb = WS_CS + (kpart >> 2) * 81920 + (kpart & 3) * 4;
  auto colsum = [&](lptr cp, unsigned ccol) {
    const __bf16 o1 = (__bf16)1.0f; const bf16x8 ones = {o1, o1, o1, o1, o1, o1, o1, o1};
    const bf16x8 f0 = trfrag(cp, 512), f1 = trfrag(cp + 2048, 512), f2 = trfrag(cp + 4096, 512), f3 = trfrag(cp + 6144, 512);
    const f32x16 cs = MFMA(ones, f3, MFMA(ones, f2, MFMA(ones, f1, MFMA(ones, f0, (f32x16){}))));
    const float ch = rbf(cs[0]);
    st4(cvtpk(ch, cs[0] - ch), ws, hi ? 0x7fffff00u : l16, csb + ccol * 16);
  };
  u32x2 ep, tag; u32x4 xr; unsigned e0, e1, t0, t1;
  const unsigned srow = rb * 128 + set * 8 + w;
  float sv[8], sa, sb, smean;
  auto swapadd16 = [](float v) { auto r = __builtin_amdgcn_permlane16_swap(__float_as_uint(v), __float_as_uint(v), false, false); return __uint_as_float(r[0]) + __uint_as_float(r[1]); };
  auto swapadd32 = [](float v) { auto r = __builtin_amdgcn_permlane32_swap(__float_as_uint(v), __float_as_uint(v), false, false); return __uint_as_float(r[0]) + __uint_as_float(r[1]); };
#define STATS(i) do { \
    if ((i) == 0 || (i) == 1) { sv[4 * (i)] = bflo(xr[2 * (i)]); sv[4 * (i) + 1] = bfhi(xr[2 * (i)]); sv[4 * (i) + 2] = bflo(xr[2 * (i) + 1]); sv[4 * (i) + 3] = bfhi(xr[2 * (i) + 1]); \
      PIN(sv[4 * (i)]); PIN(sv[4 * (i) + 1]); PIN(sv[4 * (i) + 2]); PIN(sv[4 * (i) + 3]); } \
    if ((i) == 2) { sa = (sv[0] + sv[1]) + sv[2]; PIN(sa); sb = (sv[3] + sv[4]) + sv[5]; PIN(sb); } \
    if ((i) == 3) { sa = ((sa + sv[6]) + sv[7]) + sb; PIN(sa); } \
    if ((i) == 4 || (i) == 14) { sa = dppadd<0x121>(sa); PIN(sa); } if ((i) == 5 || (i) == 15) { sa = dppadd<0x122>(sa); PIN(sa); } \
    if ((i) == 6 || (i) == 16) { sa = dppadd<0x124>(sa); PIN(sa); } if ((i) == 7 || (i) == 17) { sa = dppadd<0x128>(sa); PIN(sa); } \
    if ((i) == 8 || (i) == 18) { sa = swapadd16(sa); PIN(sa); } \
    if ((i) == 9) { smean = swapadd32(sa) * (1.f / 512); PIN(smean); sa = 0.f; } \
    if ((i) >= 10 && (i) <= 13) { const float d_ = sv[2 * (i) - 20] - smean, e_ = sv[2 * (i) - 19] - smean; sa = __builtin_fmaf(e_, e_, __builtin_fmaf(d_, d_, sa)); PIN(sa); } \
    if ((i) == 19) { sa = rbf(swapadd32(sa) * (1.f / 512)); PIN(sa); } \
    if ((i) == 20) { sa = __builtin_amdgcn_sqrtf(rbf(sa + 1e-5f)); PIN(sa); } \
    if ((i) == 21) st4(cvtpk(-smean, sa), ws, lane ? 0x7fffff00u : 0u, WS_STAT + srow * 4); } while (0)
  bf16x8 xf[32], wa[3], wb[3];
  f32x16 acc0 = {}, acc1 = {}, acc2 = {};
  u32x2 pl; unsigned stat; u32x4 cs0, cs1, cs2;
#define RD1(W, ks) do { xf[ks] = LDS8(xp[(ks) & 3] + SL((ks) >> 2)); W[0] = trfrag(wp0 + SL((ks) >> 2) + ((ks) & 3) * 2048, 512); } while (0)
#define RD1B(W, ks) W[1] = trfrag(wp1 + U_G1 + SL((ks) >> 2) + ((ks) & 3) * 2048, 512)
#define RD1C(W, ks) W[2] = trfrag(wp2 + U_G1 + SL((ks) >> 2) + ((ks) & 3) * 2048, 512)
#define RD2A(W, ks) W[0] = trfrag(wp1 + SL(8 + ((ks) >> 3)) + ((ks) & 4 ? U_XA : U_G1) + ((ks) & 3) * 2048, 512)
#define RD2B(W, ks) W[1] = trfrag(wp2 + SL(8 + ((ks) >> 3)) + ((ks) & 4 ? U_XA : U_G1) + ((ks) & 3) * 2048, 512)
#define FLAG(f) do { if (w == 0) st8(tag, ws, l16 ? 0x7fffff00u : 0u, (f) + (rb * 16 + set) * FS); } while (0)
  STAMP(1);
  WAITBAR(5);
  STAMP(2);
  RD1(wa, 0); RD1B(wa, 0); RD1C(wa, 0);
  { const unsigned z_ = 0; ALD4(xr, l16, rx, srow * 1024); CLD("buffer_load_dwordx2 %0, %1, %2, %3 offen sc1", ep, z_, ws, WS_EPOCH, 1); }
  DMA1(2, 0); DMA1(2, 4);
#define STEP1(ks, WC, WN) do { constexpr int c_ = ((ks) + 1) >> 2, s_ = ((ks) + 1) & 3; \
    if (s_ == 0) { SBAR(); \
      if ((c_ == 1 || c_ == 2) && w < 3 && pos == c_ - 1) colsum(cp1, (w == 0 ? col0 : w == 1 ? col1 : col2) + 32 * half); \
      if (c_ == 2) st16((u32x4){~0u, ~0u, ~0u, ~0u}, ws, l16, WS_O + (qown * 32 + ah) * 1024);     \
      if (c_ == 5 && (w == 1 || w == 2)) colsum(lds + L_XCS + wrow + (((4 * (w - 1) + 2 * g16 + (p4 >> 1)) ^ swr) * 16), (half ? 3328 : 1536) + 64 * p2 + 32 * (w - 1)); \
      if (c_ == 7 && w == 0) { WAITV(4); PIN(pl); wait_flags(pl, vf0, ws, t0, t1); } \
      STAMP(16 + 2 * c_); WAITBAR(c_ == 2 ? 6 : c_ < 7 ? 5 : c_ == 7 ? 4 : 8); STAMP(17 + 2 * c_); \
      if (c_ == 1) { PIN(ep); PIN(xr); e0 = __builtin_amdgcn_readfirstlane(ep[0]); e1 = __builtin_amdgcn_readfirstlane(ep[1]);     \
        t0 = e0 * 0x9E3779B1u + 0x7F4A7C15u; t1 = (e1 ^ 0x85EBCA6Bu) + e0; tag = (u32x2){t0, t1}; }     \
      if (c_ == 3) dma(mw, L_XCS, vg3, (half ? rg4 : rg3), kpart * (64 * 3584)); \
      if (c_ == 4) FLAG(WS_F0); \
      if (c_ == 6) { FLAG(WS_F3); POLL(vf0); } \
      if (c_ == 7) { ALD1C(stat, vst, ws, WS_STAT); ALD4C(cs0, vc0, ws, WS_CS); ALD4C(cs1, vc1, ws, WS_CS); ALD4C(cs2, vc2, ws, WS_CS); } \
      if (c_ == 8) { wp1 = lds + wrow + gl0; wp2 = lds + wrow + gl1; PIN(wp1); PIN(wp2); vf0 = WS_F3 + ((lane & 15) * 16 + set) * FS; POLL(vf0); } } \
    SBAR(); \
    if ((ks) < 31) RD1(WN, ((ks) + 1) & 31); else RD2A(WN, 0); \
    if (s_ == 3) { RD1B(WN, ((ks) + 1) & 31); RD1C(WN, ((ks) + 1) & 31); } \
    if (s_ != 3) { if (c_ <= 5) DMA1(c_ + 2, s_); else DMA2(c_ + 2, s_); } SBAR(); \
    acc0 = MFMA(WC[0], xf[ks], acc0); SBAR(); STATS(3 * ((ks) - 3)); \
    if (s_ != 3) { if ((ks) < 31) RD1B(WN, ((ks) + 1) & 31); else RD2B(WN, 0); } \
    if (s_ == 3) { if (c_ <= 5) DMA1(c_ + 2, s_); else DMA2(c_ + 2, s_); } if (c_ == 8) DMA2(10, 1); SBAR(); \
    acc1 = MFMA(WC[1], xf[ks], acc1); SBAR(); STATS(3 * ((ks) - 3) + 1); \
    if ((ks) < 31 && s_ != 3) RD1C(WN, ((ks) + 1) & 31); \
    if (c_ >= 1 && c_ <= 5 && s_ == 0) DMA1(c_ + 2, 4); if (c_ == 8) DMA2(10, 2); SBAR(); \
    acc2 = MFMA(WC[2], xf[ks], acc2); SBAR(); STATS(3 * ((ks) - 3) + 2); SBAR(); } while (0)
#define STEP1x4(k) STEP1(k, wa, wb); STEP1(k + 1, wb, wa); STEP1(k + 2, wa, wb); STEP1(k + 3, wb, wa)
  STEP1x4(0); STEP1x4(4); STEP1x4(8); STEP1x4(12); STEP1x4(16); STEP1x4(20); STEP1x4(24); STEP1x4(28);
  DMA2(10, 3);
  asm volatile("s_waitcnt vmcnt(8)" : "+v"(stat), "+v"(cs0), "+v"(cs1), "+v"(cs2) :: "memory");
  STAMP(3);
  unsigned nm2 = (stat & 0xffffu) * 0x10001u;
  bf16x8 nmf = __builtin_bit_cast(bf16x8, (u32x4){nm2, nm2, nm2, nm2});
  acc0 = MFMA(__builtin_bit_cast(bf16x8, cs0), nmf, acc0); acc1 = MFMA(__builtin_bit_cast(bf16x8, cs1), nmf, acc1); acc2 = MFMA(__builtin_bit_cast(bf16x8, cs2), nmf, acc2);
  const float sd = bfhi(stat), rstd = __builtin_amdgcn_rcpf(sd), gsc = -1.4426950408889634f * rstd, qsc = rstd * (0.25f * 1.4426950408889634f), sc0 = sj == 1 ? qsc : rstd;
  const int tile = rb * 4 + tt, hd = 8 * Q4 + (tb_ ? 0 : 4) + 2 * cg;
  const unsigned sok = WS_KV + hd * 131072 + tile * 1024, soq = WS_Q + (hd * 64 + tile) * 1024, isv = (sj + 1) & 2;
  unsigned l16k = (l16 & 0x330) | (l16 & 64) * 2 | (l16 & 128) / 2; if (sj == 1) l16k = l16; PIN(l16k);
  SBAR();
  STAMP(4);
  const int ks2 = bi + 32 * w;
  u32x4 qf0, qf1, hp, hq;
  f32x16 acc3 = {}, acc4 = {};
  auto glu2 = [&](const f32x16& a, int i) {
    const float e0_ = __builtin_amdgcn_exp2f(a[i] * gsc), e1_ = __builtin_amdgcn_exp2f(a[i + 1] * gsc);
    return cvtpa(a[8 + i] * __builtin_amdgcn_rcpf(__builtin_fmaf(e0_, sd, sd)), a[9 + i] * __builtin_amdgcn_rcpf(__builtin_fmaf(e1_, sd, sd))); };
  const unsigned mk_ = lds0 + ((w + aq) & 7) * 1024, skv = WS_KV + ah * 131072 + ((w + aq) & 7) * 1024;
#define KVD(i) dma_sc1(mk_, (i) * 8192 + ((i) >= 8 ? L_V - 65536 : 0), l16, ws, skv + (i) * 8192)
#define STEP2(ks, WC, WN) do { constexpr int c_ = 8 + (((ks) + 1) >> 3), s_ = ((ks) + 1) & 7; \
    if (s_ == 0 && c_ < 12) { SBAR(); \
      if (c_ == 9 && w == 0) { WAITV(6); PIN(pl); wait_flags(pl, vf0, ws, t0, t1); } \
      STAMP(16 + 2 * c_); if (c_ == 9) WAITBAR(6); else if (c_ == 11) LBAR(); else if (w == 0) WAITBAR(3); else WAITBAR(2); STAMP(17 + 2 * c_); } \
    if ((ks) == 12) { asm volatile("s_waitcnt vmcnt(4)\n\ts_barrier" ::: "memory"); FLAG(WS_F1); STAMP(51); } \
    if ((ks) == 21) POLL(vf0); \
    if ((ks) == 27) { \
      if (w == 0) { WAITV(0); PIN(pl); wait_flags(pl, vf0, ws, t0, t1); } \
      STAMP(52); asm volatile("s_barrier" ::: "memory"); STAMP(53); } \
    SBAR(); \
    if ((ks) < 31) RD2A(WN, ((ks) + 1) & 31); \
    if (s_ == 7) RD2B(WN, ((ks) + 1) & 31); \
    if (c_ == 9 && s_ < 4) DMA2(11, s_); \
    if ((ks) == 13) ALD4C(cs0, vc0, ws, WS_CS); if ((ks) == 14) ALD4C(cs1, vc0, ws, WS_CS + 256); \
    if ((ks) == 27) KVD(0); if ((ks) == 28) KVD(8); if ((ks) == 29) ALD4C(qf0, l16, ws, WS_Q + (ah * 64 + qown) * 1024); SBAR(); \
    acc3 = MFMA(WC[0], xf[ks], acc3); SBAR(); \
    if ((ks) < 31 && s_ != 7) RD2B(WN, ((ks) + 1) & 31); \
    if ((ks) == 27) KVD(4); if ((ks) == 28) KVD(12); if ((ks) == 29) ALD4C(qf1, l16, ws, WS_Q + (ah * 64 + (qown ^ 1)) * 1024); SBAR(); \
    acc4 = MFMA(WC[1], xf[ks], acc4); SBAR(); \
    if ((ks) < 2) { const u32x4 p_ = pack8(acc0, ((ks) & 1) * 8, sc0); if ((ks) == 0) hp = p_; else hq = p_; \
      if (isv) st16(p_, ws, l16k, sj == 1 ? soq + ((ks) & 1) * 65536 : sok + ((ks) & 1) * 131072); } \
    if ((ks) == 2 && !isv) { LANE(n_); const unsigned one_ = (n_ >> 5) == ((n_ >> 2) & 1) ? 0x3f80u << ((n_ & 1) * 16) : 0u, dw_ = ((n_ >> 1) & 1) + ((n_ >> 2) & 6); \
      u32x4 i0_, i1_; _Pragma("unroll") for (int i_ = 0; i_ < 4; ++i_) { i0_[i_] = dw_ == i_ ? one_ : 0u; i1_[i_] = dw_ == 4 + i_ ? one_ : 0u; } \
      acc0 = MFMA(__builtin_bit_cast(bf16x8, hq), __builtin_bit_cast(bf16x8, i1_), MFMA(__builtin_bit_cast(bf16x8, hp), __builtin_bit_cast(bf16x8, i0_), (f32x16){})); } \
    if ((ks) == 3 && !isv) { LANE(n_); const unsigned vvo = (n_ & 16) * 8192 + ((n_ >> 5) * 16 + (n_ & 15)) * 16; \
      st16((u32x4){cvtpk(acc0[0], acc0[1]), cvtpk(acc0[2], acc0[3]), cvtpk(acc0[4], acc0[5]), cvtpk(acc0[6], acc0[7])}, ws, vvo, sok + 65536); \
      st16((u32x4){cvtpk(acc0[8], acc0[9]), cvtpk(acc0[10], acc0[11]), cvtpk(acc0[12], acc0[13]), cvtpk(acc0[14], acc0[15])}, ws, vvo, sok + 65536 + 512); } \
    if ((ks) >= 4 && (ks) <= 6 && tb_) st16(pack8((ks) < 6 ? acc1 : acc2, ((ks) & 1) * 8, (ks) < 6 ? rstd : qsc), ws, (ks) < 6 ? l16k : l16, (ks) < 6 ? sok + ((ks) & 1) * 131072 : soq); \
    if ((ks) == 6 && tb_) st16(pack8(acc2, 8, qsc), ws, l16, soq + 65536); \
    if ((ks) == 7) { LANE(n_); vc0 = ((n_ >> 5) * 5120 + (n_ & 16) * 112 + 1536 + 64 * p2 + 32 * cg + (n_ & 15)) * 16; PIN(vc0); } \
    if ((ks) == 9) { LANE(n_); const int odd = xcd & 1, j_ = n_ >> 4; \
      vf0 = WS_F1 + ((j_ < 2 ? n_ & 15 : 2 * aq + (n_ & 1)) * 16 + Q4 * 4 + odd * (j_ < 2 ? 2 + j_ : 1)) * FS; PIN(vf0); } \
    if ((ks) == 15 && ks2 < 144) { const rsrc_t r_ = mk(ks2 < 112 ? pwd : pwo, ks2 < 112 ? 1792 * 1024 : 512 * 1024); const unsigned so_ = (ks2 < 112 ? ks2 : ks2 - 112) * 16384 + xcd * 128; LANE(n_); const unsigned v_ = (n_ >> 3) * 1024 + ((n_ & 7) ^ ((n_ >> 2) & 4)) * 16; \
      dma(lds0 + w * 2048, L_PRIV, v_, r_, so_); dma(lds0 + w * 2048, L_PRIV + 1024, v_, r_, so_ + 8192); } \
    if ((ks) >= 4 && (ks) <= 20 && !((ks) & 1) && !tb_) { \
      if ((ks) == 16) st16(hp, ws, l16, WS_H + (tile * 112 + 4 * p1 + 2 * cg) * 1024); \
      if ((ks) == 20) st16(hq, ws, l16, WS_H + (tile * 112 + 4 * p1 + 2 * cg + 1) * 1024); \
      else if ((ks) < 12) { hp[(((ks) >> 1) + 2) & 3] = glu2(acc1, ((ks) + 4) & 6); PIN(hp); } \
      else { hq[(((ks) >> 1) + 2) & 3] = glu2(acc2, ((ks) + 4) & 6); PIN(hq); } } SBAR(); } while (0)
#define STEP2x4(k) STEP2(k, wa, wb); STEP2(k + 1, wb, wa); STEP2(k + 2, wa, wb); STEP2(k + 3, wb, wa)
  STEP2x4(0); STEP2x4(4); STEP2x4(8); STEP2x4(12); STEP2x4(16); STEP2x4(20); STEP2x4(24); STEP2x4(28);
  asm volatile("s_waitcnt vmcnt(6)" : "+v"(cs0), "+v"(cs1) :: "memory");
  STAMP(5);
  PIN(nm2); nmf = __builtin_bit_cast(bf16x8, (u32x4){nm2, nm2, nm2, nm2});
  acc3 = MFMA(__builtin_bit_cast(bf16x8, cs0), nmf, acc3); acc4 = MFMA(__builtin_bit_cast(bf16x8, cs1), nmf, acc4);
  {
    LANE(n_); const lptr s2_ = lds + L_PRIV + w * 2048 + (4 * (n_ >> 5) + ((n_ >> 2) & 3)) * 128 + (n_ & 1) * 8; const int pc_ = (((n_ >> 3) & 2) + ((n_ >> 1) & 1)) ^ ((n_ >> 1) & 4);
    hp = __builtin_bit_cast(u32x4, trfrag(s2_ + pc_ * 16, 1024)); hq = __builtin_bit_cast(u32x4, trfrag(s2_ + (pc_ ^ 4) * 16, 1024)); }
  st16(glu8(acc3, gsc, sd), ws, l16, WS_H + (tile * 112 + 4 * p2 + 2 * cg) * 1024);
  LANE(ln); const int r3 = ln & 31, hf = ln >> 5;
  const int tb = xcd * 4 + (bi & 3), n64 = bi >> 2;
  lptr kp = lds + kh * 32768 + l16, kq = lds + (kh ^ 1) * 32768 + l16;
  const bool vrow = r3 < 16;
  int vstep = vrow ? 4096 : 0;
  lptr vp = lds + (vrow ? L_V + kh * 32768 + hf * 256 : L_ONES - 256) + r3 * 16;
  int idx = 0xDDDDDDDD;
  float aone = hf ? 0.f : 1.f;
  PIN(kp); PIN(kq); PIN(vstep); PIN(vp); PIN(idx); PIN(aone);
  f32x16 nm0, nm1, o0, o1, sA, sB;
  const f32x16 z = {};
  { u32x4 zz = {}; PIN(zz); o0 = MFMA(__builtin_bit_cast(bf16x8, zz), __builtin_bit_cast(bf16x8, zz), z); PIN(o0); o1 = MFMA(__builtin_bit_cast(bf16x8, zz), __builtin_bit_cast(bf16x8, zz), o0); PIN(o1); }
  STAMP(6);
  WAITBAR(1);
  PIN(qf0); PIN(qf1);
  STAMP(7);
  const bf16x8 q0 = __builtin_bit_cast(bf16x8, qf0), q1 = __builtin_bit_cast(bf16x8, qf1);
  auto hmax = [&](float m) { auto rr = __builtin_amdgcn_permlane32_swap(__float_as_uint(m), __float_as_uint(m), false, false);
    return __builtin_fmaxf(__uint_as_float(rr[0]), __uint_as_float(rr[1])); };
  auto max16 = [&](float m, const f32x16& s) {
#pragma unroll
    for (int i = 0; i < 16; i += 2) m = __builtin_fmaxf(__builtin_fmaxf(m, s[i]), s[i + 1]);
    return m; };
  __builtin_amdgcn_s_setprio(1);
  bf16x8 k1 = LDS8(kp), va, vb;
  {
    const bf16x8 k0 = LDS8(kq);
    va = LDS8(vp); SBAR();
    if (ks2 < 144) { st16(hp, ws, l16, WS_W2 + (2 * xcd * 144 + ks2) * 1024); st16(hq, ws, l16, WS_W2 + ((2 * xcd + 1) * 144 + ks2) * 1024); }
    hp[0] = glu2(acc4, 0); PIN(hp); SBAR();
    const f32x16 a2 = MFMA(k1, q0, z), a0 = MFMA(k0, q0, z); SBAR();
    hp[1] = glu2(acc4, 2); PIN(hp); SBAR();
    const f32x16 a3 = MFMA(k1, q1, z), a1 = MFMA(k0, q1, z); SBAR();
    float x0 = a0[0], x2 = a2[0];
#pragma unroll
    for (int i = 1; i < 15; i += 2) { x2 = max3(x2, a2[i], a2[i + 1]); x0 = max3(x0, a0[i], a0[i + 1]); }
    nm0 = __builtin_amdgcn_mfma_f32_32x32x2f32(aone, -REF_OFF - hmax(max3(max3(x0, x2, a0[15]), a2[15], a2[15])), z, 0, 0, 0); SBAR();
    float x1 = a1[0], x3 = a3[0];
#pragma unroll
    for (int i = 1; i < 15; i += 2) { x3 = max3(x3, a3[i], a3[i + 1]); x1 = max3(x1, a1[i], a1[i + 1]); }
    float m1 = -REF_OFF - hmax(max3(max3(x1, x3, a1[15]), a3[15], a3[15])); PIN(m1); SBAR();
    sA = MFMA(k1, q0, nm0); SBAR();
    hp[2] = glu2(acc4, 4); PIN(hp); SBAR();
    nm1 = __builtin_amdgcn_mfma_f32_32x32x2f32(aone, m1, z, 0, 0, 0); SBAR();
    hp[3] = glu2(acc4, 6); PIN(hp);
    st16(hp, ws, l16, WS_H + (tile * 112 + 4 * p2 + 2 * cg + 1) * 1024); SBAR();
  }
  f32x8 pa, pb;
  f32x16 g00, g01, g10, g11;
  bf16x8 fa[4], fb[4];
  unsigned sow = WS_W2 + (2 * n64 * 144 + 14 * w) * 1024, soh = WS_H + (2 * tb * 112 + 14 * w) * 1024;
#define EX(P, S, i) P[(i) & 7] = __builtin_amdgcn_exp2f(S[i])
#define T2(u, A0, A1, B0, B1) T2X(o1 = SMF(vb, pb, o1, idx), u, A0, A1, B0, B1)
#define T2X(FIN, u, A0, A1, B0, B1) do { \
    EX(pa, sA, 0); EX(pa, sA, 1); SBAR(); FIN; SBAR(); vb = LDS8(vp + (u) * 1024 + 512); SBAR(); \
    EX(pa, sA, 2); EX(pa, sA, 3); EX(pa, sA, 4); EX(pa, sA, 5); SBAR(); sB = MFMA(k1, q1, nm1); SBAR(); k1 = LDS8(kp + ((u) + 1) * 1024); SBAR(); \
    EX(pa, sA, 6); EX(pa, sA, 7); EX(pb, sA, 8); EX(pb, sA, 9); SBAR(); o0 = SMF(va, pa, o0, idx); SBAR(); \
    EX(pb, sA, 10); EX(pb, sA, 11); EX(pb, sA, 12); EX(pb, sA, 13); SBAR(); A0; A1; SBAR(); EX(pb, sA, 14); EX(pb, sA, 15); SBAR(); \
    EX(pa, sB, 0); EX(pa, sB, 1); SBAR(); o0 = SMF(vb, pb, o0, idx); SBAR(); \
    EX(pa, sB, 2); EX(pa, sB, 3); EX(pa, sB, 4); EX(pa, sB, 5); SBAR(); sA = MFMA(k1, q0, nm0); SBAR(); \
    EX(pa, sB, 6); EX(pa, sB, 7); EX(pb, sB, 8); EX(pb, sB, 9); SBAR(); o1 = SMF(va, pa, o1, idx); SBAR(); va = LDS8(vp + ((u) + 1) * 1024); SBAR(); \
    EX(pb, sB, 10); EX(pb, sB, 11); EX(pb, sB, 12); EX(pb, sB, 13); SBAR(); B0; B1; SBAR(); EX(pb, sB, 14); EX(pb, sB, 15); SBAR(); } while (0)
#define NOP_ do {} while (0)
#define ADV() do { kp += 4096; vp += vstep; } while (0)
#define G2P(u, C, N) do { \
    T2(u, g00 = MFMA(C[0], C[2], g00), (N[0] = ld16c(ws, l16, sow), N[2] = ld16c(ws, l16, soh)), g10 = MFMA(C[1], C[2], g10), N[1] = ld16c(ws, l16, sow + 144 * 1024)); \
    T2(u + 1, g11 = MFMA(C[1], C[3], g11), N[3] = ld16c(ws, l16, soh + 112 * 1024), g01 = MFMA(C[0], C[3], g01), NOP_); \
    sow += 1024; soh += 1024; } while (0)
#define PRIO_AT(k, n) asm volatile("s_cmp_lg_u32 %0, " #k "\n\ts_cbranch_scc1 1f\n\ts_setprio " #n "\n1:" :: "s"(it) : "scc")
#define PRIO(n) __builtin_amdgcn_s_setprio(n)
  T2X(NOP_, 0, KVD(1), KVD(5), KVD(9), NOP_); PRIO(0); T2(1, KVD(13), KVD(2), KVD(6), NOP_);
  STAMP(41);
  asm volatile("s_waitcnt vmcnt(6)\n\ts_barrier" ::: "memory");
  STAMP(42);
  FLAG(WS_F2);
  if (w == 0 && b == 0) st8((u32x2){e0 + 1, e1}, ws, l16 ? 0x7fffff00u : 0u, WS_EPOCH);
  const unsigned vf2 = WS_F2 + (ln < 16 ? (tb >> 1) * 16 + ln : (((n64 & 1) * 8 + (ln & 7)) * 16 + (n64 >> 1) * 4 + ((ln >> 3) & 3))) * FS;
  PRIO(2); T2(2, KVD(10), KVD(14), KVD(3), NOP_); T2(3, KVD(7), KVD(11), KVD(15), NOP_); ADV();
  PRIO(1); T2(0, NOP_, NOP_, POLL(vf2), NOP_); PRIO(0); T2(1, NOP_, NOP_, NOP_, NOP_);
  STAMP(43);
  if (w == 0) { WAITV(0); PIN(pl); wait_flags(pl, vf2, ws, t0, t1); }
  STAMP(44);
  asm volatile("s_waitcnt vmcnt(0)\n\ts_barrier" ::: "memory");
  STAMP(8);
  const unsigned vout = (tb * 64 + ((w >> 1) & 1) * 32 + r3) * 1024 + n64 * 128 + (w >> 2) * 64 + (w & 1) * 32 + hf * 16;
  u32x4 xres;
  PRIO(3);
  fb[0] = ld16c(ws, l16, sow); fb[2] = ld16c(ws, l16, soh); fb[1] = ld16c(ws, l16, sow + 144 * 1024); fb[3] = ld16c(ws, l16, soh + 112 * 1024); SBAR();
  T2(2, fa[0] = ld16c(ws, l16, sow + 1024), fa[2] = ld16c(ws, l16, soh + 1024), fa[1] = ld16c(ws, l16, sow + 145 * 1024), fa[3] = ld16c(ws, l16, soh + 113 * 1024));
  {
    T2(3, g00 = MFMA(fb[0], fb[2], z), g10 = MFMA(fb[1], fb[2], z), g11 = MFMA(fb[1], fb[3], z), g01 = MFMA(fb[0], fb[3], z)); ADV(); }
  xres = __builtin_amdgcn_raw_buffer_load_b128(rx, vout, 0, 0);
  if (w < 2) {
    const unsigned v = ln * 128;
    asm volatile("s_mov_b32 m0, %0\n\ts_nop 0\n\tbuffer_load_dword %1, %2, %3 offen sc1 lds" :: "s"(lds0 + LDS_BYTES - 256), "v"(v), "s"(ws), "s"(WS_W2 + ((2 * n64 + w) * 144 + 112 + 8 * (bi & 3)) * 1024) : "memory"); }
  sow += 2048; soh += 2048;
#pragma unroll 1
  for (int it = 0; it < 5; ++it) { PRIO_AT(3, 2); G2P(0, fa, fb); G2P(2, fb, fa); ADV(); }
  const int hg = (w + n64) & 7;
  const unsigned swo = WS_W2 + (2 * n64 * 144 + 112 + 4 * hg) * 1024, lwo = (w < 3 ? w : w < 6 ? w + 1 : w + 3) * 8192;
#define WOD(i) dma_sc1(lds0 + lwo, (i) * 1024, l16, ws, swo + ((i) & 3) * 1024 + ((i) >> 2) * (144 * 1024))
  asm volatile("s_barrier" ::: "memory");
  PRIO(2);
  T2(0, g00 = MFMA(fa[0], fa[2], g00), (fb[0] = ld16c(ws, l16, sow), fb[2] = ld16c(ws, l16, soh)), g10 = MFMA(fa[1], fa[2], g10), (fb[1] = ld16c(ws, l16, sow + 144 * 1024), fb[3] = ld16c(ws, l16, soh + 112 * 1024)));
  T2(1, g11 = MFMA(fa[1], fa[3], g11), { SBAR(); WOD(0); WOD(1); WOD(2); WOD(3); }, g01 = MFMA(fa[0], fa[3], g01), { WOD(4); WOD(5); WOD(6); WOD(7); });
  PRIO(1);
  T2(2, g00 = MFMA(fb[0], fb[2], g00), (fa[0] = ld16c(ws, l16, sow + 1024), fa[2] = ld16c(ws, l16, soh + 1024)), g10 = MFMA(fb[1], fb[2], g10), (fa[1] = ld16c(ws, l16, sow + 145 * 1024), fa[3] = ld16c(ws, l16, soh + 113 * 1024)));
  PRIO(0);
  T2(3, g11 = MFMA(fb[1], fb[3], g11), NOP_, g01 = MFMA(fb[0], fb[3], g01), NOP_);
  o1 = SMF(vb, pb, o1, idx);
  STAMP(9);
  typedef __attribute__((address_space(3))) f32x4* l4p;
  { const lptr d = lds + L_PRIV + w * 2048 + l16;
    *(l4p)(d) = (f32x4){o1[0], o1[1], o1[2], o1[3]}; *(l4p)(d + 1024) = (f32x4){o1[4], o1[5], o1[6], o1[7]}; }
  { *(__attribute__((address_space(3))) float*)(lds + 65536 + w * 256 + ln * 4) = o1[8]; }
  LBAR();
  {
    const lptr s = lds + L_PRIV + (w ^ 4) * 2048 + l16;
    const f32x4 a = *(l4p)(s), c = *(l4p)(s + 1024);
#pragma unroll
    for (int i = 0; i < 4; ++i) { o0[i] += a[i]; o0[4 + i] += c[i]; }
    o0[8] += *(__attribute__((address_space(3))) float*)(lds + 65536 + (w ^ 4) * 256 + ln * 4);
    float am = 0.f, sm = 0.f;
#pragma unroll
    for (int i = 0; i < 8; ++i) { am = __builtin_fmaxf(am, __builtin_fabsf(o0[i])); sm += __builtin_fabsf(o0[i]); }
    const bool ok = !FORCE_FB && o0[8] < 0x1p120f && o0[8] > 0x1p-100f && sm < INFINITY && am > 0x1p-104f;
    if (__builtin_expect(!__all(ok), 0)) {
      const unsigned sk = WS_KV + ah * 131072, vv = vrow ? (hf * 16 + r3) * 16 : 0x7fffff00u, one = (r3 & 27) == 16 ? 0x3f803f80u : 0u;
      float m = -INFINITY;
#pragma unroll 1
      for (int t = 0; t < 64; ++t) m = max16(m, MFMA(ld16c(ws, l16, sk + t * 1024), q0, z));
      m = -hmax(m);
#pragma unroll
      for (int i = 0; i < 16; ++i) nm0[i] = m;
      o0 = z;
#pragma unroll 1
      for (int t = 0; t < 64; ++t) {
        const f32x16 s_ = MFMA(ld16c(ws, l16, sk + t * 1024), q0, nm0);
        const u32x4 v0 = __builtin_bit_cast(u32x4, ld16c(ws, vv, sk + 65536 + t * 1024)) | one, v1 = __builtin_bit_cast(u32x4, ld16c(ws, vv, sk + 65536 + t * 1024 + 512)) | one;
#pragma unroll
        for (int i = 0; i < 8; ++i) { pa[i] = __builtin_amdgcn_exp2f(s_[i]); pb[i] = __builtin_amdgcn_exp2f(s_[8 + i]); }
        o0 = SMF(__builtin_bit_cast(bf16x8, v0), pa, o0, idx); o0 = SMF(__builtin_bit_cast(bf16x8, v1), pb, o0, idx);
      }
    }
  }
  PIN(fa[0]); PIN(fa[1]); PIN(fa[2]); PIN(fa[3]);
  WAITV(0);
  { u32x4 ov = pack8(o0, 0, __builtin_amdgcn_rcpf(o0[8]));
#pragma unroll
    for (int i = 0; i < 4; ++i) ov[i] = ov[i] < 0xfffffffeu ? ov[i] : 0xfffffffeu;
    st16(ov, ws, l16, WS_O + (qown * 32 + ah) * 1024); }
  STAMP(45);
  {
    const unsigned soo = WS_O + (2 * tb * 32 + 4 * hg) * 1024; unsigned spins;
#define OL0 "buffer_load_dwordx4 v[0:3], %[l], %[r], %[o0] offen sc1\n\t"
#define OL1 "buffer_load_dwordx4 v[4:7], %[l], %[r], %[o1] offen sc1\n\t"
#define OL2 "buffer_load_dwordx4 v[8:11], %[l], %[r], %[o0] offen offset:1024 sc1\n\t"
#define OL3 "buffer_load_dwordx4 v[12:15], %[l], %[r], %[o1] offen offset:1024 sc1\n\t"
#define OL4 "buffer_load_dwordx4 v[16:19], %[l], %[r], %[o0] offen offset:2048 sc1\n\t"
#define OL5 "buffer_load_dwordx4 v[20:23], %[l], %[r], %[o1] offen offset:2048 sc1\n\t"
#define OL6 "buffer_load_dwordx4 v[24:27], %[l], %[r], %[o0] offen offset:3072 sc1\n\t"
#define OL7 "buffer_load_dwordx4 v[28:31], %[l], %[r], %[o1] offen offset:3072 sc1\n\t"
#define OCHK(r0, r1, r2, r3) "v_max3_u32 v80, v" #r0 ", v" #r1 ", v" #r2 "\n\tv_max_u32 v80, v80, v" #r3 "\n\tv_cmp_eq_u32 vcc, -1, v80\n\t"
#define OSTEP(n, r0, r1, r2, r3, ga, gb, wa_, wb_) \
    "s_getpc_b64 %[ret]\n\ts_waitcnt vmcnt(" #n ")\n\t" OCHK(r0, r1, r2, r3) "s_cbranch_vccnz 3f\n\t" \
    "v_mfma_f32_32x32x16_bf16 " ga ", " wa_ ", v[" #r0 ":" #r3 "], " ga "\n\tv_mfma_f32_32x32x16_bf16 " gb ", " wb_ ", v[" #r0 ":" #r3 "], " gb "\n\t"
#define ORE(r0, r1, r2, r3, load) OCHK(r0, r1, r2, r3) "s_cbranch_vccz 4f\n\t" load "4:\n\t"
    unsigned long long ret;
        const lptr lw = lds + lwo + l16;
    asm volatile("ds_read_b128 v[48:51], %[lw]\n\tds_read_b128 v[52:55], %[lw] offset:1024\n\tds_read_b128 v[56:59], %[lw] offset:2048\n\tds_read_b128 v[60:63], %[lw] offset:3072\n\t"
                 "ds_read_b128 v[64:67], %[lw] offset:4096\n\tds_read_b128 v[68:71], %[lw] offset:5120\n\tds_read_b128 v[72:75], %[lw] offset:6144\n\tds_read_b128 v[76:79], %[lw] offset:7168\n\t"
                 "s_mov_b32 %[sp], 0\n\ts_waitcnt lgkmcnt(0)\n\ts_barrier\n\t" OL0 OL1 OL2 OL3 OL4 OL5 OL6 OL7
                 "v_mfma_f32_32x32x16_bf16 %[g00], %[f0], %[f2], %[g00]\n\tv_mfma_f32_32x32x16_bf16 %[g10], %[f1], %[f2], %[g10]\n\t"
                 "v_mfma_f32_32x32x16_bf16 %[g01], %[f0], %[f3], %[g01]\n\tv_mfma_f32_32x32x16_bf16 %[g11], %[f1], %[f3], %[g11]\n\t"
                 OSTEP(7, 0, 1, 2, 3, "%[g00]", "%[g10]", "v[48:51]", "v[64:67]") OSTEP(6, 4, 5, 6, 7, "%[g01]", "%[g11]", "v[48:51]", "v[64:67]")
                 OSTEP(5, 8, 9, 10, 11, "%[g00]", "%[g10]", "v[52:55]", "v[68:71]") OSTEP(4, 12, 13, 14, 15, "%[g01]", "%[g11]", "v[52:55]", "v[68:71]")
                 OSTEP(3, 16, 17, 18, 19, "%[g00]", "%[g10]", "v[56:59]", "v[72:75]") OSTEP(2, 20, 21, 22, 23, "%[g01]", "%[g11]", "v[56:59]", "v[72:75]")
                 OSTEP(1, 24, 25, 26, 27, "%[g00]", "%[g10]", "v[60:63]", "v[76:79]") OSTEP(0, 28, 29, 30, 31, "%[g01]", "%[g11]", "v[60:63]", "v[76:79]")
                 "s_branch 2f\n"
                 "3:\n\ts_add_u32 %[sp], %[sp], 1\n\ts_cmp_gt_u32 %[sp], %[cap]\n\ts_cbranch_scc1 2f\n\ts_waitcnt vmcnt(0)\n\t"
                 ORE(0, 1, 2, 3, OL0) ORE(4, 5, 6, 7, OL1) ORE(8, 9, 10, 11, OL2) ORE(12, 13, 14, 15, OL3)
                 ORE(16, 17, 18, 19, OL4) ORE(20, 21, 22, 23, OL5) ORE(24, 25, 26, 27, OL6) ORE(28, 29, 30, 31, OL7)
                 "s_waitcnt vmcnt(0)\n\ts_setpc_b64 %[ret]\n"
                 "2:\n\ts_waitcnt vmcnt(0) lgkmcnt(0)\n\ts_nop 15"
                 : [g00] "+v"(g00), [g01] "+v"(g01), [g10] "+v"(g10), [g11] "+v"(g11), [sp] "=&s"(spins), [ret] "=&s"(ret)
                 : [l] "v"(l16), [r] "s"(ws), [lw] "v"(lw), [f0] "v"(fa[0]), [f1] "v"(fa[1]), [f2] "v"(fa[2]), [f3] "v"(fa[3]), [o0] "s"(soo), [o1] "s"(soo + 32768), [cap] "i"(SPIN_CAP)
                 : "memory", "scc", "vcc", "v0", "v1", "v2", "v3", "v4", "v5", "v6", "v7", "v8", "v9", "v10", "v11", "v12", "v13", "v14", "v15", "v16", "v17", "v18", "v19",
                   "v20", "v21", "v22", "v23", "v24", "v25", "v26", "v27", "v28", "v29", "v30", "v31",
                   "v48", "v49", "v50", "v51", "v52", "v53", "v54", "v55", "v56", "v57", "v58", "v59",
                   "v60", "v61", "v62", "v63", "v64", "v65", "v66", "v67", "v68", "v69", "v70", "v71", "v72", "v73", "v74", "v75", "v76", "v77", "v78", "v79", "v80");
  }
  STAMP(12);
  { const lptr d = lds + w * 16384 + l16;
#pragma unroll
    for (int i = 0; i < 4; ++i) {
      *(l4p)(d + i * 1024) = (f32x4){g00[4 * i], g00[4 * i + 1], g00[4 * i + 2], g00[4 * i + 3]};
      *(l4p)(d + (4 + i) * 1024) = (f32x4){g01[4 * i], g01[4 * i + 1], g01[4 * i + 2], g01[4 * i + 3]};
      *(l4p)(d + (8 + i) * 1024) = (f32x4){g10[4 * i], g10[4 * i + 1], g10[4 * i + 2], g10[4 * i + 3]};
      *(l4p)(d + (12 + i) * 1024) = (f32x4){g11[4 * i], g11[4 * i + 1], g11[4 * i + 2], g11[4 * i + 3]}; } }
  lptr rs0 = lds + (2 * w + hf) * 1024 + r3 * 16, rs1 = rs0 + 65536; PIN(rs0); PIN(rs1);
  f32x4 ra = {bflo(xres[0]), bfhi(xres[0]), bflo(xres[1]), bfhi(xres[1])}, rc = {bflo(xres[2]), bfhi(xres[2]), bflo(xres[3]), bfhi(xres[3])}; PIN(ra); PIN(rc);
  LBAR();
  STAMP(13);
  {
    f32x4 pa_[8], pc_[8];
#pragma unroll
    for (int i = 0; i < 8; ++i) { pa_[i] = *(l4p)((i < 4 ? rs0 : rs1) + (i & 3) * 16384); pc_[i] = *(l4p)((i < 4 ? rs0 : rs1) + (i & 3) * 16384 + 512); }
    SBAR();
#pragma unroll
    for (int i = 0; i < 8; ++i) { ra += pa_[i]; rc += pc_[i]; PIN(ra); PIN(rc); }
    const u32x4 r = {cvtpk(ra[0], ra[1]), cvtpk(ra[2], ra[3]), cvtpk(rc[0], rc[1]), cvtpk(rc[2], rc[3])};
    __builtin_amdgcn_raw_buffer_store_b128(r, mk(pout, 2048 * 1024), vout, 0, 0);
  }
  STAMP(14);
  STAMP_END();
}

__global__ __launch_bounds__(512, 2) void nullk(unsigned* p) { if (p == nullptr) p[threadIdx.x] = 1; }
__global__ __launch_bounds__(512, 2) void nullbig(unsigned* p) { asm volatile("v_mov_b32 v230, 0" ::: "v230"); if (p == nullptr) p[threadIdx.x] = 1; }
extern "C" void kernel_launch(void* const* d_in, const int* in_sizes, int n_in, void* d_out, int out_size, void* d_ws, size_t ws_size, hipStream_t stream) {
  (void)hipFuncSetAttribute((const void*)fused_block, hipFuncAttributeMaxDynamicSharedMemorySize, LDS_BYTES);
  hipLaunchKernelGGL(fused_block, dim3(256), dim3(512), LDS_BYTES, stream, d_in[0], d_in[1], d_in[2], d_in[4], d_in[5], d_in[6], d_out, d_ws);
  (void)hipFuncSetAttribute((const void*)nullbig, hipFuncAttributeMaxDynamicSharedMemorySize, LDS_BYTES);
  hipLaunchKernelGGL(nullbig, dim3(256), dim3(512), LDS_BYTES, stream, (unsigned*)d_ws);
}
```

```cpp
#include <hip/hip_runtime.h>
#include <hip/hip_bf16.h>

typedef __bf16 bf16x8 __attribute__((ext_vector_type(8)));
typedef __bf16 bf16x16 __attribute__((ext_vector_type(16)));
typedef __bf16 bf16x2 __attribute__((ext_vector_type(2)));
typedef short s16x4 __attribute__((ext_vector_type(4)));
typedef float f32x16 __attribute__((ext_vector_type(16)));
typedef float f32x8 __attribute__((ext_vector_type(8)));
typedef float f32x4 __attribute__((ext_vector_type(4)));
typedef float f32x2 __attribute__((ext_vector_type(2)));
typedef unsigned u32x4 __attribute__((ext_vector_type(4)));
typedef unsigned u32x2 __attribute__((ext_vector_type(2)));
typedef unsigned u32x16 __attribute__((ext_vector_type(16)));
typedef __amdgpu_buffer_rsrc_t rsrc_t;
typedef __attribute__((address_space(3))) char* lptr;
#define DBG_STAMPS 0
#define DI __device__ __forceinline__
#define SBAR() __builtin_amdgcn_sched_barrier(0)
#define MFMA(a, b, c) __builtin_amdgcn_mfma_f32_32x32x16_bf16(a, b, c, 0, 0, 0)
#define SMF(a, b, c, idx) __builtin_amdgcn_smfmac_f32_32x32x32_bf16(a, __builtin_bit_cast(bf16x16, b), c, idx, 0, 0)
#define LDS8(p) (*(const __attribute__((address_space(3))) bf16x8*)(p))
#define PIN(x) asm volatile("" : "+v"(x))
#define LANE(n) unsigned n = l16; PIN(n); n >>= 4

#define X_OFF 100.f
#define X_EARLY 3
#define X_NT " nt"
#define X_W2AT 5
#define X_XR 1
#define X_EXAT 0
#define X_OT 0
#define X_PR 1
#define FORCE_FB 0

constexpr unsigned WS_EPOCH = 0;
constexpr unsigned FS = 128;
constexpr unsigned WS_F0 = 262144, WS_F1 = WS_F0 + 256 * FS, WS_F2 = WS_F1 + 256 * FS, WS_F3 = WS_F2 + 256 * FS;
constexpr unsigned WS_STAT = 32768;
constexpr unsigned WS_CS = 65536;
constexpr unsigned WS_Q = 1u << 20;
constexpr unsigned WS_KV = WS_Q + (2u << 20);
constexpr unsigned WS_O = WS_KV + (4u << 20);
constexpr unsigned WS_W2 = WS_O + (2u << 20);
constexpr unsigned WS_H = WS_W2 + (3u << 20);
constexpr unsigned WS_DBG = WS_H + (7u << 20);
constexpr unsigned WS_END = WS_DBG + (1u << 20);
constexpr int U_XA = 0, U_G1 = 24576, U_XB = 49152, U_G2 = 73728, U_G0 = 98304, UD = 49152;
constexpr int L_XCS = 122880, L_V = 73728, L_PRIV = 139264, L_ONES = 155648, L_PAIR = 90112, LDS_BYTES = 163840;
constexpr unsigned SPIN_CAP = 1u << 15, CODE_PF = 26624;
constexpr float REF_OFF = X_OFF;

DI rsrc_t mk(const void* p, unsigned bytes) { return __builtin_amdgcn_make_buffer_rsrc((void*)p, (short)0, (int)bytes, 0x00020000); }
DI unsigned cvtpk(float lo, float hi) { f32x2 v = {lo, hi}; return __builtin_bit_cast(unsigned, __builtin_convertvector(v, bf16x2)); }
DI unsigned cvtpa(float lo, float hi) { unsigned r; asm("v_cvt_pk_bf16_f32 %0, %1, %2" : "=v"(r) : "v"(lo), "v"(hi)); return r; }
DI float bflo(unsigned u) { return __uint_as_float(u << 16); }
DI float bfhi(unsigned u) { return __uint_as_float(u & 0xffff0000u); }
DI float rbf(float f) { return bflo(cvtpk(f, f)); }
DI float max3(float a, float b, float c) { return __builtin_fmaxf(__builtin_fmaxf(a, b), c); }
DI s16x4 trd(lptr p) { return __builtin_amdgcn_ds_read_tr16_b64_v4i16((__attribute__((address_space(3))) s16x4*)p); }
DI bf16x8 trfrag(lptr p, int step) { s16x4 a = trd(p), b = trd(p + step); return __builtin_bit_cast(bf16x8, __builtin_shufflevector(a, b, 0, 1, 2, 3, 4, 5, 6, 7)); }
#define dma(base, imm, voff, r, soff) asm volatile("s_add_i32 m0, %0, %4\n\ts_nop 0\n\tbuffer_load_dwordx4 %1, %2, %3 offen lds" :: "s"(base), "v"(voff), "s"(r), "s"(soff), "i"(imm) : "memory", "scc")
#define dma_nt(base, imm, voff, r, soff) asm volatile("s_add_i32 m0, %0, %4\n\ts_nop 0\n\tbuffer_load_dwordx4 %1, %2, %3 offen" X_NT " lds" :: "s"(base), "v"(voff), "s"(r), "s"(soff), "i"(imm) : "memory", "scc")
#define dma_sc1(base, imm, voff, r, soff) asm volatile("s_add_i32 m0, %0, %4\n\ts_nop 0\n\tbuffer_load_dwordx4 %1, %2, %3 offen sc1 lds" :: "s"(base), "v"(voff), "s"(r), "s"(soff), "i"(imm) : "memory", "scc")
#define ALD4(dst, voff, r, soff) asm volatile("buffer_load_dwordx4 %0, %1, %2, %3 offen" : "=v"(dst) : "v"(voff), "s"(r), "s"(soff) : "memory")
#define ALD4C(dst, voff, r, soff) asm volatile("buffer_load_dwordx4 %0, %1, %2, %3 offen sc1" : "=v"(dst) : "v"(voff), "s"(r), "s"(soff) : "memory")
#define ALD2C(dst, voff, r, soff) asm volatile("buffer_load_dwordx2 %0, %1, %2, %3 offen sc1" : "=v"(dst) : "v"(voff), "s"(r), "s"(soff) : "memory")
#define ALD1C(dst, voff, r, soff) asm volatile("buffer_load_dword %0, %1, %2, %3 offen sc1" : "=v"(dst) : "v"(voff), "s"(r), "s"(soff) : "memory")
#define CLD(op, dst, voff, r, soff, lim) asm volatile("s_cmp_ge_u32 %4, %5\n\ts_cbranch_scc1 1f\n\t" op "\n1:" : "=v"(dst) : "v"(voff), "s"(r), "s"(soff), "s"(w), "i"(lim) : "memory", "scc")
#define POLL(voff) CLD("buffer_load_dwordx2 %0, %1, %2, %3 offen sc1", pl, voff, ws, 0, 1)
#define WAITV(n) asm volatile("s_waitcnt vmcnt(%0)" :: "i"(n) : "memory")
#define WAITBAR(n) asm volatile("s_waitcnt vmcnt(%0) lgkmcnt(0)\n\ts_barrier" :: "i"(n) : "memory")
#define LBAR() asm volatile("s_waitcnt lgkmcnt(0)\n\ts_barrier" ::: "memory")
DI void st16(u32x4 v, rsrc_t r, unsigned voff, unsigned soff) {
  asm volatile("buffer_store_dwordx4 %0, %1, %2, %3 offen sc1\n\ts_nop 1" :: "v"(v), "v"(voff), "s"(r), "s"(soff) : "memory"); }
DI void st8(u32x2 v, rsrc_t r, unsigned voff, unsigned soff) { __builtin_amdgcn_raw_buffer_store_b64(v, r, voff, soff, 16); }
DI void st4(unsigned v, rsrc_t r, unsigned voff, unsigned soff) { __builtin_amdgcn_raw_buffer_store_b32(v, r, voff, soff, 16); }
DI bf16x8 ld16c(rsrc_t r, unsigned voff, unsigned soff) { return __builtin_bit_cast(bf16x8, __builtin_amdgcn_raw_buffer_load_b128(r, voff, soff, 16)); }
template <int CTRL> DI float dppadd(float v) { return v + __int_as_float(__builtin_amdgcn_update_dpp(0, __float_as_int(v), CTRL, 0xf, 0xf, true)); }
DI void wait_flags(u32x2 f, unsigned voff, rsrc_t ws, unsigned t0, unsigned t1) {
#pragma unroll 1
  for (unsigned spins = 0; spins < SPIN_CAP; ++spins) {
    if (__all(f[0] == t0 && f[1] == t1)) break;
    __builtin_amdgcn_s_sleep(2);
    asm volatile("buffer_load_dwordx2 %0, %1, %2, 0 offen sc1\n\ts_waitcnt vmcnt(0)" : "=&v"(f) : "v"(voff), "s"(ws) : "memory");
  }
}
#define STAMP(i)
#define STAMP_END()
#define STAMPV(i)

DI u32x4 pack8(const f32x16& a, int o, float s) {
  return (u32x4){cvtpa(a[o] * s, a[o + 1] * s), cvtpa(a[o + 2] * s, a[o + 3] * s), cvtpa(a[o + 4] * s, a[o + 5] * s), cvtpa(a[o + 6] * s, a[o + 7] * s)}; }
DI u32x4 glu8(const f32x16& a, float nc, float sd) {
  float h[8];
#pragma unroll
  for (int i = 0; i < 8; ++i) { const float e = __builtin_amdgcn_exp2f(a[i] * nc); h[i] = a[8 + i] * __builtin_amdgcn_rcpf(__builtin_fmaf(e, sd, sd)); }
  return (u32x4){cvtpa(h[0], h[1]), cvtpa(h[2], h[3]), cvtpa(h[4], h[5]), cvtpa(h[6], h[7])}; }

typedef unsigned long long u64x8 __attribute__((ext_vector_type(8)));
__global__ __launch_bounds__(512, 2) void fused_block(const void* a_x, const void* a_wqkv, const void* a_wo, const void* a_wg, const void* a_wu, const void* a_wd, void* a_out, void* a_ws) {
  extern __shared__ __attribute__((aligned(16))) char smem[];
  u64x8 arg;
  asm volatile("s_load_dwordx16 %0, %1, 0x0" : "=s"(arg) : "s"(__builtin_amdgcn_kernarg_segment_ptr()));
  const lptr lds = (lptr)smem;
  const unsigned lds0 = (unsigned)(size_t)lds;
  const int tid = threadIdx.x, lane = tid & 63, w = __builtin_amdgcn_readfirstlane(tid >> 6);
  const int b = blockIdx.x, xcd = b & 7, bi = b >> 3;
  const int r32 = lane & 31, hi = lane >> 5;
  if (w == 0) {
    unsigned long long pc; asm volatile("s_getpc_b64 %0" : "=s"(pc));
    const rsrc_t rc = mk((const void*)(pc & ~127ull), CODE_PF);
    const unsigned v = lane < 8 ? (bi * 8 + lane) * 128 : 0x7fffff00u;
    asm volatile("s_mov_b32 m0, %0\n\ts_nop 0\n\tbuffer_load_dword %1, %2, 0 offen lds" :: "s"(lds0 + LDS_BYTES - 256), "v"(v), "s"(rc) : "memory"); }
  const int rb = (xcd & 1) * 8 + (bi & 7), Q4 = xcd >> 1, sj = bi >> 3, set = Q4 * 4 + sj;
  const int rot = 2 * (xcd & 1) + (bi & 4), pos = (bi >> 1) & 1, half = bi & 1, kpart = rot + pos;
  const unsigned l3 = lane >> 3, l7 = lane & 7, l16 = lane * 16;
  const int wx = (w + 2 * sj) & 7, ww = (w + 2 * bi) & 7;
  unsigned vx0 = (rb * 128 + wx * 16 + l3) * 1024 + ((l7 ^ (lane >> 4)) * 16), vx1 = (vx0 + 8192) ^ 64;
  unsigned mx = lds0 + (wx >> 2) * UD + (wx & 3) * 2048, mw = lds0 + ww * 1024;
  const bool tb_ = sj == 0;
  const int p1 = 7 * Q4 + 2 * sj - 1, p2 = p1 + 1;
  const unsigned col0 = 128 * Q4 + (tb_ ? 1024 : sj * 512 - 448);
  const unsigned col1 = tb_ ? 512 + 128 * Q4 : 1536 + 64 * p1, col2 = tb_ ? 128 * Q4 : 3328 + 64 * p1;
  const unsigned str12 = tb_ ? 3072 : 3584, wr = ww * 8 + l3, wpc = (l7 ^ ((lane >> 2) & 4)) * 16;
  unsigned vg0 = wr * 3072 + wpc + col0 * 2, vg1 = wr * str12 + wpc + (tb_ ? col1 * 2 : p1 * 128), vg2 = (vg1 - (tb_ ? 1024 : 0)) ^ 32;
  unsigned so0 = rot * 128, so1 = rot * (64 * 3072), so2 = rot * 64 * str12;
  PIN(vx0); PIN(vx1); PIN(vg0); PIN(vg1); PIN(vg2); asm volatile("" : "+s"(mx), "+s"(mw), "+s"(so0), "+s"(so1), "+s"(so2));
  SBAR();
  asm volatile("s_waitcnt lgkmcnt(0)" : "+s"(arg) :: "memory");
  const void *px = (const void*)arg[0], *pwqkv = (const void*)arg[1], *pwo = (const void*)arg[2], *pwg = (const void*)arg[3], *pwu = (const void*)arg[4], *pwd = (const void*)arg[5];
  void *pout = (void*)arg[6], *pws = (void*)arg[7];
  const rsrc_t ws = mk(pws, WS_END), rx = mk(px, 2048 * 1024), rg0 = mk(pwqkv, 512 * 3072);
  const rsrc_t rg1 = mk(tb_ ? pwqkv : pwg, 512 * str12), rg2 = mk(tb_ ? pwqkv : pwu, 512 * str12);
  const rsrc_t rg3 = mk(pwg, 512 * 3584), rg4 = mk(pwu, 512 * 3584);
#define KC(c) (unsigned)(((c) + rot) & 7)
#define SL(c) (((c) % 3) * 8192)
#define DMA1(c, i) do { \
    if ((i) == 0) dma(mx, SL(c), vx0, rx, KC(c) * 128); if ((i) == 1) dma(mx, SL(c) + 1024, vx1, rx, KC(c) * 128); \
    if ((i) == 2) dma(mw, SL(c) + U_G0, vg0, rg0, KC(c) * (64 * 3072)); if ((i) == 3) dma(mw, SL(c) + U_G1, vg1, rg1, KC(c) * 64 * str12); \
    if ((i) == 4) dma(mw, SL(c) + U_G2, vg2, rg2, KC(c) * 64 * str12); } while (0)
#define DMA2(c, i) do { constexpr int sl_ = SL(c) + ((i) & 2 ? U_XA : U_G1); const unsigned kc_ = KC(2 * ((c) - 8) + ((i) >> 1)) * (64 * 3584); \
    if ((i) & 1) dma(mw, sl_ + UD, vg4, rg4, kc_); else dma(mw, sl_, vg3, rg3, kc_); } while (0)
  dma(mx, 0, vx0, rx, so0); dma(mx, 1024, vx1, rx, so0); SBAR(); dma(mw, U_G0, vg0, rg0, so1); SBAR(); dma(mw, U_G1, vg1, rg1, so2); dma(mw, U_G2, vg2, rg2, so2); SBAR();
  STAMP(47);
  unsigned vg3 = wr * 3584 + wpc + p2 * 128, vg4 = vg3 ^ 32; PIN(vg3); PIN(vg4);
  const int tt = w & 3, cg = w >> 2;
  const int q4 = (lane & 15) >> 2, p4 = lane & 3, g16 = (lane >> 4) & 1, swr = (q4 >> 1) * 4;
  const int wrow = (8 * hi + q4) * 128 + (p4 & 1) * 8;
  const int pq = 4 * cg + 2 * g16 + (p4 >> 1);
  const int gl0 = g16 * UD + (pq ^ swr) * 16, gl1 = gl0 ^ 32;
  lptr wp0 = lds + U_G0 + wrow + (pq ^ swr) * 16;
  lptr wp1 = lds + wrow + (tb_ ? (pq ^ swr) * 16 : gl0);
  lptr wp2 = lds + wrow + (tb_ ? UD + (pq ^ swr ^ 2) * 16 : gl1);
  lptr xp[4];
#pragma unroll
  for (int s = 0; s < 4; ++s) { xp[s] = lds + (tt >> 1) * UD + (tt & 1) * 4096 + r32 * 128 + (((2 * s + hi) ^ ((r32 >> 1) & 7)) * 16); PIN(xp[s]); }
  PIN(wp0); PIN(wp1); PIN(wp2);
  const int ah = xcd * 4 + (bi & 3), aq = bi >> 2, kh = w >> 2;
  const int qown = aq * 8 + 2 * (w & 3) + kh;
  const unsigned gcol = (r32 & 15) + 32 * cg;
  unsigned vf0 = WS_F0 + (lane < 16 ? rb * 16 + lane : (lane & 15) * 16 + set) * FS, vst = (rb * 128 + tt * 32 + r32) * 4, vc0, vc1, vc2;
  { const unsigned vb_ = hi * 5120 + 32 * cg + r32, vg_ = hi * 5120 + (r32 < 16 ? col1 : col2) + gcol;
    vc0 = (col0 + vb_) * 16; vc1 = (tb_ ? col1 + vb_ : vg_) * 16; vc2 = (tb_ ? col2 + vb_ : vg_ + 16) * 16; }
  PIN(vf0); PIN(vst); PIN(vc0); PIN(vc1); PIN(vc2);
  lptr cp1 = lds + (w == 0 ? U_G0 : w == 1 ? U_G1 : U_G2) + pos * 8192 + wrow + (((4 * half + 2 * g16 + (p4 >> 1)) ^ swr ^ (w & 2)) * 16); PIN(cp1);
  const unsigned csb = WS_CS + (kpart >> 2) * 81920 + (kpart & 3) * 4;
  auto colsum = [&](lptr cp, unsigned ccol) {
    const __bf16 o1 = (__bf16)1.0f; const bf16x8 ones = {o1, o1, o1, o1, o1, o1, o1, o1};
    const bf16x8 f0 = trfrag(cp, 512), f1 = trfrag(cp + 2048, 512), f2 = trfrag(cp + 4096, 512), f3 = trfrag(cp + 6144, 512);
    const f32x16 cs = MFMA(ones, f3, MFMA(ones, f2, MFMA(ones, f1, MFMA(ones, f0, (f32x16){}))));
    const float ch = rbf(cs[0]);
    st4(cvtpk(ch, cs[0] - ch), ws, hi ? 0x7fffff00u : l16, csb + ccol * 16);
  };
  u32x2 ep, tag; u32x4 xr; unsigned e0, e1, t0, t1;
  const unsigned srow = rb * 128 + set * 8 + w;
  float sv[8], sa, sb, smean;
  auto swapadd16 = [](float v) { auto r = __builtin_amdgcn_permlane16_swap(__float_as_uint(v), __float_as_uint(v), false, false); return __uint_as_float(r[0]) + __uint_as_float(r[1]); };
  auto swapadd32 = [](float v) { auto r = __builtin_amdgcn_permlane32_swap(__float_as_uint(v), __float_as_uint(v), false, false); return __uint_as_float(r[0]) + __uint_as_float(r[1]); };
#define STATS(i) do { \
    if ((i) == 0 || (i) == 1) { sv[4 * ((i) & 1)] = bflo(xr[2 * ((i) & 1)]); sv[4 * ((i) & 1) + 1] = bfhi(xr[2 * ((i) & 1)]); sv[4 * ((i) & 1) + 2] = bflo(xr[2 * ((i) & 1) + 1]); sv[4 * ((i) & 1) + 3] = bfhi(xr[2 * ((i) & 1) + 1]); \
      PIN(sv[4 * ((i) & 1)]); PIN(sv[4 * ((i) & 1) + 1]); PIN(sv[4 * ((i) & 1) + 2]); PIN(sv[4 * ((i) & 1) + 3]); } \
    if ((i) == 2) { sa = (sv[0] + sv[1]) + sv[2]; PIN(sa); sb = (sv[3] + sv[4]) + sv[5]; PIN(sb); } \
    if ((i) == 3) { sa = ((sa + sv[6]) + sv[7]) + sb; PIN(sa); } \
    if ((i) == 4 || (i) == 14) { sa = dppadd<0x121>(sa); PIN(sa); } if ((i) == 5 || (i) == 15) { sa = dppadd<0x122>(sa); PIN(sa); } \
    if ((i) == 6 || (i) == 16) { sa = dppadd<0x124>(sa); PIN(sa); } if ((i) == 7 || (i) == 17) { sa = dppadd<0x128>(sa); PIN(sa); } \
    if ((i) == 8 || (i) == 18) { sa = swapadd16(sa); PIN(sa); } \
    if ((i) == 9) { smean = swapadd32(sa) * (1.f / 512); PIN(smean); sa = 0.f; } \
    if ((i) >= 10 && (i) <= 13) { const float d_ = sv[(2 * (i) - 20) & 7] - smean, e_ = sv[(2 * (i) - 19) & 7] - smean; sa = __builtin_fmaf(e_, e_, __builtin_fmaf(d_, d_, sa)); PIN(sa); } \
    if ((i) == 19) { sa = rbf(swapadd32(sa) * (1.f / 512)); PIN(sa); } \
    if ((i) == 20) { sa = __builtin_amdgcn_sqrtf(rbf(sa + 1e-5f)); PIN(sa); } \
    if ((i) == 21) st4(cvtpk(-smean, sa), ws, lane ? 0x7fffff00u : 0u, WS_STAT + srow * 4); } while (0)
  bf16x8 xf[32], wa[3], wb[3];
  f32x16 acc0 = {}, acc1 = {}, acc2 = {};
  u32x2 pl; unsigned stat; u32x4 cs0, cs1, cs2;
#define RD1(W, ks) do { xf[ks] = LDS8(xp[(ks) & 3] + SL((ks) >> 2)); W[0] = trfrag(wp0 + SL((ks) >> 2) + ((ks) & 3) * 2048, 512); } while (0)
#define RD1B(W, ks) W[1] = trfrag(wp1 + U_G1 + SL((ks) >> 2) + ((ks) & 3) * 2048, 512)
#define RD1C(W, ks) W[2] = trfrag(wp2 + U_G1 + SL((ks) >> 2) + ((ks) & 3) * 2048, 512)
#define RD2A(W, ks) W[0] = trfrag(wp1 + SL(8 + ((ks) >> 3)) + ((ks) & 4 ? U_XA : U_G1) + ((ks) & 3) * 2048, 512)
#define RD2B(W, ks) W[1] = trfrag(wp2 + SL(8 + ((ks) >> 3)) + ((ks) & 4 ? U_XA : U_G1) + ((ks) & 3) * 2048, 512)
  const int ks2 = bi + 32 * ((w + 7) & 7);
#define W2LOAD() do { if (ks2 < 144) { const rsrc_t r_ = mk(ks2 < 112 ? pwd : pwo, ks2 < 112 ? 1792 * 1024 : 512 * 1024); const unsigned so_ = (ks2 < 112 ? ks2 : ks2 - 112) * 16384 + xcd * 128; \
      LANE(n_); const unsigned v_ = (n_ >> 3) * 1024 + ((n_ & 7) ^ ((n_ >> 2) & 4)) * 16; \
      dma_nt(lds0 + w * 2048, L_PRIV, v_, r_, so_); dma_nt(lds0 + w * 2048, L_PRIV + 1024, v_, r_, so_ + 8192); } } while (0)
#define FLAG(f) do { if (w == 0) st8(tag, ws, l16 ? 0x7fffff00u : 0u, (f) + (rb * 16 + set) * FS); } while (0)
  if (tid < 272) { const unsigned o_ = (tid & 11) == 0 ? 0x3f803f80u : 0u;
    *(__attribute__((address_space(3))) u32x4*)(lds + L_ONES + tid * 16) = (u32x4){o_, o_, o_, o_}; }
  SBAR();
  asm volatile("s_barrier" ::: "memory");
  STAMP(48);
  DMA1(1, 0); DMA1(1, 1); DMA1(1, 2); DMA1(1, 3); DMA1(1, 4);
  STAMP(1);
  WAITBAR(5);
  STAMP(2);
  RD1(wa, 0); RD1B(wa, 0); RD1C(wa, 0);
#define XRLOAD() do { const unsigned z_ = 0; ALD4(xr, l16, rx, srow * 1024); CLD("buffer_load_dwordx2 %0, %1, %2, %3 offen sc1", ep, z_, ws, WS_EPOCH, 1); } while (0)
#define STATG(g) do { if (X_XR) { STATS(2 * ((g) - 12)); STATS(2 * ((g) - 12) + 1); } else STATS(g); } while (0)
  if (!X_XR) XRLOAD();
  DMA1(2, 0); DMA1(2, 4);
#define STEP1(ks, WC, WN) do { constexpr int c_ = ((ks) + 1) >> 2, s_ = ((ks) + 1) & 3; \
    if (s_ == 0) { SBAR(); \
      if ((c_ == 1 || c_ == 2) && w < 3 && pos == c_ - 1) colsum(cp1, (w == 0 ? col0 : w == 1 ? col1 : col2) + 32 * half); \
      if (c_ == 2) st16((u32x4){~0u, ~0u, ~0u, ~0u}, ws, l16, WS_O + (qown * 32 + ah) * 1024);     \
      if (c_ == 5 && (w == 1 || w == 2)) colsum(lds + L_XCS + wrow + (((4 * (w - 1) + 2 * g16 + (p4 >> 1)) ^ swr) * 16), (half ? 3328 : 1536) + 64 * p2 + 32 * (w - 1)); \
      if (c_ == 7 && w == 0) { WAITV(4); PIN(pl); wait_flags(pl, vf0, ws, t0, t1); } \
      if (c_ == 1 && X_XR) XRLOAD(); \
      if (c_ == 3 && X_EXAT) dma(mw, L_XCS, vg3, (half ? rg4 : rg3), kpart * (64 * 3584)); \
      if (c_ == X_W2AT) W2LOAD(); \
      STAMP(16 + 2 * c_); if (c_ == X_W2AT && ks2 < 144) WAITBAR(7); else WAITBAR(c_ == 2 || (c_ == 1 && X_XR) || (c_ == 3 && X_EXAT) ? 6 : c_ < 7 ? 5 : c_ == 7 ? 4 : 8); STAMP(17 + 2 * c_); \
      if (c_ == 1 + X_XR) { PIN(ep); PIN(xr); e0 = __builtin_amdgcn_readfirstlane(ep[0]); e1 = __builtin_amdgcn_readfirstlane(ep[1]);     \
        t0 = e0 * 0x9E3779B1u + 0x7F4A7C15u; t1 = (e1 ^ 0x85EBCA6Bu) + e0; tag = (u32x2){t0, t1}; }     \
      if (c_ == 3 && !X_EXAT) dma(mw, L_XCS, vg3, (half ? rg4 : rg3), kpart * (64 * 3584)); \
      if (c_ == 4) FLAG(WS_F0); \
      if (c_ == 6) { FLAG(WS_F3); POLL(vf0); } \
      if (c_ == 7) { ALD1C(stat, vst, ws, WS_STAT); ALD4C(cs0, vc0, ws, WS_CS); ALD4C(cs1, vc1, ws, WS_CS); ALD4C(cs2, vc2, ws, WS_CS); } \
      if (c_ == 8) { wp1 = lds + wrow + gl0; wp2 = lds + wrow + gl1; PIN(wp1); PIN(wp2); vf0 = WS_F3 + ((lane & 15) * 16 + set) * FS; POLL(vf0); } } \
    SBAR(); \
    if ((ks) < 31) RD1(WN, ((ks) + 1) & 31); else RD2A(WN, 0); \
    if (s_ == X_EARLY) { RD1B(WN, ((ks) + 1) & 31); RD1C(WN, ((ks) + 1) & 31); } \
    if (s_ != X_EARLY) { if (c_ <= 5) DMA1(c_ + 2, s_); else DMA2(c_ + 2, s_); } SBAR(); \
    acc0 = MFMA(WC[0], xf[ks], acc0); SBAR(); STATG(3 * ((ks) - 3)); \
    if (s_ != X_EARLY) { if ((ks) < 31) RD1B(WN, ((ks) + 1) & 31); else RD2B(WN, 0); } \
    if (s_ == X_EARLY) { if (c_ <= 5) DMA1(c_ + 2, s_); else DMA2(c_ + 2, s_); } if (c_ == 8) DMA2(10, 1); SBAR(); \
    acc1 = MFMA(WC[1], xf[ks], acc1); SBAR(); STATG(3 * ((ks) - 3) + 1); \
    if ((ks) < 31 && s_ != X_EARLY) RD1C(WN, ((ks) + 1) & 31); \
    if (c_ >= 1 && c_ <= 5 && s_ == 0) DMA1(c_ + 2, 4); if (c_ == 8) DMA2(10, 2); SBAR(); \
    acc2 = MFMA(WC[2], xf[ks], acc2); SBAR(); STATG(3 * ((ks) - 3) + 2); SBAR(); } while (0)
#define STEP1x4(k) STEP1(k, wa, wb); STEP1(k + 1, wb, wa); STEP1(k + 2, wa, wb); STEP1(k + 3, wb, wa)
  STEP1x4(0); STEP1x4(4); STEP1x4(8); STEP1x4(12); STEP1x4(16); STEP1x4(20); STEP1x4(24); STEP1x4(28);
  DMA2(10, 3);
  asm volatile("s_waitcnt vmcnt(8)" : "+v"(stat), "+v"(cs0), "+v"(cs1), "+v"(cs2) :: "memory");
  STAMP(3);
  unsigned nm2 = (stat & 0xffffu) * 0x10001u;
  bf16x8 nmf = __builtin_bit_cast(bf16x8, (u32x4){nm2, nm2, nm2, nm2});
  acc0 = MFMA(__builtin_bit_cast(bf16x8, cs0), nmf, acc0); acc1 = MFMA(__builtin_bit_cast(bf16x8, cs1), nmf, acc1); acc2 = MFMA(__builtin_bit_cast(bf16x8, cs2), nmf, acc2);
  const float sd = bfhi(stat), rstd = __builtin_amdgcn_rcpf(sd), gsc = -1.4426950408889634f * rstd, qsc = rstd * (0.25f * 1.4426950408889634f), sc0 = sj == 1 ? qsc : rstd;
  const int tile = rb * 4 + tt, hd = 8 * Q4 + (tb_ ? 0 : 4) + 2 * cg;
  const unsigned sok = WS_KV + hd * 131072 + tile * 1024, soq = WS_Q + (hd * 64 + tile) * 1024, isv = (sj + 1) & 2;
  unsigned l16k = (l16 & 0x330) | (l16 & 64) * 2 | (l16 & 128) / 2; if (sj == 1) l16k = l16; PIN(l16k);
  SBAR();
  STAMP(4);
  u32x4 qf0, qf1, hp, hq;
  f32x16 acc3 = {}, acc4 = {};
  auto glu2 = [&](const f32x16& a, int i) {
    const float e0_ = __builtin_amdgcn_exp2f(a[i] * gsc), e1_ = __builtin_amdgcn_exp2f(a[i + 1] * gsc);
    return cvtpa(a[8 + i] * __builtin_amdgcn_rcpf(__builtin_fmaf(e0_, sd, sd)), a[9 + i] * __builtin_amdgcn_rcpf(__builtin_fmaf(e1_, sd, sd))); };
  const unsigned mk_ = lds0 + ((w + aq) & 7) * 1024, skv = WS_KV + ah * 131072 + ((w + aq) & 7) * 1024;
#define KVD(i) dma_sc1(mk_, (i) * 8192 + ((i) >= 8 ? L_V - 65536 : 0), l16, ws, skv + (i) * 8192)
#define STEP2(ks, WC, WN) do { constexpr int c_ = 8 + (((ks) + 1) >> 3), s_ = ((ks) + 1) & 7; \
    if (s_ == 0 && c_ < 12) { SBAR(); \
      if (c_ == 9 && w == 0) { WAITV(6); PIN(pl); wait_flags(pl, vf0, ws, t0, t1); } \
      STAMP(16 + 2 * c_); if (c_ == 9) WAITBAR(6); else if (c_ == 11) LBAR(); else if (w == 0) WAITBAR(3); else WAITBAR(2); STAMP(17 + 2 * c_); } \
    if ((ks) == 12) { asm volatile("s_waitcnt vmcnt(4)\n\ts_barrier" ::: "memory"); FLAG(WS_F1); STAMP(51); } \
    if ((ks) == 21) POLL(vf0); \
    if ((ks) == 27) { \
      if (w == 0) { WAITV(0); PIN(pl); wait_flags(pl, vf0, ws, t0, t1); } \
      STAMP(52); asm volatile("s_barrier" ::: "memory"); STAMP(53); } \
    SBAR(); \
    if ((ks) < 31) RD2A(WN, ((ks) + 1) & 31); \
    if (s_ == 7) RD2B(WN, ((ks) + 1) & 31); \
    if (c_ == 9 && s_ < 4) DMA2(11, s_); \
    if ((ks) == 13) ALD4C(cs0, vc0, ws, WS_CS); if ((ks) == 14) ALD4C(cs1, vc0, ws, WS_CS + 256); \
    if ((ks) == 27) KVD(0); if ((ks) == 28) KVD(8); if ((ks) == 29) ALD4C(qf1, l16, ws, WS_Q + (ah * 64 + qown) * 1024); SBAR(); \
    acc3 = MFMA(WC[0], xf[ks], acc3); SBAR(); \
    if ((ks) < 31 && s_ != 7) RD2B(WN, ((ks) + 1) & 31); \
    if ((ks) == 27) KVD(4); if ((ks) == 28) KVD(12); if ((ks) == 29) ALD4C(qf0, l16, ws, WS_Q + (ah * 64 + (qown ^ 1)) * 1024); SBAR(); \
    acc4 = MFMA(WC[1], xf[ks], acc4); SBAR(); \
    if ((ks) < 2) { const u32x4 p_ = pack8(acc0, ((ks) & 1) * 8, sc0); if ((ks) == 0) hp = p_; else hq = p_; \
      if (isv) st16(p_, ws, l16k, sj == 1 ? soq + ((ks) & 1) * 65536 : sok + ((ks) & 1) * 131072); } \
    if ((ks) == 2 && !isv) { LANE(n_); const unsigned one_ = (n_ >> 5) == ((n_ >> 2) & 1) ? 0x3f80u << ((n_ & 1) * 16) : 0u, dw_ = ((n_ >> 1) & 1) + ((n_ >> 2) & 6); \
      u32x4 i0_, i1_; _Pragma("unroll") for (int i_ = 0; i_ < 4; ++i_) { i0_[i_] = dw_ == i_ ? one_ : 0u; i1_[i_] = dw_ == 4 + i_ ? one_ : 0u; } \
      acc0 = MFMA(__builtin_bit_cast(bf16x8, hq), __builtin_bit_cast(bf16x8, i1_), MFMA(__builtin_bit_cast(bf16x8, hp), __builtin_bit_cast(bf16x8, i0_), (f32x16){})); } \
    if ((ks) == 3 && !isv) { LANE(n_); const unsigned vvo = (n_ & 16) * 8192 + ((n_ >> 5) * 16 + (n_ & 15)) * 16; \
      st16((u32x4){cvtpk(acc0[0], acc0[1]), cvtpk(acc0[2], acc0[3]), cvtpk(acc0[4], acc0[5]), cvtpk(acc0[6], acc0[7])}, ws, vvo, sok + 65536); \
      st16((u32x4){cvtpk(acc0[8], acc0[9]), cvtpk(acc0[10], acc0[11]), cvtpk(acc0[12], acc0[13]), cvtpk(acc0[14], acc0[15])}, ws, vvo, sok + 65536 + 512); } \
    if ((ks) >= 4 && (ks) <= 6 && tb_) st16(pack8((ks) < 6 ? acc1 : acc2, ((ks) & 1) * 8, (ks) < 6 ? rstd : qsc), ws, (ks) < 6 ? l16k : l16, (ks) < 6 ? sok + ((ks) & 1) * 131072 : soq); \
    if ((ks) == 6 && tb_) st16(pack8(acc2, 8, qsc), ws, l16, soq + 65536); \
    if ((ks) == 7) { LANE(n_); vc0 = ((n_ >> 5) * 5120 + (n_ & 16) * 112 + 1536 + 64 * p2 + 32 * cg + (n_ & 15)) * 16; PIN(vc0); } \
    if ((ks) == 9) { LANE(n_); const int odd = xcd & 1, j_ = n_ >> 4; \
      vf0 = WS_F1 + ((j_ < 2 ? n_ & 15 : 2 * aq + (n_ & 1)) * 16 + Q4 * 4 + odd * (j_ < 2 ? 2 + j_ : 1)) * FS; PIN(vf0); } \
    if ((ks) == 15 && !X_W2AT) W2LOAD(); \
    if ((ks) >= 4 && (ks) <= 20 && !((ks) & 1) && !tb_) { \
      if ((ks) == 16) st16(hp, ws, l16, WS_H + (tile * 112 + 4 * p1 + 2 * cg) * 1024); \
      if ((ks) == 20) st16(hq, ws, l16, WS_H + (tile * 112 + 4 * p1 + 2 * cg + 1) * 1024); \
      else if ((ks) < 12) { hp[(((ks) >> 1) + 2) & 3] = glu2(acc1, ((ks) + 4) & 6); PIN(hp); } \
      else { hq[(((ks) >> 1) + 2) & 3] = glu2(acc2, ((ks) + 4) & 6); PIN(hq); } } SBAR(); } while (0)
#define STEP2x4(k) STEP2(k, wa, wb); STEP2(k + 1, wb, wa); STEP2(k + 2, wa, wb); STEP2(k + 3, wb, wa)
  STEP2x4(0); STEP2x4(4); STEP2x4(8); STEP2x4(12); STEP2x4(16); STEP2x4(20); STEP2x4(24); STEP2x4(28);
  asm volatile("s_waitcnt vmcnt(6)" : "+v"(cs0), "+v"(cs1) :: "memory");
  STAMP(5);
  PIN(nm2); nmf = __builtin_bit_cast(bf16x8, (u32x4){nm2, nm2, nm2, nm2});
  acc3 = MFMA(__builtin_bit_cast(bf16x8, cs0), nmf, acc3); acc4 = MFMA(__builtin_bit_cast(bf16x8, cs1), nmf, acc4);
  {
    LANE(n_); const lptr s2_ = lds + L_PRIV + w * 2048 + (4 * (n_ >> 5) + ((n_ >> 2) & 3)) * 128 + (n_ & 1) * 8; const int pc_ = (((n_ >> 3) & 2) + ((n_ >> 1) & 1)) ^ ((n_ >> 1) & 4);
    hp = __builtin_bit_cast(u32x4, trfrag(s2_ + pc_ * 16, 1024)); hq = __builtin_bit_cast(u32x4, trfrag(s2_ + (pc_ ^ 4) * 16, 1024)); }
  st16(glu8(acc3, gsc, sd), ws, l16, WS_H + (tile * 112 + 4 * p2 + 2 * cg) * 1024);
  LANE(ln); const int r3 = ln & 31, hf = ln >> 5;
  const int tb = xcd * 4 + (bi & 3), n64 = bi >> 2;
  lptr kp = lds + kh * 32768 + l16, kq = lds + (kh ^ 1) * 32768 + l16;
  const bool vrow = r3 < 16;
  int vstep = vrow ? 4096 : 0;
  lptr vp = lds + (vrow ? L_V + kh * 32768 + hf * 256 : L_ONES - 256) + r3 * 16;
  int idx = 0xDDDDDDDD;
  float aone = hf ? 0.f : 1.f;
  PIN(kp); PIN(kq); PIN(vstep); PIN(vp); PIN(idx); PIN(aone);
  f32x16 nm0, nm1, o0, o1, sA, sB;
  const f32x16 z = {};
  { u32x4 zz = {}; PIN(zz); o0 = MFMA(__builtin_bit_cast(bf16x8, zz), __builtin_bit_cast(bf16x8, zz), z); PIN(o0); o1 = MFMA(__builtin_bit_cast(bf16x8, zz), __builtin_bit_cast(bf16x8, zz), o0); PIN(o1); }
  STAMP(6);
  WAITBAR(1);
  PIN(qf0); PIN(qf1);
  STAMP(7);
  const bf16x8 q0 = __builtin_bit_cast(bf16x8, qf0), q1 = __builtin_bit_cast(bf16x8, qf1);
  auto hmax = [&](float m) { auto rr = __builtin_amdgcn_permlane32_swap(__float_as_uint(m), __float_as_uint(m), false, false);
    return __builtin_fmaxf(__uint_as_float(rr[0]), __uint_as_float(rr[1])); };
  auto max16 = [&](float m, const f32x16& s) {
#pragma unroll
    for (int i = 0; i < 16; i += 2) m = __builtin_fmaxf(__builtin_fmaxf(m, s[i]), s[i + 1]);
    return m; };
  __builtin_amdgcn_s_setprio(1);
  bf16x8 k1 = LDS8(kp), va, vb;
  {
    const bf16x8 k0 = LDS8(kq);
    va = LDS8(vp); SBAR();
    if (ks2 < 144) { st16(hp, ws, l16, WS_W2 + (2 * xcd * 144 + ks2) * 1024); st16(hq, ws, l16, WS_W2 + ((2 * xcd + 1) * 144 + ks2) * 1024); }
    hp[0] = glu2(acc4, 0); PIN(hp); SBAR();
    const f32x16 a2 = MFMA(k1, q0, z), a0 = MFMA(k0, q0, z); SBAR();
    hp[1] = glu2(acc4, 2); PIN(hp); SBAR();
    const f32x16 a3 = MFMA(k1, q1, z), a1 = MFMA(k0, q1, z); SBAR();
    float x0 = a0[0], x2 = a2[0];
#pragma unroll
    for (int i = 1; i < 15; i += 2) { x2 = max3(x2, a2[i], a2[i + 1]); x0 = max3(x0, a0[i], a0[i + 1]); }
    nm0 = __builtin_amdgcn_mfma_f32_32x32x2f32(aone, -REF_OFF - hmax(max3(max3(x0, x2, a0[15]), a2[15], a2[15])), z, 0, 0, 0); SBAR();
    float x1 = a1[0], x3 = a3[0];
#pragma unroll
    for (int i = 1; i < 15; i += 2) { x3 = max3(x3, a3[i], a3[i + 1]); x1 = max3(x1, a1[i], a1[i + 1]); }
    float m1 = -REF_OFF - hmax(max3(max3(x1, x3, a1[15]), a3[15], a3[15])); PIN(m1); SBAR();
    sA = MFMA(k1, q0, nm0); SBAR();
    hp[2] = glu2(acc4, 4); PIN(hp); SBAR();
    nm1 = __builtin_amdgcn_mfma_f32_32x32x2f32(aone, m1, z, 0, 0, 0); SBAR();
    hp[3] = glu2(acc4, 6); PIN(hp);
    st16(hp, ws, l16, WS_H + (tile * 112 + 4 * p2 + 2 * cg + 1) * 1024); SBAR();
  }
  f32x8 pa, pb;
  f32x16 g00, g01, g10, g11;
  bf16x8 fa[4], fb[4];
  unsigned sow = WS_W2 + (2 * n64 * 144 + 14 * w) * 1024, soh = WS_H + ((2 * tb + kh) * 112 + 14 * w) * 1024;
  const unsigned dh = kh ? -112 * 1024 : 112 * 1024;
#define EX(P, S, i) P[(i) & 7] = __builtin_amdgcn_exp2f(S[i])
#define T2(u, A0, A1, B0, B1) T2X(o1 = SMF(vb, pb, o1, idx), u, A0, A1, B0, B1)
#define T2X(FIN, u, A0, A1, B0, B1) do { \
    EX(pa, sA, 0); EX(pa, sA, 1); SBAR(); FIN; SBAR(); vb = LDS8(vp + (u) * 1024 + 512); SBAR(); \
    EX(pa, sA, 2); EX(pa, sA, 3); EX(pa, sA, 4); EX(pa, sA, 5); SBAR(); sB = MFMA(k1, q1, nm1); SBAR(); k1 = LDS8(kp + ((u) + 1) * 1024); SBAR(); \
    EX(pa, sA, 6); EX(pa, sA, 7); EX(pb, sA, 8); EX(pb, sA, 9); SBAR(); o0 = SMF(va, pa, o0, idx); SBAR(); \
    EX(pb, sA, 10); EX(pb, sA, 11); EX(pb, sA, 12); EX(pb, sA, 13); SBAR(); A0; A1; SBAR(); EX(pb, sA, 14); EX(pb, sA, 15); SBAR(); \
    EX(pa, sB, 0); EX(pa, sB, 1); SBAR(); o0 = SMF(vb, pb, o0, idx); SBAR(); \
    EX(pa, sB, 2); EX(pa, sB, 3); EX(pa, sB, 4); EX(pa, sB, 5); SBAR(); sA = MFMA(k1, q0, nm0); SBAR(); \
    EX(pa, sB, 6); EX(pa, sB, 7); EX(pb, sB, 8); EX(pb, sB, 9); SBAR(); o1 = SMF(va, pa, o1, idx); SBAR(); va = LDS8(vp + ((u) + 1) * 1024); SBAR(); \
    EX(pb, sB, 10); EX(pb, sB, 11); EX(pb, sB, 12); EX(pb, sB, 13); SBAR(); B0; B1; SBAR(); EX(pb, sB, 14); EX(pb, sB, 15); SBAR(); } while (0)
#define NOP_ do {} while (0)
#define ADV() do { kp += 4096; vp += vstep; } while (0)
#define G2P(u, C) do { \
    T2(u, g00 = MFMA(C[0], C[2], g00), NOP_, g10 = MFMA(C[1], C[2], g10), { SBAR(); C[2] = ld16c(ws, l16, soh); }); \
    T2(u + 1, g11 = MFMA(C[1], C[3], g11), { SBAR(); C[1] = ld16c(ws, l16, sow + 144 * 1024); }, g01 = MFMA(C[0], C[3], g01), { SBAR(); C[0] = ld16c(ws, l16, sow); C[3] = ld16c(ws, l16, soh + dh); }); \
    sow += 1024; soh += 1024; } while (0)
#define PRIO_AT(k, n) asm volatile("s_cmp_lg_u32 %0, " #k "\n\ts_cbranch_scc1 1f\n\ts_setprio " #n "\n1:" :: "s"(it) : "scc")
#define PRIO(n) __builtin_amdgcn_s_setprio(n)
  T2X(NOP_, 0, KVD(1), KVD(5), KVD(9), NOP_); PRIO(0); T2(1, KVD(13), KVD(2), KVD(6), NOP_);
  STAMP(41);
  asm volatile("s_waitcnt vmcnt(6)\n\ts_barrier" ::: "memory");
  STAMP(42);
  FLAG(WS_F2);
  if (w == 0 && b == 0) st8((u32x2){e0 + 1, e1}, ws, l16 ? 0x7fffff00u : 0u, WS_EPOCH);
  const unsigned vf2 = WS_F2 + (ln < 16 ? (tb >> 1) * 16 + ln : (((n64 & 1) * 8 + (ln & 7)) * 16 + (n64 >> 1) * 4 + ((ln >> 3) & 3))) * FS;
  PRIO(2); T2(2, KVD(10), KVD(14), KVD(3), NOP_); T2(3, KVD(7), KVD(11), KVD(15), NOP_); ADV();
  PRIO(1); T2(0, NOP_, NOP_, POLL(vf2), NOP_); PRIO(0); T2(1, NOP_, NOP_, NOP_, NOP_);
  STAMP(43);
  if (w == 0) { WAITV(0); PIN(pl); wait_flags(pl, vf2, ws, t0, t1); }
  STAMP(44);
  asm volatile("s_waitcnt vmcnt(0)\n\ts_barrier" ::: "memory");
  STAMP(8);
  const unsigned vout = (tb * 64 + w * 8 + (ln >> 3)) * 1024 + n64 * 128 + (ln & 7) * 16;
  u32x4 xres;
  PRIO(3);
  xres = __builtin_amdgcn_raw_buffer_load_b128(rx, vout, 0, 0);
  if (w < 2) {
    const unsigned v = ln * 128;
    asm volatile("s_mov_b32 m0, %0\n\ts_nop 0\n\tbuffer_load_dword %1, %2, %3 offen sc1 lds" :: "s"(lds0 + LDS_BYTES - 256), "v"(v), "s"(ws), "s"(WS_W2 + ((2 * n64 + w) * 144 + 112 + 8 * (bi & 3)) * 1024) : "memory"); }
  { bf16x8 zz = {};
#define GZ(g) do { PIN(zz); g = MFMA(zz, zz, z); PIN(g); SBAR(); } while (0)
    T2(2, GZ(g00), (fa[2] = ld16c(ws, l16, soh), fa[0] = ld16c(ws, l16, sow)), GZ(g10), (fa[1] = ld16c(ws, l16, sow + 144 * 1024), fa[3] = ld16c(ws, l16, soh + dh)));
    T2(3, GZ(g11), (fb[2] = ld16c(ws, l16, soh + 1024), fb[1] = ld16c(ws, l16, sow + 145 * 1024)), GZ(g01), (fb[0] = ld16c(ws, l16, sow + 1024), fb[3] = ld16c(ws, l16, soh + dh + 1024))); ADV(); }
  sow += 2048; soh += 2048;
#pragma unroll 1
  for (int it = 0; it < 5; ++it) { STAMPV(54 + it); if (X_PR) { PRIO_AT(2, 2); PRIO_AT(4, 1); } else PRIO_AT(3, 2); G2P(0, fa); if (X_PR) PRIO_AT(4, 0); G2P(2, fb); ADV(); }
  auto lwo_of = [](int g) { return (unsigned)(g < 3 ? g : g < 6 ? g + 1 : g + 3) * 8192; };
  const unsigned swo = WS_W2 + (2 * n64 * 144 + 112 + 4 * w) * 1024, lwo = lwo_of(w);
#define WOD(i) dma_sc1(lds0 + lwo, (i) * 1024, l16, ws, swo + ((i) & 3) * 1024 + ((i) >> 2) * (144 * 1024))
  STAMPV(59);
  asm volatile("s_barrier" ::: "memory");
  STAMPV(60);
  PRIO(2);
  bf16x8 fc[4];
  typedef __attribute__((address_space(3))) f32x4* l4p;
  typedef __attribute__((address_space(3))) float* l1p;
  lptr xw = lds + L_PRIV + w * 2048 + l16, xl = lds + 65536 + w * 256 + ln * 4; PIN(xw); PIN(xl);
#define GM(g, a, b) do { g = MFMA(a, b, g); PIN(g); SBAR(); } while (0)
  T2(0, GM(g00, fa[0], fa[2]), { WOD(0); WOD(1); WOD(2); }, GM(g10, fa[1], fa[2]), { WOD(3); WOD(4); fa[2] = ld16c(ws, l16, soh); });
  T2(1, GM(g11, fa[1], fa[3]), { WOD(5); WOD(6); fa[1] = ld16c(ws, l16, sow + 144 * 1024); }, GM(g01, fa[0], fa[3]), { WOD(7); fa[0] = ld16c(ws, l16, sow); fa[3] = ld16c(ws, l16, soh + dh); });
  PRIO(1);
  T2(2, fc[0] = ld16c(ws, l16, sow + 1024), fc[2] = ld16c(ws, l16, soh + 1024), fc[1] = ld16c(ws, l16, sow + 145 * 1024), fc[3] = ld16c(ws, l16, soh + dh + 1024));
  PRIO(0);
  auto xchg = [&]() { PIN(o0); *(l4p)(xw) = (f32x4){o0[0], o0[1], o0[2], o0[3]}; *(l4p)(xw + 1024) = (f32x4){o0[4], o0[5], o0[6], o0[7]}; *(l1p)(xl) = o0[8]; };
  T2(3, NOP_, NOP_, xchg(), NOP_);
  o1 = SMF(vb, pb, o1, idx);
  STAMP(9);
  xw = (lptr)((unsigned)(size_t)(xw - L_PRIV) ^ 8192) + L_PRIV; xl = (lptr)((unsigned)(size_t)xl ^ 1024); PIN(xw); PIN(xl); SBAR();
  LBAR();
  float oo[9];
  {
    const f32x4 a = *(l4p)(xw), c = *(l4p)(xw + 1024); const float lp = *(l1p)(xl);
    SBAR(); asm volatile("" :: "v"(o1));
    oo[8] = o1[8] + lp;
    float sm = 0.f;
#pragma unroll
    for (int i = 0; i < 4; ++i) { oo[i] = o1[i] + a[i]; oo[4 + i] = o1[4 + i] + c[i]; }
#pragma unroll
    for (int i = 0; i < 8; ++i) sm += __builtin_fabsf(oo[i]);
    const bool ok = !FORCE_FB && __builtin_amdgcn_fmed3f(oo[8], 0x1p-104f, 0x1p120f) == oo[8] && __builtin_amdgcn_fmed3f(sm, 0x1p-110f, 0x1p127f) == sm;
    if (__builtin_expect(__builtin_amdgcn_ballot_w64(!ok) != 0, 0)) {
      const unsigned sk = WS_KV + ah * 131072, vv = vrow ? (hf * 16 + r3) * 16 : 0x7fffff00u, one = (r3 & 27) == 16 ? 0x3f803f80u : 0u;
      float m = -INFINITY;
#pragma unroll 1
      for (int t = 0; t < 64; ++t) m = max16(m, MFMA(ld16c(ws, l16, sk + t * 1024), q1, z));
      m = -hmax(m);
#pragma unroll
      for (int i = 0; i < 16; ++i) nm0[i] = m;
      o0 = z;
#pragma unroll 1
      for (int t = 0; t < 64; ++t) {
        const f32x16 s_ = MFMA(ld16c(ws, l16, sk + t * 1024), q1, nm0);
        const u32x4 v0 = __builtin_bit_cast(u32x4, ld16c(ws, vv, sk + 65536 + t * 1024)) | one, v1 = __builtin_bit_cast(u32x4, ld16c(ws, vv, sk + 65536 + t * 1024 + 512)) | one;
#pragma unroll
        for (int i = 0; i < 8; ++i) { pa[i] = __builtin_amdgcn_exp2f(s_[i]); pb[i] = __builtin_amdgcn_exp2f(s_[8 + i]); }
        o0 = SMF(__builtin_bit_cast(bf16x8, v0), pa, o0, idx); o0 = SMF(__builtin_bit_cast(bf16x8, v1), pb, o0, idx);
      }
      float unit = 1.f; PIN(unit);
#pragma unroll
      for (int i = 0; i < 9; ++i) oo[i] = o0[i] * unit;
    }
  }
  { const float rl = __builtin_amdgcn_rcpf(oo[8]);
    u32x4 ov = {cvtpa(oo[0] * rl, oo[1] * rl), cvtpa(oo[2] * rl, oo[3] * rl), cvtpa(oo[4] * rl, oo[5] * rl), cvtpa(oo[6] * rl, oo[7] * rl)};
#pragma unroll
    for (int i = 0; i < 4; ++i) ov[i] = ov[i] < 0xfffffffeu ? ov[i] : 0xfffffffeu;
#pragma unroll
    for (int i = 0; i < 4; ++i) { PIN(fa[i]); PIN(fb[i]); PIN(fc[i]); }
    WAITV(0);
    st16(ov, ws, l16, WS_O + (qown * 32 + ah) * 1024); }
  if (X_OT && w == X_OT - 1) { const unsigned v_ = ln * 128;
    asm volatile("s_mov_b32 m0, %0\n\ts_nop 0\n\tbuffer_load_dword %1, %2, %3 offen sc1 lds" :: "s"(lds0 + LDS_BYTES - 256), "v"(v_), "s"(ws), "s"(WS_O + tb * 65536 + n64 * 8192) : "memory"); }
  STAMP(45);
  asm volatile("s_barrier" ::: "memory");
  {
    lptr d = lds + L_PAIR + w * 8448 + l16; PIN(d);
    GM(g01, fb[0], fb[3]); GM(g11, fb[1], fb[3]); GM(g01, fa[0], fa[3]); GM(g11, fa[1], fa[3]); GM(g01, fc[0], fc[3]); GM(g11, fc[1], fc[3]);
    GM(g00, fb[0], fb[2]); GM(g10, fb[1], fb[2]);
#define QUAD(g, i) (f32x4){g[4 * (i)], g[4 * (i) + 1], g[4 * (i) + 2], g[4 * (i) + 3]}
    *(l4p)(d) = QUAD(g01, 0); *(l4p)(d + 1024) = QUAD(g01, 1); SBAR(); GM(g00, fa[0], fa[2]);
    *(l4p)(d + 2048) = QUAD(g01, 2); *(l4p)(d + 3072) = QUAD(g01, 3); SBAR(); GM(g10, fa[1], fa[2]);
    *(l4p)(d + 4096) = QUAD(g11, 0); *(l4p)(d + 5120) = QUAD(g11, 1); SBAR(); GM(g00, fc[0], fc[2]);
    *(l4p)(d + 6144) = QUAD(g11, 2); *(l4p)(d + 7168) = QUAD(g11, 3); SBAR(); GM(g10, fc[1], fc[2]);
  }
  LBAR();
  {
    const int hq = (w + n64) & 3;
    const unsigned soo = WS_O + ((2 * tb + kh) * 32 + 8 * hq) * 1024; unsigned spins; unsigned long long ret; u32x16 ofa, ofb;
#define OL0 "buffer_load_dwordx4 v[0:3], %[l], %[r], %[o0] offen sc1\n\t"
#define OL1 "buffer_load_dwordx4 v[4:7], %[l], %[r], %[o0] offen offset:1024 sc1\n\t"
#define OL2 "buffer_load_dwordx4 v[8:11], %[l], %[r], %[o0] offen offset:2048 sc1\n\t"
#define OL3 "buffer_load_dwordx4 v[12:15], %[l], %[r], %[o0] offen offset:3072 sc1\n\t"
#define OL4 "buffer_load_dwordx4 v[16:19], %[l], %[r], %[o1] offen sc1\n\t"
#define OL5 "buffer_load_dwordx4 v[20:23], %[l], %[r], %[o1] offen offset:1024 sc1\n\t"
#define OL6 "buffer_load_dwordx4 v[24:27], %[l], %[r], %[o1] offen offset:2048 sc1\n\t"
#define OL7 "buffer_load_dwordx4 v[28:31], %[l], %[r], %[o1] offen offset:3072 sc1\n\t"
    asm volatile(OL0 OL1 OL2 OL3 OL4 OL5 OL6 OL7 : "=&{v[0:15]}"(ofa), "=&{v[16:31]}"(ofb) : [l] "v"(l16), [r] "s"(ws), [o0] "s"(soo), [o1] "s"(soo + 4096) : "memory");
    const lptr s = lds + L_PAIR + (w ^ 4) * 8448 + l16, wl0 = lds + lwo_of(2 * hq) + l16, wl1 = lds + lwo_of(2 * hq + 1) + l16;
    f32x4 pr[8]; bf16x8 wa_[8], wb_[8];
#pragma unroll
    for (int i = 0; i < 8; ++i) pr[i] = *(l4p)(s + i * 1024);
#pragma unroll
    for (int i = 0; i < 8; ++i) { wa_[i] = LDS8((i < 4 ? wl0 : wl1) + (i & 3) * 1024); wb_[i] = LDS8((i < 4 ? wl0 : wl1) + (i & 3) * 1024 + 4096); }
    SBAR();
#pragma unroll
    for (int i = 0; i < 16; ++i) { g00[i] += pr[i >> 2][i & 3]; g10[i] += pr[4 + (i >> 2)][i & 3]; }
    PIN(g00); PIN(g10);
#define OCHK(r0, r1, r2, r3) "v_max3_u32 v32, v" #r0 ", v" #r1 ", v" #r2 "\n\tv_max_u32 v32, v32, v" #r3 "\n\tv_cmp_eq_u32 vcc, -1, v32\n\t"
#define OSTEP(i, n, r0, r1, r2, r3) \
    "s_getpc_b64 %[ret]\n\ts_waitcnt vmcnt(" #n ")\n\t" OCHK(r0, r1, r2, r3) "s_cbranch_vccnz 3f\n\t" \
    "v_mfma_f32_32x32x16_bf16 %[g0], %[a" #i "], v[" #r0 ":" #r3 "], %[g0]\n\tv_mfma_f32_32x32x16_bf16 %[g1], %[b" #i "], v[" #r0 ":" #r3 "], %[g1]\n\t"
#define ORE(r0, r1, r2, r3, load) OCHK(r0, r1, r2, r3) "s_cbranch_vccz 4f\n\t" load "4:\n\t"
    static_assert(SPIN_CAP == 0x8000, "literal below");
    asm volatile("s_mov_b32 %[sp], 0\n\t"
                 OSTEP(0, 7, 0, 1, 2, 3) OSTEP(1, 6, 4, 5, 6, 7) OSTEP(2, 5, 8, 9, 10, 11) OSTEP(3, 4, 12, 13, 14, 15)
                 OSTEP(4, 3, 16, 17, 18, 19) OSTEP(5, 2, 20, 21, 22, 23) OSTEP(6, 1, 24, 25, 26, 27) OSTEP(7, 0, 28, 29, 30, 31)
                 "s_branch 2f\n"
                 "3:\n\ts_add_u32 %[sp], %[sp], 1\n\ts_cmp_gt_u32 %[sp], 0x8000\n\ts_cbranch_scc1 2f\n\ts_waitcnt vmcnt(0)\n\t"
                 ORE(0, 1, 2, 3, OL0) ORE(4, 5, 6, 7, OL1) ORE(8, 9, 10, 11, OL2) ORE(12, 13, 14, 15, OL3)
                 ORE(16, 17, 18, 19, OL4) ORE(20, 21, 22, 23, OL5) ORE(24, 25, 26, 27, OL6) ORE(28, 29, 30, 31, OL7)
                 "s_waitcnt vmcnt(0)\n\ts_setpc_b64 %[ret]\n"
                 "2:\n\ts_waitcnt vmcnt(0)\n\ts_nop 11"
                 : [g0] "+v"(g00), [g1] "+v"(g10), "+{v[0:15]}"(ofa), "+{v[16:31]}"(ofb), [sp] "=&s"(spins), [ret] "=&s"(ret)
                 : [l] "v"(l16), [r] "s"(ws), [o0] "s"(soo), [o1] "s"(soo + 4096),
                   [a0] "v"(wa_[0]), [a1] "v"(wa_[1]), [a2] "v"(wa_[2]), [a3] "v"(wa_[3]), [a4] "v"(wa_[4]), [a5] "v"(wa_[5]), [a6] "v"(wa_[6]), [a7] "v"(wa_[7]),
                   [b0] "v"(wb_[0]), [b1] "v"(wb_[1]), [b2] "v"(wb_[2]), [b3] "v"(wb_[3]), [b4] "v"(wb_[4]), [b5] "v"(wb_[5]), [b6] "v"(wb_[6]), [b7] "v"(wb_[7])
                 : "memory", "scc", "vcc", "v32");
  }
  STAMP(12);
  { const lptr d = lds + L_PAIR + (w ^ 4) * 8448 + l16;
#pragma unroll
    for (int i = 0; i < 4; ++i) { *(l4p)(d + i * 1056) = QUAD(g00, i); *(l4p)(d + (4 + i) * 1056) = QUAD(g10, i); } }
  lptr rs = lds + L_PAIR + (kh ^ 1) * (4 * 8448) + (ln & 7) * 1056 + ((w & 3) * 8 + (ln >> 3)) * 16; PIN(rs);
  f32x4 ra = {bflo(xres[0]), bfhi(xres[0]), bflo(xres[1]), bfhi(xres[1])}, rc = {bflo(xres[2]), bfhi(xres[2]), bflo(xres[3]), bfhi(xres[3])}; PIN(ra); PIN(rc);
  LBAR();
  STAMP(13);
  {
    f32x4 pa_[4], pc_[4];
#pragma unroll
    for (int i = 0; i < 4; ++i) { pa_[i] = *(l4p)(rs + i * 8448); pc_[i] = *(l4p)(rs + i * 8448 + 512); }
    SBAR();
#pragma unroll
    for (int i = 0; i < 4; ++i) { ra += pa_[i]; rc += pc_[i]; PIN(ra); PIN(rc); }
    const u32x4 r = {cvtpk(ra[0], ra[1]), cvtpk(ra[2], ra[3]), cvtpk(rc[0], rc[1]), cvtpk(rc[2], rc[3])};
    __builtin_amdgcn_raw_buffer_store_b128(r, mk(pout, 2048 * 1024), vout, 0, 0);
  }
  STAMP(14);
  STAMP_END();
}

extern "C" void kernel_launch(void* const* d_in, const int* in_sizes, int n_in, void* d_out, int out_size, void* d_ws, size_t ws_size, hipStream_t stream) {
  (void)hipFuncSetAttribute((const void*)fused_block, hipFuncAttributeMaxDynamicSharedMemorySize, LDS_BYTES);
  hipLaunchKernelGGL(fused_block, dim3(256), dim3(512), LDS_BYTES, stream, d_in[0], d_in[1], d_in[2], d_in[4], d_in[5], d_in[6], d_out, d_ws);
}
```

```cpp
#include <hip/hip_runtime.h>
#include <hip/hip_bf16.h>

typedef __bf16 bf16x8 __attribute__((ext_vector_type(8)));
typedef __bf16 bf16x16 __attribute__((ext_vector_type(16)));
typedef __bf16 bf16x2 __attribute__((ext_vector_type(2)));
typedef short s16x4 __attribute__((ext_vector_type(4)));
typedef float f32x16 __attribute__((ext_vector_type(16)));
typedef float f32x8 __attribute__((ext_vector_type(8)));
typedef float f32x4 __attribute__((ext_vector_type(4)));
typedef float f32x2 __attribute__((ext_vector_type(2)));
typedef unsigned u32x4 __attribute__((ext_vector_type(4)));
typedef unsigned u32x2 __attribute__((ext_vector_type(2)));
typedef unsigned u32x16 __attribute__((ext_vector_type(16)));
typedef __amdgpu_buffer_rsrc_t rsrc_t;
typedef __attribute__((address_space(3))) char* lptr;
#define DBG_STAMPS 0
#define DI __device__ __forceinline__
#define SBAR() __builtin_amdgcn_sched_barrier(0)
#define MFMA(a, b, c) __builtin_amdgcn_mfma_f32_32x32x16_bf16(a, b, c, 0, 0, 0)
#define SMF(a, b, c, idx) __builtin_amdgcn_smfmac_f32_32x32x32_bf16(a, __builtin_bit_cast(bf16x16, b), c, idx, 0, 0)
#define LDS8(p) (*(const __attribute__((address_space(3))) bf16x8*)(p))
#define PIN(x) asm volatile("" : "+v"(x))
#define LANE(n) unsigned n = l16; PIN(n); n >>= 4

#define X_P1 0
#define X_ST 0
#define X_FINE 0
#define FORCE_FB 0

constexpr unsigned WS_EPOCH = 0;
constexpr unsigned FS = 128;
constexpr unsigned WS_F0 = 262144, WS_F1 = WS_F0 + 256 * FS, WS_F2 = WS_F1 + 256 * FS, WS_F3 = WS_F2 + 256 * FS;
constexpr unsigned WS_STAT = 32768;
constexpr unsigned WS_CS = 65536;
constexpr unsigned WS_Q = 1u << 20;
constexpr unsigned WS_KV = WS_Q + (2u << 20);
constexpr unsigned WS_O = WS_KV + (4u << 20);
constexpr unsigned WS_W2 = WS_O + (2u << 20);
constexpr unsigned WS_H = WS_W2 + (3u << 20);
constexpr unsigned WS_DBG = WS_H + (7u << 20);
constexpr unsigned WS_END = WS_DBG + (1u << 20);
constexpr int U_XA = 0, U_G1 = 24576, U_XB = 49152, U_G2 = 73728, U_G0 = 98304, UD = 49152;
constexpr int L_XCS = 122880, L_V = 73728, L_PRIV = 139264, L_ONES = 155648, L_EYE = L_ONES + 5120, L_PAIR = 90112, LDS_BYTES = 163840;
constexpr unsigned SPIN_CAP = 1u << 15;
constexpr float REF_OFF = 100.f;

DI rsrc_t mk(const void* p, unsigned bytes) { return __builtin_amdgcn_make_buffer_rsrc((void*)p, (short)0, (int)bytes, 0x00020000); }
DI unsigned cvtpk(float lo, float hi) { f32x2 v = {lo, hi}; return __builtin_bit_cast(unsigned, __builtin_convertvector(v, bf16x2)); }
DI unsigned cvtpa(float lo, float hi) { unsigned r; asm("v_cvt_pk_bf16_f32 %0, %1, %2" : "=v"(r) : "v"(lo), "v"(hi)); return r; }
DI float bflo(unsigned u) { return __uint_as_float(u << 16); }
DI float bfhi(unsigned u) { return __uint_as_float(u & 0xffff0000u); }
DI float rbf(float f) { return bflo(cvtpk(f, f)); }
DI float max3(float a, float b, float c) { return __builtin_fmaxf(__builtin_fmaxf(a, b), c); }
DI s16x4 trd(lptr p) { return __builtin_amdgcn_ds_read_tr16_b64_v4i16((__attribute__((address_space(3))) s16x4*)p); }
DI bf16x8 trfrag(lptr p, int step) { s16x4 a = trd(p), b = trd(p + step); return __builtin_bit_cast(bf16x8, __builtin_shufflevector(a, b, 0, 1, 2, 3, 4, 5, 6, 7)); }
#define dma(base, imm, voff, r, soff) asm volatile("s_add_i32 m0, %0, %4\n\ts_nop 0\n\tbuffer_load_dwordx4 %1, %2, %3 offen lds" :: "s"(base), "v"(voff), "s"(r), "s"(soff), "i"(imm) : "memory", "scc")
#define dma_m0(base, imm) asm volatile("s_add_i32 m0, %0, %1" :: "s"(base), "i"(imm) : "memory", "scc")
#define dma_go(voff, r, soff) asm volatile("buffer_load_dwordx4 %0, %1, %2 offen lds" :: "v"(voff), "s"(r), "s"(soff) : "memory")
#define dma_nt(base, imm, voff, r, soff) asm volatile("s_add_i32 m0, %0, %4\n\ts_nop 0\n\tbuffer_load_dwordx4 %1, %2, %3 offen nt lds" :: "s"(base), "v"(voff), "s"(r), "s"(soff), "i"(imm) : "memory", "scc")
#define dma_sc1(base, imm, voff, r, soff) asm volatile("s_add_i32 m0, %0, %4\n\ts_nop 0\n\tbuffer_load_dwordx4 %1, %2, %3 offen sc1 lds" :: "s"(base), "v"(voff), "s"(r), "s"(soff), "i"(imm) : "memory", "scc")
#define ALD4(dst, voff, r, soff) asm volatile("buffer_load_dwordx4 %0, %1, %2, %3 offen" : "=v"(dst) : "v"(voff), "s"(r), "s"(soff) : "memory")
#define ALD4C(dst, voff, r, soff) asm volatile("buffer_load_dwordx4 %0, %1, %2, %3 offen sc1" : "=v"(dst) : "v"(voff), "s"(r), "s"(soff) : "memory")
#define ALD2C(dst, voff, r, soff) asm volatile("buffer_load_dwordx2 %0, %1, %2, %3 offen sc1" : "=v"(dst) : "v"(voff), "s"(r), "s"(soff) : "memory")
#define ALD1C(dst, voff, r, soff) asm volatile("buffer_load_dword %0, %1, %2, %3 offen sc1" : "=v"(dst) : "v"(voff), "s"(r), "s"(soff) : "memory")
#define CLD(op, dst, voff, r, soff, lim) asm volatile("s_cmp_ge_u32 %4, %5\n\ts_cbranch_scc1 1f\n\t" op "\n1:" : "=v"(dst) : "v"(voff), "s"(r), "s"(soff), "s"(w), "i"(lim) : "memory", "scc")
#define POLL(voff) CLD("buffer_load_dwordx2 %0, %1, %2, %3 offen sc1", pl, voff, ws, 0, 1)
#define WAITV(n) asm volatile("s_waitcnt vmcnt(%0)" :: "i"(n) : "memory")
#define WAITBAR(n) asm volatile("s_waitcnt vmcnt(%0) lgkmcnt(0)\n\ts_barrier" :: "i"(n) : "memory")
#define LBAR() asm volatile("s_waitcnt lgkmcnt(0)\n\ts_barrier" ::: "memory")
DI void st16(u32x4 v, rsrc_t r, unsigned voff, unsigned soff) {
  asm volatile("buffer_store_dwordx4 %0, %1, %2, %3 offen sc1\n\ts_nop 1" :: "v"(v), "v"(voff), "s"(r), "s"(soff) : "memory"); }
DI void st4(unsigned v, rsrc_t r, unsigned voff, unsigned soff) { __builtin_amdgcn_raw_buffer_store_b32(v, r, voff, soff, 16); }
DI bf16x8 ld16c(rsrc_t r, unsigned voff, unsigned soff) { return __builtin_bit_cast(bf16x8, __builtin_amdgcn_raw_buffer_load_b128(r, voff, soff, 16)); }
template <int CTRL> DI float dppadd(float v) { return v + __int_as_float(__builtin_amdgcn_update_dpp(0, __float_as_int(v), CTRL, 0xf, 0xf, true)); }
#define WAITF(n) do { unsigned sp_; asm volatile("s_cmp_lg_u32 %[w], 0\n\ts_cbranch_scc1 9f\n\ts_mov_b32 %[sp], 0\n\ts_waitcnt vmcnt(%[c])\n" \
    "1:\n\tv_cmp_ne_u64 vcc, %[t], %[p]\n\ts_cbranch_vccz 9f\n\ts_add_u32 %[sp], %[sp], 1\n\ts_cmp_gt_u32 %[sp], %[cap]\n\ts_cbranch_scc1 9f\n\ts_sleep 2\n\t" \
    "buffer_load_dwordx2 %[p], %[v], %[r], 0 offen sc1\n\ts_waitcnt vmcnt(0)\n\ts_branch 1b\n9:" \
    : [p] "+v"(pl), [sp] "=&s"(sp_) : [v] "v"(vf0), [r] "s"(ws), [t] "s"(tag), [w] "s"(w), [c] "i"(n), [cap] "i"(SPIN_CAP) : "memory", "scc", "vcc"); } while (0)
#define PUT(val, soff, who) do { unsigned long long t_; asm volatile("s_cmp_lg_u32 %4, 0\n\ts_cbranch_scc1 9f\n\tv_mov_b64 %0, %1\n\ts_mov_b64 exec, 1\n\t" \
    "buffer_store_dwordx2 %0, off, %2, %3 sc1\n\ts_mov_b64 exec, -1\n9:" : "=&v"(t_) : "s"(val), "s"(ws), "s"(soff), "s"(who) : "memory", "scc"); } while (0)
#define WAITV2(n0, n) asm volatile("s_waitcnt vmcnt(%1)\n\ts_cmp_eq_u32 %0, 0\n\ts_cbranch_scc1 9f\n\ts_waitcnt vmcnt(%2)\n9:" :: "s"(w), "i"(n0), "i"(n) : "memory", "scc")
#define STAMP(i) do { if ((i) == 2) { unsigned long long zt_; SBAR(); asm volatile("s_memrealtime %0\n\ts_waitcnt lgkmcnt(0)" : "=&s"(zt_) :: "memory"); dg1 = (unsigned)zt_; SBAR(); } } while (0)
#define STAMPV(i)
#define STAMP_END() do { if (w == 7) { const unsigned long long e_ = dg0 + 4000 + (unsigned)((dg1 - (unsigned)dg0) * 0); while (__builtin_amdgcn_s_memrealtime() < e_) __builtin_amdgcn_s_sleep(1); } } while (0)

DI u32x4 pack8(const f32x16& a, int o, float s) {
  return (u32x4){cvtpa(a[o] * s, a[o + 1] * s), cvtpa(a[o + 2] * s, a[o + 3] * s), cvtpa(a[o + 4] * s, a[o + 5] * s), cvtpa(a[o + 6] * s, a[o + 7] * s)}; }
DI u32x4 glu8(const f32x16& a, float nc, float sd) {
  float h[8];
#pragma unroll
  for (int i = 0; i < 8; ++i) { const float e = __builtin_amdgcn_exp2f(a[i] * nc); h[i] = a[8 + i] * __builtin_amdgcn_rcpf(__builtin_fmaf(e, sd, sd)); }
  return (u32x4){cvtpa(h[0], h[1]), cvtpa(h[2], h[3]), cvtpa(h[4], h[5]), cvtpa(h[6], h[7])}; }

typedef unsigned long long u64x8 __attribute__((ext_vector_type(8)));
__global__ __launch_bounds__(512, 2) void fused_block(const void* a_x, const void* a_wqkv, const void* a_wo, const void* a_wg, const void* a_wu, const void* a_wd, void* a_out, void* a_ws) {
  extern __shared__ __attribute__((aligned(16))) char smem[];
  u64x8 arg;
  asm volatile("s_load_dwordx16 %0, %1, 0x0" : "=s"(arg) : "s"(__builtin_amdgcn_kernarg_segment_ptr()));
  unsigned long long dg0; unsigned dg1 = 0; asm volatile("s_memrealtime %0" : "=s"(dg0) :: "memory");
  const lptr lds = (lptr)smem;
  const unsigned lds0 = (unsigned)(size_t)lds;
  const int tid = threadIdx.x, lane = tid & 63, w = __builtin_amdgcn_readfirstlane(tid >> 6);
  const int b = blockIdx.x, xcd = b & 7, bi = b >> 3;
  const int r32 = lane & 31, hi = lane >> 5;
  if (w == 0) {
    unsigned long long pc; unsigned len; asm volatile("s_getpc_b64 %0\n.Lfb_pc:\n\ts_mov_b32 %1, .Lfb_end-.Lfb_pc" : "=s"(pc), "=s"(len));
    const rsrc_t rc = mk((const void*)pc, len);
    const unsigned v = lane < 8 ? (bi * 8 + lane) * 128 : 0x7fffff00u;
    asm volatile("s_mov_b32 m0, %0\n\ts_nop 0\n\tbuffer_load_dword %1, %2, 0 offen lds" :: "s"(lds0 + LDS_BYTES - 256), "v"(v), "s"(rc) : "memory"); }
  const int rb = (xcd & 1) * 8 + (bi & 7), Q4 = xcd >> 1, sj = bi >> 3, set = Q4 * 4 + sj;
  const int rot = 2 * (xcd & 1) + (bi & 4), pos = (bi >> 1) & 1, half = bi & 1, kpart = rot + pos;
  const unsigned l3 = lane >> 3, l7 = lane & 7, l16 = lane * 16;
  const int wx = (w + 2 * sj) & 7, ww = (w + 2 * bi) & 7;
  unsigned vx0 = (rb * 128 + wx * 16 + l3) * 1024 + ((l7 ^ (lane >> 4)) * 16), vx1 = (vx0 + 8192) ^ 64;
  unsigned mx = lds0 + (wx >> 2) * UD + (wx & 3) * 2048, mw = lds0 + ww * 1024;
  const bool tb_ = sj == 0;
  const int p1 = 7 * Q4 + 2 * sj - 1, p2 = p1 + 1;
  const unsigned col0 = 128 * Q4 + (tb_ ? 1024 : sj * 512 - 448);
  const unsigned col1 = tb_ ? 512 + 128 * Q4 : 1536 + 64 * p1, col2 = tb_ ? 128 * Q4 : 3328 + 64 * p1;
  const unsigned str12 = tb_ ? 3072 : 3584, wr = ww * 8 + l3, wpc = (l7 ^ ((lane >> 2) & 4)) * 16;
  unsigned vg0 = wr * 3072 + wpc + col0 * 2, vg1 = wr * str12 + wpc + (tb_ ? col1 * 2 : p1 * 128), vg2 = (vg1 - (tb_ ? 1024 : 0)) ^ 32;
  unsigned so0 = rot * 128, so1 = rot * (64 * 3072), so2 = rot * 64 * str12;
  PIN(vx0); PIN(vx1); PIN(vg0); PIN(vg1); PIN(vg2); asm volatile("" : "+s"(mx), "+s"(mw), "+s"(so0), "+s"(so1), "+s"(so2));
  SBAR();
  asm volatile("s_waitcnt lgkmcnt(0)" : "+s"(arg), "+s"(dg0) :: "memory");
  const void *px = (const void*)arg[0], *pwqkv = (const void*)arg[1], *pwo = (const void*)arg[2], *pwg = (const void*)arg[3], *pwu = (const void*)arg[4], *pwd = (const void*)arg[5];
  void *pout = (void*)arg[6], *pws = (void*)arg[7];
  const rsrc_t ws = mk(pws, WS_END), rx = mk(px, 2048 * 1024), rg0 = mk(pwqkv, 512 * 3072);
  const rsrc_t rg1 = mk(tb_ ? pwqkv : pwg, 512 * str12), rg2 = mk(tb_ ? pwqkv : pwu, 512 * str12);
  const rsrc_t rg3 = mk(pwg, 512 * 3584), rg4 = mk(pwu, 512 * 3584);
#define KC(c) (unsigned)(((c) + rot) & 7)
#define SL(c) (((c) % 3) * 8192)
#define DMA1(c, i) do { \
    if ((i) == 0) dma(mx, SL(c), vx0, rx, KC(c) * 128); if ((i) == 1) dma(mx, SL(c) + 1024, vx1, rx, KC(c) * 128); \
    if ((i) == 2) dma(mw, SL(c) + U_G0, vg0, rg0, KC(c) * (64 * 3072)); if ((i) == 3) dma(mw, SL(c) + U_G1, vg1, rg1, KC(c) * 64 * str12); \
    if ((i) == 4) dma(mw, SL(c) + U_G2, vg2, rg2, KC(c) * 64 * str12); } while (0)
#define M01(c, i) dma_m0((i) < 2 ? mx : mw, SL(c) + ((i) == 0 ? 0 : (i) == 1 ? 1024 : (i) == 2 ? U_G0 : (i) == 3 ? U_G1 : U_G2))
#define GO1(c, i) do { if ((i) == 0) dma_go(vx0, rx, KC(c) * 128); if ((i) == 1) dma_go(vx1, rx, KC(c) * 128); if ((i) == 2) dma_go(vg0, rg0, KC(c) * (64 * 3072)); \
    if ((i) == 3) dma_go(vg1, rg1, KC(c) * 64 * str12); if ((i) == 4) dma_go(vg2, rg2, KC(c) * 64 * str12); } while (0)
#define M02(c, i) dma_m0(mw, SL(c) + ((i) & 2 ? U_XA : U_G1) + ((i) & 1) * UD)
#define GO2(c, i) do { const unsigned kc_ = KC(2 * ((c) - 8) + ((i) >> 1)) * (64 * 3584); if ((i) & 1) dma_go(vg4, rg4, kc_); else dma_go(vg3, rg3, kc_); } while (0)
#define DMA2(c, i) do { constexpr int sl_ = SL(c) + ((i) & 2 ? U_XA : U_G1); const unsigned kc_ = KC(2 * ((c) - 8) + ((i) >> 1)) * (64 * 3584); \
    if ((i) & 1) dma(mw, sl_ + UD, vg4, rg4, kc_); else dma(mw, sl_, vg3, rg3, kc_); } while (0)
  dma(mx, 0, vx0, rx, so0); dma(mx, 1024, vx1, rx, so0); SBAR(); dma(mw, U_G0, vg0, rg0, so1); SBAR(); dma(mw, U_G1, vg1, rg1, so2); dma(mw, U_G2, vg2, rg2, so2); SBAR();
  STAMP(47);
  unsigned vg3 = wr * 3584 + wpc + p2 * 128, vg4 = vg3 ^ 32; PIN(vg3); PIN(vg4);
  const int tt = w & 3, cg = w >> 2;
  const int q4 = (lane & 15) >> 2, p4 = lane & 3, g16 = (lane >> 4) & 1, swr = (q4 >> 1) * 4;
  const int wrow = (8 * hi + q4) * 128 + (p4 & 1) * 8;
  const int pq = 4 * cg + 2 * g16 + (p4 >> 1);
  const int gl0 = g16 * UD + (pq ^ swr) * 16, gl1 = gl0 ^ 32;
  lptr wp0 = lds + U_G0 + wrow + (pq ^ swr) * 16;
  lptr wp1 = lds + wrow + (tb_ ? (pq ^ swr) * 16 : gl0);
  lptr wp2 = lds + wrow + (tb_ ? UD + (pq ^ swr ^ 2) * 16 : gl1);
  lptr xp[4];
#pragma unroll
  for (int s = 0; s < 4; ++s) { xp[s] = lds + (tt >> 1) * UD + (tt & 1) * 4096 + r32 * 128 + (((2 * s + hi) ^ ((r32 >> 1) & 7)) * 16); PIN(xp[s]); }
  PIN(wp0); PIN(wp1); PIN(wp2);
  const int ah = xcd * 4 + (bi & 3), aq = bi >> 2, kh = w >> 2;
  const int qown = aq * 8 + 2 * (w & 3) + kh;
  const unsigned csb = WS_CS + set * 10240;
  auto colsum = [&](lptr cp, unsigned vo, unsigned so) {
    const __bf16 o1 = (__bf16)1.0f; const bf16x8 ones = {o1, o1, o1, o1, o1, o1, o1, o1};
    const bf16x8 f0 = trfrag(cp, 512), f1 = trfrag(cp + 2048, 512), f2 = trfrag(cp + 4096, 512), f3 = trfrag(cp + 6144, 512);
    const f32x16 cs = MFMA(ones, f3, MFMA(ones, f2, MFMA(ones, f1, MFMA(ones, f0, (f32x16){}))));
    const float ch = rbf(cs[0]);
    st4(cvtpk(ch, cs[0] - ch), ws, l16 < 512 ? vo : 0x7fffff00u, csb + so + (kpart >> 2) * 512 + (kpart & 3) * 4);
  };
  unsigned vf0 = 0;
  u32x2 ep; u32x4 xr; unsigned e0, e1; unsigned long long tag;
  const unsigned srow = rb * 128 + set * 8 + w;
  float sv[8], sa, sb, smean;
  auto swapadd16 = [](float v) { auto r = __builtin_amdgcn_permlane16_swap(__float_as_uint(v), __float_as_uint(v), false, false); return __uint_as_float(r[0]) + __uint_as_float(r[1]); };
  auto swapadd32 = [](float v) { auto r = __builtin_amdgcn_permlane32_swap(__float_as_uint(v), __float_as_uint(v), false, false); return __uint_as_float(r[0]) + __uint_as_float(r[1]); };
#define STATS(i) do { \
    if ((i) == 0 || (i) == 1) { sv[4 * ((i) & 1)] = bflo(xr[2 * ((i) & 1)]); sv[4 * ((i) & 1) + 1] = bfhi(xr[2 * ((i) & 1)]); sv[4 * ((i) & 1) + 2] = bflo(xr[2 * ((i) & 1) + 1]); sv[4 * ((i) & 1) + 3] = bfhi(xr[2 * ((i) & 1) + 1]); \
      PIN(sv[4 * ((i) & 1)]); PIN(sv[4 * ((i) & 1) + 1]); PIN(sv[4 * ((i) & 1) + 2]); PIN(sv[4 * ((i) & 1) + 3]); } \
    if ((i) == 2) { sa = (sv[0] + sv[1]) + sv[2]; PIN(sa); sb = (sv[3] + sv[4]) + sv[5]; PIN(sb); } \
    if ((i) == 3) { sa = ((sa + sv[6]) + sv[7]) + sb; PIN(sa); } \
    if ((i) == 4 || (i) == 14) { sa = dppadd<0x121>(sa); PIN(sa); } if ((i) == 5 || (i) == 15) { sa = dppadd<0x122>(sa); PIN(sa); } \
    if ((i) == 6 || (i) == 16) { sa = dppadd<0x124>(sa); PIN(sa); } if ((i) == 7 || (i) == 17) { sa = dppadd<0x128>(sa); PIN(sa); } \
    if ((i) == 8 || (i) == 18) { sa = swapadd16(sa); PIN(sa); } \
    if ((i) == 9) { smean = swapadd32(sa) * (1.f / 512); PIN(smean); sa = 0.f; } \
    if ((i) >= 10 && (i) <= 13) { const float d_ = sv[(2 * (i) - 20) & 7] - smean, e_ = sv[(2 * (i) - 19) & 7] - smean; sa = __builtin_fmaf(e_, e_, __builtin_fmaf(d_, d_, sa)); PIN(sa); } \
    if ((i) == 19) { sa = rbf(swapadd32(sa) * (1.f / 512)); PIN(sa); } \
    if ((i) == 20) { sa = __builtin_amdgcn_sqrtf(rbf(sa + 1e-5f)); PIN(sa); } \
    if ((i) == 21) st4(cvtpk(-smean, sa), ws, lane ? 0x7fffff00u : 0u, WS_STAT + srow * 4); } while (0)
  bf16x8 xf[32], wa[3], wb[3];
  f32x16 acc0 = {}, acc1 = {}, acc2 = {};
  u32x2 pl; unsigned stat; u32x4 cs0, cs1, cs2;
#define RD1(W, ks) do { xf[ks] = LDS8(xp[(ks) & 3] + SL((ks) >> 2)); W[0] = trfrag(wp0 + SL((ks) >> 2) + ((ks) & 3) * 2048, 512); } while (0)
#define RD1B(W, ks) W[1] = trfrag(wp1 + U_G1 + SL((ks) >> 2) + ((ks) & 3) * 2048, 512)
#define RD1C(W, ks) W[2] = trfrag(wp2 + U_G1 + SL((ks) >> 2) + ((ks) & 3) * 2048, 512)
#define RD2A(W, ks) W[0] = trfrag(wp1 + SL(8 + ((ks) >> 3)) + ((ks) & 4 ? U_XA : U_G1) + ((ks) & 3) * 2048, 512)
#define RD2B(W, ks) W[1] = trfrag(wp2 + SL(8 + ((ks) >> 3)) + ((ks) & 4 ? U_XA : U_G1) + ((ks) & 3) * 2048, 512)
  const int ks2 = bi + 32 * ((w + 7) & 7);
#define W2LOAD() do { if (ks2 < 144) { const rsrc_t r_ = mk(ks2 < 112 ? pwd : pwo, ks2 < 112 ? 1792 * 1024 : 512 * 1024); const unsigned so_ = (ks2 < 112 ? ks2 : ks2 - 112) * 16384 + xcd * 128; \
      LANE(n_); const unsigned v_ = (n_ >> 3) * 1024 + ((n_ & 7) ^ ((n_ >> 2) & 4)) * 16; \
      dma_nt(lds0 + w * 2048, L_PRIV, v_, r_, so_); dma_nt(lds0 + w * 2048, L_PRIV + 1024, v_, r_, so_ + 8192); } } while (0)
#define FLAG(f) PUT(tag, (f) + (set * 16 + rb) * FS, w)
  if (w < 5) { const unsigned o_ = (tid & 11) == 0 ? 0x3f803f80u : 0u;
    *(__attribute__((address_space(3))) u32x4*)(lds + L_ONES + tid * 16) = (u32x4){o_, o_, o_, o_}; }
  else if (w < 7) {
    const unsigned one_ = hi == ((lane >> 2) & 1) ? 0x3f80u << ((lane & 1) * 16) : 0u, dw_ = ((lane >> 1) & 1) + ((lane >> 2) & 6) - 4 * (w - 5);
    *(__attribute__((address_space(3))) u32x4*)(lds + L_ONES + tid * 16) = (u32x4){dw_ == 0 ? one_ : 0u, dw_ == 1 ? one_ : 0u, dw_ == 2 ? one_ : 0u, dw_ == 3 ? one_ : 0u}; }
  SBAR();
  if (X_ST != 1) asm volatile("s_barrier" ::: "memory");
  STAMP(48);
  DMA1(1, 0); DMA1(1, 1); if (X_ST != 2) { DMA1(1, 2); DMA1(1, 3); DMA1(1, 4); }
  STAMP(1);
  WAITBAR(X_ST == 2 ? 2 : 5);
  STAMP(2);
  RD1(wa, 0); RD1B(wa, 0); RD1C(wa, 0);
#define STATG(g) do { STATS(2 * ((g) - 12)); STATS(2 * ((g) - 12) + 1); } while (0)
  if (X_ST == 2) { DMA1(1, 2); DMA1(1, 3); DMA1(1, 4); }
  DMA1(2, 0); DMA1(2, 4);
#define STEP1(ks, WC, WN) do { constexpr int c_ = ((ks) + 1) >> 2, s_ = ((ks) + 1) & 3; \
    if (s_ == 0) { SBAR(); \
      if ((c_ == 1 || c_ == 2) && w < 3 && pos == c_ - 1) \
        colsum((lptr)((unsigned)(size_t)(w == 0 ? wp0 : w == 1 ? wp1 + U_G1 : wp2 + U_G1) ^ (half * 64)) + SL(c_ - 1), l16, (half * 5 + w) * 1024); \
      if (c_ == 1) { const unsigned z_ = 0; ALD4(xr, l16, rx, srow * 1024); CLD("buffer_load_dwordx2 %0, %1, %2, %3 offen sc1", ep, z_, ws, WS_EPOCH, 1); } \
      if (c_ == 2) st16((u32x4){~0u, ~0u, ~0u, ~0u}, ws, l16, WS_O + (qown * 32 + ah) * 1024);     \
      if (c_ == 5 && (w == 1 || w == 2)) colsum((lptr)((unsigned)(size_t)wp0 ^ ((w - 1) * 64)) + (L_XCS - U_G0), (l16 & 240) + (l16 & 256) * 4, ((w - 1) * 5 + 3) * 1024 + half * 256); \
      if (c_ == 5) W2LOAD(); \
      if (c_ == 6 && w == 0) { LANE(n_); vf0 = WS_F0 + (n_ < 16 ? set * 16 + n_ : (n_ & 15) * 16 + rb) * FS; PIN(vf0); } \
      if (c_ == 7) WAITF(4); \
      STAMP(16 + 2 * c_); if (c_ == 5 && ks2 < 144) WAITV(7); else WAITV(c_ <= 2 ? 6 : c_ < 7 ? 5 : c_ == 7 ? 4 : 8); LBAR(); STAMP(17 + 2 * c_); \
      if (c_ == 2) { PIN(ep); PIN(xr); e0 = __builtin_amdgcn_readfirstlane(ep[0]); e1 = __builtin_amdgcn_readfirstlane(ep[1]);     \
        tag = (unsigned long long)((e1 ^ 0x85EBCA6Bu) + e0) << 32 | (e0 * 0x9E3779B1u + 0x7F4A7C15u); }     \
      if (c_ == 3) dma(mw, L_XCS, vg3, (half ? rg4 : rg3), kpart * (64 * 3584)); \
      if (c_ == 4) FLAG(WS_F0); \
      if (c_ == 6) { FLAG(WS_F3); POLL(vf0); } \
      if (c_ == 7) { const unsigned v_ = (l16 & 496) >> 2; ALD1C(stat, v_, ws, WS_STAT + (rb * 4 + tt) * 128); \
        ALD4C(cs0, l16, ws, csb + cg * 5120); ALD4C(cs1, l16, ws, csb + cg * 5120 + 1024); ALD4C(cs2, l16, ws, csb + cg * 5120 + 2048); } \
      if (c_ == 8) { wp1 = wp0 - U_G0 + (l16 & 256) * (UD / 256); wp2 = (lptr)((unsigned)(size_t)wp1 ^ 32); PIN(wp1); PIN(wp2); vf0 = WS_F3 + set * 16 * FS + (l16 & 240) * (FS / 16); POLL(vf0); } } \
    if (X_P1 == 1) __builtin_amdgcn_s_setprio(3 - s_); if (X_P1 == 2 && (s_ == 0 || s_ == 3)) __builtin_amdgcn_s_setprio(s_ == 0); \
    SBAR(); \
    if (c_ <= 5) M01(c_ + 2, s_); else M02(c_ + 2, s_); \
    if ((ks) < 31) RD1(WN, ((ks) + 1) & 31); else RD2A(WN, 0); \
    if (c_ <= 5) GO1(c_ + 2, s_); else GO2(c_ + 2, s_); SBAR(); \
    acc0 = MFMA(WC[0], xf[ks], acc0); SBAR(); STATG(3 * ((ks) - 3)); \
    if (c_ == 8) M02(10, 1); \
    if ((ks) < 31) RD1B(WN, ((ks) + 1) & 31); else RD2B(WN, 0); \
    if (c_ == 8) GO2(10, 1); SBAR(); \
    acc1 = MFMA(WC[1], xf[ks], acc1); SBAR(); STATG(3 * ((ks) - 3) + 1); \
    if (c_ >= 1 && c_ <= 5 && s_ == 0) M01(c_ + 2, 4); \
    if ((ks) < 31) RD1C(WN, ((ks) + 1) & 31); \
    if (c_ >= 1 && c_ <= 5 && s_ == 0) GO1(c_ + 2, 4); if (c_ == 8) DMA2(10, 2); SBAR(); \
    acc2 = MFMA(WC[2], xf[ks], acc2); SBAR(); STATG(3 * ((ks) - 3) + 2); SBAR(); } while (0)
#define STEP1x4(k) STEP1(k, wa, wb); STEP1(k + 1, wb, wa); STEP1(k + 2, wa, wb); STEP1(k + 3, wb, wa)
  STEP1x4(0); STEP1x4(4); STEP1x4(8); STEP1x4(12); STEP1x4(16); STEP1x4(20); STEP1x4(24); STEP1x4(28);
  DMA2(10, 3);
  if (X_P1) __builtin_amdgcn_s_setprio(0);
  asm volatile("s_waitcnt vmcnt(8)" : "+v"(stat), "+v"(cs0), "+v"(cs1), "+v"(cs2) :: "memory");
  STAMP(3);
  unsigned nm2 = (stat & 0xffffu) * 0x10001u;
  bf16x8 nmf = __builtin_bit_cast(bf16x8, (u32x4){nm2, nm2, nm2, nm2});
  acc0 = MFMA(__builtin_bit_cast(bf16x8, cs0), nmf, acc0); acc1 = MFMA(__builtin_bit_cast(bf16x8, cs1), nmf, acc1); acc2 = MFMA(__builtin_bit_cast(bf16x8, cs2), nmf, acc2);
  const float sd = bfhi(stat), rstd = __builtin_amdgcn_rcpf(sd), gsc = -1.4426950408889634f * rstd, qsc = rstd * (0.25f * 1.4426950408889634f), sc0 = sj == 1 ? qsc : rstd;
  const int tile = rb * 4 + tt, hd = 8 * Q4 + (tb_ ? 0 : 4) + 2 * cg;
  const unsigned sok = WS_KV + hd * 131072 + tile * 1024, soq = WS_Q + (hd * 64 + tile) * 1024, isv = (sj + 1) & 2;
  unsigned l16k = (l16 & 0x330) | (l16 & 64) * 2 | (l16 & 128) / 2; if (sj == 1) l16k = l16; PIN(l16k);
  SBAR();
  STAMP(4);
  u32x4 qf0, qf1, hp, hq; bf16x8 eye0, eye1;
  f32x16 acc3 = {}, acc4 = {};
  auto glu2 = [&](const f32x16& a, int i) {
    const float e0_ = __builtin_amdgcn_exp2f(a[i] * gsc), e1_ = __builtin_amdgcn_exp2f(a[i + 1] * gsc);
    return cvtpa(a[8 + i] * __builtin_amdgcn_rcpf(__builtin_fmaf(e0_, sd, sd)), a[9 + i] * __builtin_amdgcn_rcpf(__builtin_fmaf(e1_, sd, sd))); };
  const unsigned mk_ = lds0 + ((w + aq) & 7) * 1024, skv = WS_KV + ah * 131072 + ((w + aq) & 7) * 1024;
#define KVD(i) dma_sc1(mk_, (i) * 8192 + ((i) >= 8 ? L_V - 65536 : 0), l16, ws, skv + (i) * 8192)
#define PRE2(ks, TA) do { constexpr int c_ = 8 + (((ks) + 1) >> 3), s_ = ((ks) + 1) & 7; \
    if (s_ == 0 && c_ < 12) { SBAR(); \
      if (c_ == 9) WAITF(6); \
      STAMP(16 + 2 * c_); if (c_ == 9) WAITV(6); else if (c_ == 10) WAITV2(3, 2);  \
      LBAR(); STAMP(17 + 2 * c_); } \
    if ((ks) == 12) { asm volatile("s_waitcnt vmcnt(4)\n\ts_barrier" ::: "memory"); FLAG(WS_F1); STAMP(51); } \
    if ((ks) == 21) { vf0 = WS_F1 + Q4 * 64 * FS + (l16 & (xcd & 1 ? 1008 : 240)) * (FS / 16); POLL(vf0); }     \
    if ((ks) == 27) { \
      WAITF(0); WAITV(0); PIN(cs0); PIN(cs1); \
      STAMP(52); asm volatile("s_barrier" ::: "memory"); STAMP(53); } \
    SBAR(); } while (0)
  float ga, gb;
#define GLUS(g) do { if ((g) >= 0 && (g) < 40) { const f32x16& a_ = (g) < 20 ? acc1 : acc2; constexpr int i_ = ((g) / 5 & 3) * 2, t_ = (g) % 5; \
      if (t_ == 0) { ga = a_[i_] * gsc; PIN(ga); gb = a_[i_ + 1] * gsc; } if (t_ == 1) { ga = __builtin_amdgcn_exp2f(ga); gb = __builtin_amdgcn_exp2f(gb); } \
      if (t_ == 2) { ga = __builtin_fmaf(ga, sd, sd); PIN(ga); gb = __builtin_fmaf(gb, sd, sd); } if (t_ == 3) { ga = __builtin_amdgcn_rcpf(ga); gb = __builtin_amdgcn_rcpf(gb); } \
      if (t_ < 4) { PIN(ga); PIN(gb); } \
      else if ((g) < 20) { hp[i_ / 2] = cvtpa(a_[8 + i_] * ga, a_[9 + i_] * gb); PIN(hp); } else { hq[i_ / 2] = cvtpa(a_[8 + i_] * ga, a_[9 + i_] * gb); PIN(hq); } } } while (0)
#define STEP2(ks, TA, WC, WN) do { constexpr int c_ = 8 + (((ks) + 1) >> 3), s_ = ((ks) + 1) & 7; \
    if (c_ == 9 && s_ < 4) M02(11, s_); \
    if ((ks) < 31) RD2A(WN, ((ks) + 1) & 31); \
    if (s_ == 7) RD2B(WN, ((ks) + 1) & 31); \
    if (c_ == 9 && s_ < 4) GO2(11, s_); \
    if ((ks) == 13) ALD4C(cs0, l16, ws, csb + cg * 5120 + 3072); if ((ks) == 14) ALD4C(cs1, l16, ws, csb + cg * 5120 + 4096); \
    if ((ks) == 27) KVD(0); if ((ks) == 28) KVD(8); if ((ks) == 29) ALD4C(qf1, l16, ws, WS_Q + (ah * 64 + qown) * 1024); SBAR(); \
    acc3 = MFMA(WC[0], xf[ks], acc3); SBAR(); if (X_FINE) GLUS(2 * (ks) - 8); \
    if ((ks) < 31 && s_ != 7) RD2B(WN, ((ks) + 1) & 31); \
    if ((ks) == 27) KVD(4); if ((ks) == 28) KVD(12); if ((ks) == 29) ALD4C(qf0, l16, ws, WS_Q + (ah * 64 + (qown ^ 1)) * 1024); SBAR(); \
    acc4 = MFMA(WC[1], xf[ks], acc4); SBAR(); if (X_FINE) GLUS(2 * (ks) - 7); \
    if ((ks) < 2) { const u32x4 p_ = pack8(acc0, ((ks) & 1) * 8, sc0); if ((ks) == 0) hp = p_; else hq = p_; \
      if (isv) st16(p_, ws, l16k, sj == 1 ? soq + ((ks) & 1) * 65536 : sok + ((ks) & 1) * 131072); } \
    if ((ks) == 1) { eye0 = LDS8(lds + L_EYE + l16); eye1 = LDS8(lds + L_EYE + 1024 + l16); } \
    if ((ks) == 2 && !isv) acc0 = MFMA(__builtin_bit_cast(bf16x8, hq), eye1, MFMA(__builtin_bit_cast(bf16x8, hp), eye0, (f32x16){})); \
    if ((ks) == 3 && !isv) { LANE(n_); const unsigned vvo = (n_ & 16) * 8192 + ((n_ >> 5) * 16 + (n_ & 15)) * 16; \
      st16((u32x4){cvtpk(acc0[0], acc0[1]), cvtpk(acc0[2], acc0[3]), cvtpk(acc0[4], acc0[5]), cvtpk(acc0[6], acc0[7])}, ws, vvo, sok + 65536); \
      st16((u32x4){cvtpk(acc0[8], acc0[9]), cvtpk(acc0[10], acc0[11]), cvtpk(acc0[12], acc0[13]), cvtpk(acc0[14], acc0[15])}, ws, vvo, sok + 65536 + 512); } \
    if ((ks) >= 4 && (ks) <= 6 && tb_) st16(pack8((ks) < 6 ? acc1 : acc2, ((ks) & 1) * 8, (ks) < 6 ? rstd : qsc), ws, (ks) < 6 ? l16k : l16, (ks) < 6 ? sok + ((ks) & 1) * 131072 : soq); \
    if ((ks) == 6 && tb_) st16(pack8(acc2, 8, qsc), ws, l16, soq + 65536); \
    if (X_FINE && (ks) == 16 && !tb_) st16(hp, ws, l16, WS_H + (tile * 112 + 4 * p1 + 2 * cg) * 1024); \
    if (X_FINE && (ks) == 30 && !tb_) st16(hq, ws, l16, WS_H + (tile * 112 + 4 * p1 + 2 * cg + 1) * 1024); \
    if (!X_FINE && (ks) >= 4 && (ks) <= 20 && !((ks) & 1) && !tb_) { \
      if ((ks) == 16) st16(hp, ws, l16, WS_H + (tile * 112 + 4 * p1 + 2 * cg) * 1024); \
      if ((ks) == 20) st16(hq, ws, l16, WS_H + (tile * 112 + 4 * p1 + 2 * cg + 1) * 1024); \
      else if ((ks) < 12) { hp[(((ks) >> 1) + 2) & 3] = glu2(acc1, ((ks) + 4) & 6); PIN(hp); } \
      else { hq[(((ks) >> 1) + 2) & 3] = glu2(acc2, ((ks) + 4) & 6); PIN(hq); } } \
    if ((ks) == 26) { PIN(nm2); nmf = __builtin_bit_cast(bf16x8, (u32x4){nm2, nm2, nm2, nm2}); PIN(nmf); } \
    if ((ks) == 29) { acc3 = MFMA(__builtin_bit_cast(bf16x8, cs0), nmf, acc3); acc4 = MFMA(__builtin_bit_cast(bf16x8, cs1), nmf, acc4); } \
    SBAR(); PRE2((ks) + 1, TA); } while (0)
#define STEP2x4(k, TA) STEP2(k, TA, wa, wb); STEP2(k + 1, TA, wb, wa); STEP2(k + 2, TA, wa, wb); STEP2(k + 3, TA, wb, wa)
#define MID2(TA) STEP2x4(4, TA); STEP2x4(8, TA); STEP2x4(12, TA); STEP2x4(16, TA); STEP2x4(20, TA); STEP2(24, TA, wa, wb); STEP2(25, TA, wb, wa); STEP2(26, TA, wa, wb)
  STEP2x4(0, 0);
  MID2(0);
  STEP2(27, 0, wb, wa); STEP2x4(28, 0);
  STAMP(5);
  {
    LANE(n_); const lptr s2_ = lds + L_PRIV + w * 2048 + (4 * (n_ >> 5) + ((n_ >> 2) & 3)) * 128 + (n_ & 1) * 8; const int pc_ = (((n_ >> 3) & 2) + ((n_ >> 1) & 1)) ^ ((n_ >> 1) & 4);
    hp = __builtin_bit_cast(u32x4, trfrag(s2_ + pc_ * 16, 1024)); hq = __builtin_bit_cast(u32x4, trfrag(s2_ + (pc_ ^ 4) * 16, 1024)); }
  st16(glu8(acc3, gsc, sd), ws, l16, WS_H + (tile * 112 + 4 * p2 + 2 * cg) * 1024);
  LANE(ln); const int r3 = ln & 31, hf = ln >> 5;
  const int tb = xcd * 4 + (bi & 3), n64 = bi >> 2;
  lptr kp = lds + kh * 32768 + l16, kq = lds + (kh ^ 1) * 32768 + l16;
  const bool vrow = r3 < 16;
  int vstep = vrow ? 4096 : 0;
  lptr vp = lds + (vrow ? L_V + kh * 32768 + hf * 256 : L_ONES - 256) + r3 * 16;
  int idx = 0xDDDDDDDD;
  float aone = hf ? 0.f : 1.f;
  PIN(kp); PIN(kq); PIN(vstep); PIN(vp); PIN(idx); PIN(aone);
  f32x16 nm0, nm1, o0, o1, sA, sB;
  const f32x16 z = {};
  { u32x4 zz = {}; PIN(zz); o0 = MFMA(__builtin_bit_cast(bf16x8, zz), __builtin_bit_cast(bf16x8, zz), z); PIN(o0); o1 = MFMA(__builtin_bit_cast(bf16x8, zz), __builtin_bit_cast(bf16x8, zz), o0); PIN(o1); }
  STAMP(6);
  WAITBAR(1);
  PIN(qf0); PIN(qf1);
  STAMP(7);
  const bf16x8 q0 = __builtin_bit_cast(bf16x8, qf0), q1 = __builtin_bit_cast(bf16x8, qf1);
  auto hmax = [&](float m) { auto rr = __builtin_amdgcn_permlane32_swap(__float_as_uint(m), __float_as_uint(m), false, false);
    return __builtin_fmaxf(__uint_as_float(rr[0]), __uint_as_float(rr[1])); };
  auto max16 = [&](float m, const f32x16& s) {
#pragma unroll
    for (int i = 0; i < 16; i += 2) m = __builtin_fmaxf(__builtin_fmaxf(m, s[i]), s[i + 1]);
    return m; };
  __builtin_amdgcn_s_setprio(1);
  bf16x8 k1 = LDS8(kp), va, vb;
  {
    const bf16x8 k0 = LDS8(kq);
    va = LDS8(vp); SBAR();
    if (ks2 < 144) { st16(hp, ws, l16, WS_W2 + (2 * xcd * 144 + ks2) * 1024); st16(hq, ws, l16, WS_W2 + ((2 * xcd + 1) * 144 + ks2) * 1024); }
    hp[0] = glu2(acc4, 0); PIN(hp); SBAR();
    const f32x16 a2 = MFMA(k1, q0, z), a0 = MFMA(k0, q0, z); SBAR();
    hp[1] = glu2(acc4, 2); PIN(hp); SBAR();
    const f32x16 a3 = MFMA(k1, q1, z), a1 = MFMA(k0, q1, z); SBAR();
    float x0 = a0[0], x2 = a2[0];
#pragma unroll
    for (int i = 1; i < 15; i += 2) { x2 = max3(x2, a2[i], a2[i + 1]); x0 = max3(x0, a0[i], a0[i + 1]); }
    nm0 = __builtin_amdgcn_mfma_f32_32x32x2f32(aone, -REF_OFF - hmax(max3(max3(x0, x2, a0[15]), a2[15], a2[15])), z, 0, 0, 0); SBAR();
    float x1 = a1[0], x3 = a3[0];
#pragma unroll
    for (int i = 1; i < 15; i += 2) { x3 = max3(x3, a3[i], a3[i + 1]); x1 = max3(x1, a1[i], a1[i + 1]); }
    float m1 = -REF_OFF - hmax(max3(max3(x1, x3, a1[15]), a3[15], a3[15])); PIN(m1); SBAR();
    sA = MFMA(k1, q0, nm0); SBAR();
    hp[2] = glu2(acc4, 4); PIN(hp); SBAR();
    nm1 = __builtin_amdgcn_mfma_f32_32x32x2f32(aone, m1, z, 0, 0, 0); SBAR();
    hp[3] = glu2(acc4, 6); PIN(hp);
    st16(hp, ws, l16, WS_H + (tile * 112 + 4 * p2 + 2 * cg + 1) * 1024); SBAR();
  }
  f32x8 pa, pb;
  f32x16 g00, g01, g10, g11;
  bf16x8 fa[4], fb[4];
  unsigned sow = WS_W2 + (2 * n64 * 144 + 14 * w) * 1024, soh = WS_H + ((2 * tb + kh) * 112 + 14 * w) * 1024;
  const unsigned dh = kh ? -112 * 1024 : 112 * 1024;
#define EX(P, S, i) P[(i) & 7] = __builtin_amdgcn_exp2f(S[i])
#define T2(u, A0, A1, B0, B1) T2X(o1 = SMF(vb, pb, o1, idx), u, A0, A1, B0, B1)
#define T2X(FIN, u, A0, A1, B0, B1) do { \
    EX(pa, sA, 0); EX(pa, sA, 1); SBAR(); FIN; SBAR(); vb = LDS8(vp + (u) * 1024 + 512); SBAR(); \
    EX(pa, sA, 2); EX(pa, sA, 3); EX(pa, sA, 4); EX(pa, sA, 5); SBAR(); sB = MFMA(k1, q1, nm1); SBAR(); k1 = LDS8(kp + ((u) + 1) * 1024); SBAR(); \
    EX(pa, sA, 6); EX(pa, sA, 7); EX(pb, sA, 8); EX(pb, sA, 9); SBAR(); o0 = SMF(va, pa, o0, idx); SBAR(); \
    EX(pb, sA, 10); EX(pb, sA, 11); EX(pb, sA, 12); EX(pb, sA, 13); SBAR(); A0; A1; SBAR(); EX(pb, sA, 14); EX(pb, sA, 15); SBAR(); \
    EX(pa, sB, 0); EX(pa, sB, 1); SBAR(); o0 = SMF(vb, pb, o0, idx); SBAR(); \
    EX(pa, sB, 2); EX(pa, sB, 3); EX(pa, sB, 4); EX(pa, sB, 5); SBAR(); sA = MFMA(k1, q0, nm0); SBAR(); \
    EX(pa, sB, 6); EX(pa, sB, 7); EX(pb, sB, 8); EX(pb, sB, 9); SBAR(); o1 = SMF(va, pa, o1, idx); SBAR(); va = LDS8(vp + ((u) + 1) * 1024); SBAR(); \
    EX(pb, sB, 10); EX(pb, sB, 11); EX(pb, sB, 12); EX(pb, sB, 13); SBAR(); B0; B1; SBAR(); EX(pb, sB, 14); EX(pb, sB, 15); SBAR(); } while (0)
#define NOP_ do {} while (0)
#define ADV() do { kp += 4096; vp += vstep; } while (0)
#define G2P(u, C) do { \
    T2(u, g00 = MFMA(C[0], C[2], g00), NOP_, g10 = MFMA(C[1], C[2], g10), { SBAR(); C[2] = ld16c(ws, l16, soh); }); \
    T2(u + 1, g11 = MFMA(C[1], C[3], g11), { SBAR(); C[1] = ld16c(ws, l16, sow + 144 * 1024); }, g01 = MFMA(C[0], C[3], g01), { SBAR(); C[0] = ld16c(ws, l16, sow); C[3] = ld16c(ws, l16, soh + dh); }); \
    sow += 1024; soh += 1024; } while (0)
#define PRIO_AT(k, n) asm volatile("s_cmp_lg_u32 %0, " #k "\n\ts_cbranch_scc1 1f\n\ts_setprio " #n "\n1:" :: "s"(it) : "scc")
#define PRIO(n) __builtin_amdgcn_s_setprio(n)
  T2X(NOP_, 0, KVD(1), KVD(5), KVD(9), NOP_); PRIO(0); T2(1, KVD(13), KVD(2), KVD(6), NOP_);
  STAMP(41);
  asm volatile("s_waitcnt vmcnt(6)\n\ts_barrier" ::: "memory");
  STAMP(42);
  FLAG(WS_F2);
  PUT((unsigned long long)e1 << 32 | (e0 + 1), WS_EPOCH, w | b);
  if (w == 0) vf0 = WS_F2 + (ln < 16 ? ln * 16 + (tb >> 1) : ((n64 >> 1) * 4 + ((ln >> 3) & 3)) * 16 + (n64 & 1) * 8 + (ln & 7)) * FS;
  PRIO(2); T2(2, KVD(10), KVD(14), KVD(3), NOP_); T2(3, KVD(7), KVD(11), KVD(15), NOP_); ADV();
  PRIO(1); T2(0, NOP_, NOP_, POLL(vf0), NOP_); PRIO(0); T2(1, NOP_, NOP_, NOP_, NOP_);
  STAMP(43);
  WAITF(0);
  STAMP(44);
  asm volatile("s_waitcnt vmcnt(0)\n\ts_barrier" ::: "memory");
  STAMP(8);
  const unsigned vout = (tb * 64 + w * 8 + (ln >> 3)) * 1024 + n64 * 128 + (ln & 7) * 16;
  u32x4 xres;
  PRIO(3);
  xres = __builtin_amdgcn_raw_buffer_load_b128(rx, vout, 0, 0);
  if (w < 2) {
    const unsigned v = ln * 128;
    asm volatile("s_mov_b32 m0, %0\n\ts_nop 0\n\tbuffer_load_dword %1, %2, %3 offen sc1 lds" :: "s"(lds0 + LDS_BYTES - 256), "v"(v), "s"(ws), "s"(WS_W2 + ((2 * n64 + w) * 144 + 112 + 8 * (bi & 3)) * 1024) : "memory"); }
  { bf16x8 zz = {};
#define GZ(g) do { PIN(zz); g = MFMA(zz, zz, z); PIN(g); SBAR(); } while (0)
    T2(2, GZ(g00), (fa[2] = ld16c(ws, l16, soh), fa[0] = ld16c(ws, l16, sow)), GZ(g10), (fa[1] = ld16c(ws, l16, sow + 144 * 1024), fa[3] = ld16c(ws, l16, soh + dh)));
    T2(3, GZ(g11), (fb[2] = ld16c(ws, l16, soh + 1024), fb[1] = ld16c(ws, l16, sow + 145 * 1024)), GZ(g01), (fb[0] = ld16c(ws, l16, sow + 1024), fb[3] = ld16c(ws, l16, soh + dh + 1024))); ADV(); }
  sow += 2048; soh += 2048;
#pragma unroll 1
  for (int it = 0; it < 5; ++it) { STAMPV(54 + it); PRIO_AT(2, 2); PRIO_AT(4, 1); G2P(0, fa); PRIO_AT(4, 0); G2P(2, fb); ADV(); }
  auto lwo_of = [](int g) { return (unsigned)(g < 3 ? g : g < 6 ? g + 1 : g + 3) * 8192; };
  const unsigned swo = WS_W2 + (2 * n64 * 144 + 112 + 4 * w) * 1024, lwo = lwo_of(w);
#define WOD(i) dma_sc1(lds0 + lwo, (i) * 1024, l16, ws, swo + ((i) & 3) * 1024 + ((i) >> 2) * (144 * 1024))
  STAMPV(59);
  asm volatile("s_barrier" ::: "memory");
  STAMPV(60);
  PRIO(2);
  bf16x8 fc[4];
  typedef __attribute__((address_space(3))) f32x4* l4p;
  typedef __attribute__((address_space(3))) float* l1p;
  lptr xw = lds + L_PRIV + w * 2048 + l16, xl = lds + 65536 + w * 256 + ln * 4; PIN(xw); PIN(xl);
#define GM(g, a, b) do { g = MFMA(a, b, g); PIN(g); SBAR(); } while (0)
  T2(0, GM(g00, fa[0], fa[2]), { WOD(0); WOD(1); WOD(2); }, GM(g10, fa[1], fa[2]), { WOD(3); WOD(4); fa[2] = ld16c(ws, l16, soh); });
  T2(1, GM(g11, fa[1], fa[3]), { WOD(5); WOD(6); fa[1] = ld16c(ws, l16, sow + 144 * 1024); }, GM(g01, fa[0], fa[3]), { WOD(7); fa[0] = ld16c(ws, l16, sow); fa[3] = ld16c(ws, l16, soh + dh); });
  PRIO(1);
  T2(2, fc[0] = ld16c(ws, l16, sow + 1024), fc[2] = ld16c(ws, l16, soh + 1024), fc[1] = ld16c(ws, l16, sow + 145 * 1024), fc[3] = ld16c(ws, l16, soh + dh + 1024));
  PRIO(0);
  auto xchg = [&]() { PIN(o0); *(l4p)(xw) = (f32x4){o0[0], o0[1], o0[2], o0[3]}; *(l4p)(xw + 1024) = (f32x4){o0[4], o0[5], o0[6], o0[7]}; *(l1p)(xl) = o0[8]; };
  T2(3, NOP_, NOP_, xchg(), NOP_);
  o1 = SMF(vb, pb, o1, idx);
  STAMP(9);
  xw = (lptr)((unsigned)(size_t)(xw - L_PRIV) ^ 8192) + L_PRIV; xl = (lptr)((unsigned)(size_t)xl ^ 1024); PIN(xw); PIN(xl); SBAR();
  LBAR();
  float oo[9];
  {
    const f32x4 a = *(l4p)(xw), c = *(l4p)(xw + 1024); const float lp = *(l1p)(xl);
    SBAR(); asm volatile("" :: "v"(o1));
    oo[8] = o1[8] + lp;
    float sm = 0.f;
#pragma unroll
    for (int i = 0; i < 4; ++i) { oo[i] = o1[i] + a[i]; oo[4 + i] = o1[4 + i] + c[i]; }
#pragma unroll
    for (int i = 0; i < 8; ++i) sm += __builtin_fabsf(oo[i]);
    const bool ok = !FORCE_FB && __builtin_amdgcn_fmed3f(oo[8], 0x1p-104f, 0x1p120f) == oo[8] && __builtin_amdgcn_fmed3f(sm, 0x1p-110f, 0x1p127f) == sm;
    if (__builtin_expect(__builtin_amdgcn_ballot_w64(!ok) != 0, 0)) {
      const unsigned sk = WS_KV + ah * 131072, vv = vrow ? (hf * 16 + r3) * 16 : 0x7fffff00u, one = (r3 & 27) == 16 ? 0x3f803f80u : 0u;
      float m = -INFINITY;
#pragma unroll 1
      for (int t = 0; t < 64; ++t) m = max16(m, MFMA(ld16c(ws, l16, sk + t * 1024), q1, z));
      m = -hmax(m);
#pragma unroll
      for (int i = 0; i < 16; ++i) nm0[i] = m;
      o0 = z;
#pragma unroll 1
      for (int t = 0; t < 64; ++t) {
        const f32x16 s_ = MFMA(ld16c(ws, l16, sk + t * 1024), q1, nm0);
        const u32x4 v0 = __builtin_bit_cast(u32x4, ld16c(ws, vv, sk + 65536 + t * 1024)) | one, v1 = __builtin_bit_cast(u32x4, ld16c(ws, vv, sk + 65536 + t * 1024 + 512)) | one;
#pragma unroll
        for (int i = 0; i < 8; ++i) { pa[i] = __builtin_amdgcn_exp2f(s_[i]); pb[i] = __builtin_amdgcn_exp2f(s_[8 + i]); }
        o0 = SMF(__builtin_bit_cast(bf16x8, v0), pa, o0, idx); o0 = SMF(__builtin_bit_cast(bf16x8, v1), pb, o0, idx);
      }
      float unit = 1.f; PIN(unit);
#pragma unroll
      for (int i = 0; i < 9; ++i) oo[i] = o0[i] * unit;
    }
  }
  { const float rl = __builtin_amdgcn_rcpf(oo[8]);
    u32x4 ov = {cvtpa(oo[0] * rl, oo[1] * rl), cvtpa(oo[2] * rl, oo[3] * rl), cvtpa(oo[4] * rl, oo[5] * rl), cvtpa(oo[6] * rl, oo[7] * rl)};
#pragma unroll
    for (int i = 0; i < 4; ++i) ov[i] = ov[i] < 0xfffffffeu ? ov[i] : 0xfffffffeu;
#pragma unroll
    for (int i = 0; i < 4; ++i) { PIN(fa[i]); PIN(fb[i]); PIN(fc[i]); }
    WAITV(0);
    st16(ov, ws, l16, WS_O + (qown * 32 + ah) * 1024); }
  STAMP(45);
  asm volatile("s_barrier" ::: "memory");
  {
    lptr d = lds + L_PAIR + w * 8704 + l16; PIN(d);
    GM(g01, fb[0], fb[3]); GM(g11, fb[1], fb[3]); GM(g01, fa[0], fa[3]); GM(g11, fa[1], fa[3]); GM(g01, fc[0], fc[3]); GM(g11, fc[1], fc[3]);
    GM(g00, fb[0], fb[2]); GM(g10, fb[1], fb[2]);
#define QUAD(g, i) (f32x4){g[4 * (i)], g[4 * (i) + 1], g[4 * (i) + 2], g[4 * (i) + 3]}
    *(l4p)(d) = QUAD(g01, 0); *(l4p)(d + 1024) = QUAD(g01, 1); SBAR(); GM(g00, fa[0], fa[2]);
    *(l4p)(d + 2048) = QUAD(g01, 2); *(l4p)(d + 3072) = QUAD(g01, 3); SBAR(); GM(g10, fa[1], fa[2]);
    *(l4p)(d + 4096) = QUAD(g11, 0); *(l4p)(d + 5120) = QUAD(g11, 1); SBAR(); GM(g00, fc[0], fc[2]);
    *(l4p)(d + 6144) = QUAD(g11, 2); *(l4p)(d + 7168) = QUAD(g11, 3); SBAR(); GM(g10, fc[1], fc[2]);
  }
  LBAR();
  {
    const int hq = (w + n64) & 3;
    const unsigned soo = WS_O + ((2 * tb + kh) * 32 + 8 * hq) * 1024; unsigned spins; unsigned long long ret; u32x16 ofa, ofb;
#define OL0 "buffer_load_dwordx4 v[0:3], %[l], %[r], %[o0] offen sc1\n\t"
#define OL1 "buffer_load_dwordx4 v[4:7], %[l], %[r], %[o0] offen offset:1024 sc1\n\t"
#define OL2 "buffer_load_dwordx4 v[8:11], %[l], %[r], %[o0] offen offset:2048 sc1\n\t"
#define OL3 "buffer_load_dwordx4 v[12:15], %[l], %[r], %[o0] offen offset:3072 sc1\n\t"
#define OL4 "buffer_load_dwordx4 v[16:19], %[l], %[r], %[o1] offen sc1\n\t"
#define OL5 "buffer_load_dwordx4 v[20:23], %[l], %[r], %[o1] offen offset:1024 sc1\n\t"
#define OL6 "buffer_load_dwordx4 v[24:27], %[l], %[r], %[o1] offen offset:2048 sc1\n\t"
#define OL7 "buffer_load_dwordx4 v[28:31], %[l], %[r], %[o1] offen offset:3072 sc1\n\t"
    asm volatile(OL0 OL1 OL2 OL3 OL4 OL5 OL6 OL7 : "=&{v[0:15]}"(ofa), "=&{v[16:31]}"(ofb) : [l] "v"(l16), [r] "s"(ws), [o0] "s"(soo), [o1] "s"(soo + 4096) : "memory");
    const lptr s = lds + L_PAIR + (w ^ 4) * 8704 + l16, wl0 = lds + lwo_of(2 * hq) + l16, wl1 = lds + lwo_of(2 * hq + 1) + l16;
    f32x4 pr[8]; bf16x8 wa_[8], wb_[8];
#pragma unroll
    for (int i = 0; i < 8; ++i) pr[i] = *(l4p)(s + i * 1024);
#pragma unroll
    for (int i = 0; i < 8; ++i) { wa_[i] = LDS8((i < 4 ? wl0 : wl1) + (i & 3) * 1024); wb_[i] = LDS8((i < 4 ? wl0 : wl1) + (i & 3) * 1024 + 4096); }
    SBAR();
#pragma unroll
    for (int i = 0; i < 16; ++i) { g00[i] += pr[i >> 2][i & 3]; g10[i] += pr[4 + (i >> 2)][i & 3]; }
    PIN(g00); PIN(g10);
#define OCHK(r0, r1, r2, r3) "v_max3_u32 v32, v" #r0 ", v" #r1 ", v" #r2 "\n\tv_max_u32 v32, v32, v" #r3 "\n\tv_cmp_eq_u32 vcc, -1, v32\n\t"
#define OSTEP(i, n, r0, r1, r2, r3) \
    "s_getpc_b64 %[ret]\n\ts_waitcnt vmcnt(" #n ")\n\t" OCHK(r0, r1, r2, r3) "s_cbranch_vccnz 3f\n\t" \
    "v_mfma_f32_32x32x16_bf16 %[g0], %[a" #i "], v[" #r0 ":" #r3 "], %[g0]\n\tv_mfma_f32_32x32x16_bf16 %[g1], %[b" #i "], v[" #r0 ":" #r3 "], %[g1]\n\t"
#define ORE(r0, r1, r2, r3, load) OCHK(r0, r1, r2, r3) "s_cbranch_vccz 4f\n\t" load "4:\n\t"
    static_assert(SPIN_CAP == 0x8000, "literal below");
    asm volatile("s_mov_b32 %[sp], 0\n\t"
                 OSTEP(0, 7, 0, 1, 2, 3) OSTEP(1, 6, 4, 5, 6, 7) OSTEP(2, 5, 8, 9, 10, 11) OSTEP(3, 4, 12, 13, 14, 15)
                 OSTEP(4, 3, 16, 17, 18, 19) OSTEP(5, 2, 20, 21, 22, 23) OSTEP(6, 1, 24, 25, 26, 27) OSTEP(7, 0, 28, 29, 30, 31)
                 "s_branch 2f\n"
                 "3:\n\ts_add_u32 %[sp], %[sp], 1\n\ts_cmp_gt_u32 %[sp], 0x8000\n\ts_cbranch_scc1 2f\n\ts_waitcnt vmcnt(0)\n\t"
                 ORE(0, 1, 2, 3, OL0) ORE(4, 5, 6, 7, OL1) ORE(8, 9, 10, 11, OL2) ORE(12, 13, 14, 15, OL3)
                 ORE(16, 17, 18, 19, OL4) ORE(20, 21, 22, 23, OL5) ORE(24, 25, 26, 27, OL6) ORE(28, 29, 30, 31, OL7)
                 "s_waitcnt vmcnt(0)\n\ts_setpc_b64 %[ret]\n"
                 "2:\n\ts_waitcnt vmcnt(0)\n\ts_nop 11"
                 : [g0] "+v"(g00), [g1] "+v"(g10), "+{v[0:15]}"(ofa), "+{v[16:31]}"(ofb), [sp] "=&s"(spins), [ret] "=&s"(ret)
                 : [l] "v"(l16), [r] "s"(ws), [o0] "s"(soo), [o1] "s"(soo + 4096),
                   [a0] "v"(wa_[0]), [a1] "v"(wa_[1]), [a2] "v"(wa_[2]), [a3] "v"(wa_[3]), [a4] "v"(wa_[4]), [a5] "v"(wa_[5]), [a6] "v"(wa_[6]), [a7] "v"(wa_[7]),
                   [b0] "v"(wb_[0]), [b1] "v"(wb_[1]), [b2] "v"(wb_[2]), [b3] "v"(wb_[3]), [b4] "v"(wb_[4]), [b5] "v"(wb_[5]), [b6] "v"(wb_[6]), [b7] "v"(wb_[7])
                 : "memory", "scc", "vcc", "v32");
  }
  STAMP(12);
  { const lptr d = lds + L_PAIR + (w ^ 4) * 8704 + l16;
#pragma unroll
    for (int i = 0; i < 4; ++i) { *(l4p)(d + i * 1088) = QUAD(g00, i); *(l4p)(d + (4 + i) * 1088) = QUAD(g10, i); } }
  lptr rs = lds + L_PAIR + (kh ^ 1) * (4 * 8704) + (ln & 7) * 1088 + ((w & 3) * 8 + (ln >> 3)) * 16; PIN(rs);
  f32x4 ra = {bflo(xres[0]), bfhi(xres[0]), bflo(xres[1]), bfhi(xres[1])}, rc = {bflo(xres[2]), bfhi(xres[2]), bflo(xres[3]), bfhi(xres[3])}; PIN(ra); PIN(rc);
  LBAR();
  STAMP(13);
  {
    f32x4 pa_[4], pc_[4];
#pragma unroll
    for (int i = 0; i < 4; ++i) { pa_[i] = *(l4p)(rs + i * 8704); pc_[i] = *(l4p)(rs + i * 8704 + 512); }
    SBAR();
#pragma unroll
    for (int i = 0; i < 4; ++i) { ra += pa_[i]; rc += pc_[i]; PIN(ra); PIN(rc); }
    const u32x4 r = {cvtpk(ra[0], ra[1]), cvtpk(ra[2], ra[3]), cvtpk(rc[0], rc[1]), cvtpk(rc[2], rc[3])};
    __builtin_amdgcn_raw_buffer_store_b128(r, mk(pout, 2048 * 1024), vout, 0, 0);
  }
  STAMP(14);
  STAMP_END();
  asm volatile(".Lfb_end:");
}

extern "C" void kernel_launch(void* const* d_in, const int* in_sizes, int n_in, void* d_out, int out_size, void* d_ws, size_t ws_size, hipStream_t stream) {
  (void)hipFuncSetAttribute((const void*)fused_block, hipFuncAttributeMaxDynamicSharedMemorySize, LDS_BYTES);
  hipLaunchKernelGGL(fused_block, dim3(256), dim3(512), LDS_BYTES, stream, d_in[0], d_in[1], d_in[2], d_in[4], d_in[5], d_in[6], d_out, d_ws);
}
```

```cpp
#include <hip/hip_runtime.h>
#include <hip/hip_bf16.h>

typedef __bf16 bf16x8 __attribute__((ext_vector_type(8)));
typedef __bf16 bf16x16 __attribute__((ext_vector_type(16)));
typedef __bf16 bf16x2 __attribute__((ext_vector_type(2)));
typedef short s16x4 __attribute__((ext_vector_type(4)));
typedef float f32x16 __attribute__((ext_vector_type(16)));
typedef float f32x8 __attribute__((ext_vector_type(8)));
typedef float f32x4 __attribute__((ext_vector_type(4)));
typedef float f32x2 __attribute__((ext_vector_type(2)));
typedef unsigned u32x4 __attribute__((ext_vector_type(4)));
typedef unsigned u32x2 __attribute__((ext_vector_type(2)));
typedef unsigned u32x16 __attribute__((ext_vector_type(16)));
typedef __amdgpu_buffer_rsrc_t rsrc_t;
typedef __attribute__((address_space(3))) char* lptr;
#define DBG_STAMPS 0
#define DI __device__ __forceinline__
#define SBAR() __builtin_amdgcn_sched_barrier(0)
#define MFMA(a, b, c) __builtin_amdgcn_mfma_f32_32x32x16_bf16(a, b, c, 0, 0, 0)
#define SMF(a, b, c, idx) __builtin_amdgcn_smfmac_f32_32x32x32_bf16(a, __builtin_bit_cast(bf16x16, b), c, idx, 0, 0)
#define LDS8(p) (*(const __attribute__((address_space(3))) bf16x8*)(p))
#define PIN(x) asm volatile("" : "+v"(x))
#define LANE(n) unsigned n = l16; PIN(n); n >>= 4

#define K_F 12
#define K_P 19
#define K_C 25
#define X_TC 3
#define FORCE_FB 0

constexpr unsigned WS_EPOCH = 0;
constexpr unsigned FS = 128;
constexpr unsigned WS_F0 = 262144, WS_F1 = WS_F0 + 256 * FS, WS_F2 = WS_F1 + 256 * FS;
constexpr unsigned WS_F4 = WS_F2 + 256 * FS;
constexpr unsigned WS_STAT = 32768;
constexpr unsigned WS_CS = 65536;
constexpr unsigned WS_Q = 1u << 20;
constexpr unsigned WS_KV = WS_Q + (2u << 20);
constexpr unsigned WS_O = WS_KV + (4u << 20);
constexpr unsigned WS_W2 = WS_O + (2u << 20);
constexpr unsigned WS_H = WS_W2 + (3u << 20);
constexpr unsigned WS_DBG = WS_H + (7u << 20);
constexpr unsigned WS_END = WS_DBG + (1u << 20);
constexpr int U_XA = 0, U_G1 = 24576, U_XB = 49152, U_G2 = 73728, U_G0 = 98304, UD = 49152;
constexpr int L_V = 73728, L_PRIV = 139264, L_ONES = 155648, L_EYE = L_ONES + 5120, L_PAIR = 90112, LDS_BYTES = 163840;
constexpr unsigned SPIN_CAP = 1u << 15;
constexpr float REF_OFF = 100.f;

DI rsrc_t mk(const void* p, unsigned bytes) { return __builtin_amdgcn_make_buffer_rsrc((void*)p, (short)0, (int)bytes, 0x00020000); }
DI unsigned cvtpk(float lo, float hi) { f32x2 v = {lo, hi}; return __builtin_bit_cast(unsigned, __builtin_convertvector(v, bf16x2)); }
DI unsigned cvtpa(float lo, float hi) { unsigned r; asm("v_cvt_pk_bf16_f32 %0, %1, %2" : "=v"(r) : "v"(lo), "v"(hi)); return r; }
DI float bflo(unsigned u) { return __uint_as_float(u << 16); }
DI float bfhi(unsigned u) { return __uint_as_float(u & 0xffff0000u); }
DI float rbf(float f) { return bflo(cvtpk(f, f)); }
DI float max3(float a, float b, float c) { return __builtin_fmaxf(__builtin_fmaxf(a, b), c); }
DI s16x4 trd(lptr p) { return __builtin_amdgcn_ds_read_tr16_b64_v4i16((__attribute__((address_space(3))) s16x4*)p); }
DI bf16x8 trfrag(lptr p, int step) { s16x4 a = trd(p), b = trd(p + step); return __builtin_bit_cast(bf16x8, __builtin_shufflevector(a, b, 0, 1, 2, 3, 4, 5, 6, 7)); }
#define dma(base, imm, voff, r, soff) asm volatile("s_add_i32 m0, %0, %4\n\ts_nop 0\n\tbuffer_load_dwordx4 %1, %2, %3 offen lds" :: "s"(base), "v"(voff), "s"(r), "s"(soff), "i"(imm) : "memory", "scc")
#define dma_m0(base, imm) asm volatile("s_add_i32 m0, %0, %1" :: "s"(base), "i"(imm) : "memory", "scc")
#define dma_go(voff, r, soff) asm volatile("buffer_load_dwordx4 %0, %1, %2 offen lds" :: "v"(voff), "s"(r), "s"(soff) : "memory")
#define dma_nt(base, imm, voff, r, soff) asm volatile("s_add_i32 m0, %0, %4\n\ts_nop 0\n\tbuffer_load_dwordx4 %1, %2, %3 offen nt lds" :: "s"(base), "v"(voff), "s"(r), "s"(soff), "i"(imm) : "memory", "scc")
#define dma_sc1(base, imm, voff, r, soff) asm volatile("s_add_i32 m0, %0, %4\n\ts_nop 0\n\tbuffer_load_dwordx4 %1, %2, %3 offen sc1 lds" :: "s"(base), "v"(voff), "s"(r), "s"(soff), "i"(imm) : "memory", "scc")
#define ALD4(dst, voff, r, soff) asm volatile("buffer_load_dwordx4 %0, %1, %2, %3 offen" : "=v"(dst) : "v"(voff), "s"(r), "s"(soff) : "memory")
#define ALD4C(dst, voff, r, soff) asm volatile("buffer_load_dwordx4 %0, %1, %2, %3 offen sc1" : "=v"(dst) : "v"(voff), "s"(r), "s"(soff) : "memory")
#define ALD2C(dst, voff, r, soff) asm volatile("buffer_load_dwordx2 %0, %1, %2, %3 offen sc1" : "=v"(dst) : "v"(voff), "s"(r), "s"(soff) : "memory")
#define ALD1C(dst, voff, r, soff) asm volatile("buffer_load_dword %0, %1, %2, %3 offen sc1" : "=v"(dst) : "v"(voff), "s"(r), "s"(soff) : "memory")
#define CLD(op, dst, voff, r, soff, lim) asm volatile("s_cmp_ge_u32 %4, %5\n\ts_cbranch_scc1 1f\n\t" op "\n1:" : "=v"(dst) : "v"(voff), "s"(r), "s"(soff), "s"(w), "i"(lim) : "memory", "scc")
#define POLL(voff) CLD("buffer_load_dwordx2 %0, %1, %2, %3 offen sc1", pl, voff, ws, 0, 1)
#define WAITV(n) asm volatile("s_waitcnt vmcnt(%0)" :: "i"(n) : "memory")
#define WAITBAR(n) asm volatile("s_waitcnt vmcnt(%0) lgkmcnt(0)\n\ts_barrier" :: "i"(n) : "memory")
#define LBAR() asm volatile("s_waitcnt lgkmcnt(0)\n\ts_barrier" ::: "memory")
DI void st16(u32x4 v, rsrc_t r, unsigned voff, unsigned soff) {
  asm volatile("buffer_store_dwordx4 %0, %1, %2, %3 offen sc1\n\ts_nop 1" :: "v"(v), "v"(voff), "s"(r), "s"(soff) : "memory"); }
DI void st4(unsigned v, rsrc_t r, unsigned voff, unsigned soff) { __builtin_amdgcn_raw_buffer_store_b32(v, r, voff, soff, 16); }
DI bf16x8 ld16c(rsrc_t r, unsigned voff, unsigned soff) { return __builtin_bit_cast(bf16x8, __builtin_amdgcn_raw_buffer_load_b128(r, voff, soff, 16)); }
template <int CTRL> DI float dppadd(float v) { return v + __int_as_float(__builtin_amdgcn_update_dpp(0, __float_as_int(v), CTRL, 0xf, 0xf, true)); }
#define WAITF(n) do { unsigned sp_; asm volatile("s_cmp_lg_u32 %[w], 0\n\ts_cbranch_scc1 9f\n\ts_mov_b32 %[sp], 0\n\ts_waitcnt vmcnt(%[c])\n" \
    "1:\n\tv_cmp_ne_u64 vcc, %[t], %[p]\n\ts_cbranch_vccz 9f\n\ts_add_u32 %[sp], %[sp], 1\n\ts_cmp_gt_u32 %[sp], %[cap]\n\ts_cbranch_scc1 9f\n\ts_sleep 2\n\t" \
    "buffer_load_dwordx2 %[p], %[v], %[r], 0 offen sc1\n\ts_waitcnt vmcnt(0)\n\ts_branch 1b\n9:" \
    : [p] "+v"(pl), [sp] "=&s"(sp_) : [v] "v"(vf0), [r] "s"(ws), [t] "s"(tag), [w] "s"(w), [c] "i"(n), [cap] "i"(SPIN_CAP) : "memory", "scc", "vcc"); } while (0)
#define PUT(val, soff, who) do { unsigned long long t_; asm volatile("s_cmp_lg_u32 %4, 0\n\ts_cbranch_scc1 9f\n\tv_mov_b64 %0, %1\n\ts_mov_b64 exec, 1\n\t" \
    "buffer_store_dwordx2 %0, off, %2, %3 sc1\n\ts_mov_b64 exec, -1\n9:" : "=&v"(t_) : "s"(val), "s"(ws), "s"(soff), "s"(who) : "memory", "scc"); } while (0)
#define WAITV2(n0, n) asm volatile("s_waitcnt vmcnt(%1)\n\ts_cmp_eq_u32 %0, 0\n\ts_cbranch_scc1 9f\n\ts_waitcnt vmcnt(%2)\n9:" :: "s"(w), "i"(n0), "i"(n) : "memory", "scc")
#define STAMP(i)
#define STAMP_END()
#define STAMPV(i)

DI u32x4 pack8(const f32x16& a, int o, float s) {
  return (u32x4){cvtpa(a[o] * s, a[o + 1] * s), cvtpa(a[o + 2] * s, a[o + 3] * s), cvtpa(a[o + 4] * s, a[o + 5] * s), cvtpa(a[o + 6] * s, a[o + 7] * s)}; }
DI u32x4 glu8(const f32x16& a, float nc, float sd) {
  float h[8];
#pragma unroll
  for (int i = 0; i < 8; ++i) { const float e = __builtin_amdgcn_exp2f(a[i] * nc); h[i] = a[8 + i] * __builtin_amdgcn_rcpf(__builtin_fmaf(e, sd, sd)); }
  return (u32x4){cvtpa(h[0], h[1]), cvtpa(h[2], h[3]), cvtpa(h[4], h[5]), cvtpa(h[6], h[7])}; }

typedef unsigned long long u64x8 __attribute__((ext_vector_type(8)));
__global__ __launch_bounds__(512, 2) void fused_block(const void* a_x, const void* a_wqkv, const void* a_wo, const void* a_wg, const void* a_wu, const void* a_wd, void* a_out, void* a_ws) {
  extern __shared__ __attribute__((aligned(16))) char smem[];
  u64x8 arg;
  asm volatile("s_load_dwordx16 %0, %1, 0x0" : "=s"(arg) : "s"(__builtin_amdgcn_kernarg_segment_ptr()));
  const lptr lds = (lptr)smem;
  const unsigned lds0 = (unsigned)(size_t)lds;
  const int tid = threadIdx.x, lane = tid & 63, w = __builtin_amdgcn_readfirstlane(tid >> 6);
  const int b = blockIdx.x, xcd = b & 7, bi = b >> 3;
  const int r32 = lane & 31, hi = lane >> 5;
  if (w == 0) {
    unsigned long long pc; unsigned len; asm volatile("s_getpc_b64 %0\n.Lfb_pc:\n\ts_mov_b32 %1, .Lfb_end-.Lfb_pc" : "=s"(pc), "=s"(len));
    const rsrc_t rc = mk((const void*)pc, len);
    const unsigned v = lane < 8 ? (bi * 8 + lane) * 128 : 0x7fffff00u;
    asm volatile("s_mov_b32 m0, %0\n\ts_nop 0\n\tbuffer_load_dword %1, %2, 0 offen lds" :: "s"(lds0 + LDS_BYTES - 256), "v"(v), "s"(rc) : "memory"); }
  const int rb = (xcd & 1) * 8 + (bi & 7), Q4 = xcd >> 1, sj = bi >> 3, set = Q4 * 4 + sj;
  const int rot = 2 * (xcd & 1) + (bi & 4), pos = (bi >> 1) & 1, half = bi & 1, kpart = rot + pos;
  const unsigned l3 = lane >> 3, l7 = lane & 7, l16 = lane * 16;
  const int wx = (w + 2 * sj) & 7, ww = (w + 2 * bi) & 7;
  unsigned vx0 = (rb * 128 + wx * 16 + l3) * 1024 + ((l7 ^ (lane >> 4)) * 16), vx1 = (vx0 + 8192) ^ 64;
  unsigned mx = lds0 + (wx >> 2) * UD + (wx & 3) * 2048, mw = lds0 + ww * 1024;
  const bool tb_ = sj == 0;
  const int p1 = 7 * Q4 + 2 * sj - 1, p2 = p1 + 1;
  const unsigned col0 = 128 * Q4 + (tb_ ? 1024 : sj * 512 - 448);
  const unsigned col1 = tb_ ? 512 + 128 * Q4 : 1536 + 64 * p1, col2 = tb_ ? 128 * Q4 : 3328 + 64 * p1;
  const unsigned str12 = tb_ ? 3072 : 3584, wr = ww * 8 + l3, wpc = (l7 ^ ((lane >> 2) & 4)) * 16;
  unsigned vg0 = wr * 3072 + wpc + col0 * 2, vg1 = wr * str12 + wpc + (tb_ ? col1 * 2 : p1 * 128), vg2 = (vg1 - (tb_ ? 1024 : 0)) ^ 32;
  unsigned so0 = rot * 128, so1 = rot * (64 * 3072), so2 = rot * 64 * str12;
  PIN(vx0); PIN(vx1); PIN(vg0); PIN(vg1); PIN(vg2); asm volatile("" : "+s"(mx), "+s"(mw), "+s"(so0), "+s"(so1), "+s"(so2));
  SBAR();
  asm volatile("s_waitcnt lgkmcnt(0)" : "+s"(arg) :: "memory");
  const void *px = (const void*)arg[0], *pwqkv = (const void*)arg[1], *pwo = (const void*)arg[2], *pwg = (const void*)arg[3], *pwu = (const void*)arg[4], *pwd = (const void*)arg[5];
  void *pout = (void*)arg[6], *pws = (void*)arg[7];
  rsrc_t ws = mk(pws, WS_END), rx = mk(px, 2048 * 1024), rg0 = mk(pwqkv, 512 * 3072);
  rsrc_t rg1 = mk(tb_ ? pwqkv : pwg, 512 * str12), rg2 = mk(tb_ ? pwqkv : pwu, 512 * str12);
  rsrc_t rg3 = mk(pwg, 512 * 3584), rg4 = mk(pwu, 512 * 3584);
#define KC(c) (unsigned)(((c) + rot) & 7)
#define SL(c) (((c) % 3) * 8192)
#define DMA1(c, i) do { \
    if ((i) == 0) dma(mx, SL(c), vx0, rx, KC(c) * 128); if ((i) == 1) dma(mx, SL(c) + 1024, vx1, rx, KC(c) * 128); \
    if ((i) == 2) dma(mw, SL(c) + U_G0, vg0, rg0, KC(c) * (64 * 3072)); if ((i) == 3) dma(mw, SL(c) + U_G1, vg1, rg1, KC(c) * 64 * str12); \
    if ((i) == 4) dma(mw, SL(c) + U_G2, vg2, rg2, KC(c) * 64 * str12); } while (0)
  unsigned sd_;
#define M0S(base, imm, off) do { SBAR(); sd_ = (off); asm volatile("s_add_i32 m0, %1, %2" :: "s"(sd_), "s"(base), "i"(imm) : "memory", "scc"); } while (0)
#define M01(c, i) M0S((i) < 2 ? mx : mw, SL(c) + ((i) == 0 ? 0 : (i) == 1 ? 1024 : (i) == 2 ? U_G0 : (i) == 3 ? U_G1 : U_G2), KC(c) * ((i) < 2 ? 128 : (i) == 2 ? 64 * 3072 : 64 * str12))
#define GO1(c, i) do { if ((i) == 0) dma_go(vx0, rx, sd_); if ((i) == 1) dma_go(vx1, rx, sd_); if ((i) == 2) dma_go(vg0, rg0, sd_); if ((i) == 3) dma_go(vg1, rg1, sd_); if ((i) == 4) dma_go(vg2, rg2, sd_); } while (0)
#define M02(c, i) M0S(mw, SL(c) + ((i) & 2 ? U_XA : U_G1) + ((i) & 1) * UD, KC(2 * ((c) - 8) + ((i) >> 1)) * (64 * 3584))
#define GO2(c, i) do { if ((i) & 1) dma_go(vg4, rg4, sd_); else dma_go(vg3, rg3, sd_); } while (0)
#define DMA2(c, i) do { constexpr int sl_ = SL(c) + ((i) & 2 ? U_XA : U_G1); const unsigned kc_ = KC(2 * ((c) - 8) + ((i) >> 1)) * (64 * 3584); \
    if ((i) & 1) dma(mw, sl_ + UD, vg4, rg4, kc_); else dma(mw, sl_, vg3, rg3, kc_); } while (0)
  dma(mx, 0, vx0, rx, so0); dma(mx, 1024, vx1, rx, so0); SBAR(); dma(mw, U_G0, vg0, rg0, so1); SBAR(); dma(mw, U_G1, vg1, rg1, so2); dma(mw, U_G2, vg2, rg2, so2); SBAR();
  STAMP(47);
  asm volatile("" : "+s"(ws), "+s"(rx), "+s"(rg0), "+s"(rg1), "+s"(rg2), "+s"(rg3), "+s"(rg4));
  unsigned vg3 = wr * 3584 + wpc + p2 * 128, vg4 = vg3 ^ 32; PIN(vg3); PIN(vg4);
  const int tt = w & 3, cg = w >> 2;
  const int q4 = (lane & 15) >> 2, p4 = lane & 3, g16 = (lane >> 4) & 1, swr = (q4 >> 1) * 4;
  const int wrow = (8 * hi + q4) * 128 + (p4 & 1) * 8;
  const int pq = 4 * cg + 2 * g16 + (p4 >> 1);
  const int gl0 = g16 * UD + (pq ^ swr) * 16, gl1 = gl0 ^ 32;
  lptr wp0 = lds + U_G0 + wrow + (pq ^ swr) * 16;
  lptr wp1 = lds + wrow + (tb_ ? (pq ^ swr) * 16 : gl0);
  lptr wp2 = lds + wrow + (tb_ ? UD + (pq ^ swr ^ 2) * 16 : gl1);
  lptr xp[4];
#pragma unroll
  for (int s = 0; s < 4; ++s) { xp[s] = lds + (tt >> 1) * UD + (tt & 1) * 4096 + r32 * 128 + (((2 * s + hi) ^ ((r32 >> 1) & 7)) * 16); PIN(xp[s]); }
  PIN(wp0); PIN(wp1); PIN(wp2);
  const int ah = xcd * 4 + (bi & 3), aq = bi >> 2, kh = w >> 2;
  const int qown = aq * 8 + 2 * (w & 3) + kh;
  const unsigned csb = WS_CS + set * 10240;
  auto colsum = [&](lptr cp, unsigned so) {
    const __bf16 o1 = (__bf16)1.0f; const bf16x8 ones = {o1, o1, o1, o1, o1, o1, o1, o1};
    const bf16x8 f0 = trfrag(cp, 512), f1 = trfrag(cp + 2048, 512), f2 = trfrag(cp + 4096, 512), f3 = trfrag(cp + 6144, 512);
    const f32x16 cs = MFMA(ones, f3, MFMA(ones, f2, MFMA(ones, f1, MFMA(ones, f0, (f32x16){}))));
    const float ch = rbf(cs[0]);
    st4(cvtpk(ch, cs[0] - ch), ws, l16 < 512 ? l16 : 0x7fffff00u, csb + so + (kpart >> 2) * 512 + (kpart & 3) * 4);
  };
  unsigned vf0 = 0;
  u32x2 ep; u32x4 xr; unsigned e0, e1; unsigned long long tag;
  const unsigned srow = rb * 128 + set * 8 + w;
  float sv[8], sa, sb, smean;
  auto swapadd16 = [](float v) { auto r = __builtin_amdgcn_permlane16_swap(__float_as_uint(v), __float_as_uint(v), false, false); return __uint_as_float(r[0]) + __uint_as_float(r[1]); };
  auto swapadd32 = [](float v) { auto r = __builtin_amdgcn_permlane32_swap(__float_as_uint(v), __float_as_uint(v), false, false); return __uint_as_float(r[0]) + __uint_as_float(r[1]); };
#define STATS(i) do { \
    if ((i) == 0 || (i) == 1) { sv[4 * ((i) & 1)] = bflo(xr[2 * ((i) & 1)]); sv[4 * ((i) & 1) + 1] = bfhi(xr[2 * ((i) & 1)]); sv[4 * ((i) & 1) + 2] = bflo(xr[2 * ((i) & 1) + 1]); sv[4 * ((i) & 1) + 3] = bfhi(xr[2 * ((i) & 1) + 1]); \
      PIN(sv[4 * ((i) & 1)]); PIN(sv[4 * ((i) & 1) + 1]); PIN(sv[4 * ((i) & 1) + 2]); PIN(sv[4 * ((i) & 1) + 3]); } \
    if ((i) == 2) { sa = (sv[0] + sv[1]) + sv[2]; PIN(sa); sb = (sv[3] + sv[4]) + sv[5]; PIN(sb); } \
    if ((i) == 3) { sa = ((sa + sv[6]) + sv[7]) + sb; PIN(sa); } \
    if ((i) == 4 || (i) == 14) { sa = dppadd<0x121>(sa); PIN(sa); } if ((i) == 5 || (i) == 15) { sa = dppadd<0x122>(sa); PIN(sa); } \
    if ((i) == 6 || (i) == 16) { sa = dppadd<0x124>(sa); PIN(sa); } if ((i) == 7 || (i) == 17) { sa = dppadd<0x128>(sa); PIN(sa); } \
    if ((i) == 8 || (i) == 18) { sa = swapadd16(sa); PIN(sa); } \
    if ((i) == 9) { smean = swapadd32(sa) * (1.f / 512); PIN(smean); sa = 0.f; } \
    if ((i) >= 10 && (i) <= 13) { const float d_ = sv[(2 * (i) - 20) & 7] - smean, e_ = sv[(2 * (i) - 19) & 7] - smean; sa = __builtin_fmaf(e_, e_, __builtin_fmaf(d_, d_, sa)); PIN(sa); } \
    if ((i) == 19) { sa = rbf(swapadd32(sa) * (1.f / 512)); PIN(sa); } \
    if ((i) == 20) { sa = __builtin_amdgcn_sqrtf(rbf(sa + 1e-5f)); PIN(sa); } \
    if ((i) == 21) st4(cvtpk(-smean, sa), ws, lane ? 0x7fffff00u : 0u, WS_STAT + srow * 4); } while (0)
  bf16x8 xf[32], wa[3], wb[3];
  f32x16 acc0 = {}, acc1 = {}, acc2 = {};
  u32x2 pl; unsigned stat; u32x4 cs0, cs1, cs2;
#define RD1(W, ks) do { xf[ks] = LDS8(xp[(ks) & 3] + SL((ks) >> 2)); W[0] = trfrag(wp0 + SL((ks) >> 2) + ((ks) & 3) * 2048, 512); } while (0)
#define RD1B(W, ks) W[1] = trfrag(wp1 + U_G1 + SL((ks) >> 2) + ((ks) & 3) * 2048, 512)
#define RD1C(W, ks) W[2] = trfrag(wp2 + U_G1 + SL((ks) >> 2) + ((ks) & 3) * 2048, 512)
#define RD2A(W, ks) W[0] = trfrag(wp1 + SL(8 + ((ks) >> 3)) + ((ks) & 4 ? U_XA : U_G1) + ((ks) & 3) * 2048, 512)
#define RD2B(W, ks) W[1] = trfrag(wp2 + SL(8 + ((ks) >> 3)) + ((ks) & 4 ? U_XA : U_G1) + ((ks) & 3) * 2048, 512)
  const int ks2 = bi + 32 * ((w + 7) & 7);
#define W2LOAD() do { if (ks2 < 144) { const rsrc_t r_ = mk(ks2 < 112 ? pwd : pwo, ks2 < 112 ? 1792 * 1024 : 512 * 1024); const unsigned so_ = (ks2 < 112 ? ks2 : ks2 - 112) * 16384 + xcd * 128; \
      LANE(n_); const unsigned v_ = (n_ >> 3) * 1024 + ((n_ & 7) ^ ((n_ >> 2) & 4)) * 16; \
      dma_nt(lds0 + w * 2048, L_PRIV, v_, r_, so_); dma_nt(lds0 + w * 2048, L_PRIV + 1024, v_, r_, so_ + 8192); } } while (0)
#define FLAG(f) PUT(tag, (f) + (set * 16 + rb) * FS, w)
  if (w < 5) { const unsigned o_ = (tid & 11) == 0 ? 0x3f803f80u : 0u;
    *(__attribute__((address_space(3))) u32x4*)(lds + L_ONES + tid * 16) = (u32x4){o_, o_, o_, o_}; }
  else if (w < 7) {
    const unsigned one_ = hi == ((lane >> 2) & 1) ? 0x3f80u << ((lane & 1) * 16) : 0u, dw_ = ((lane >> 1) & 1) + ((lane >> 2) & 6) - 4 * (w - 5);
    *(__attribute__((address_space(3))) u32x4*)(lds + L_ONES + tid * 16) = (u32x4){dw_ == 0 ? one_ : 0u, dw_ == 1 ? one_ : 0u, dw_ == 2 ? one_ : 0u, dw_ == 3 ? one_ : 0u}; }
  SBAR();
  asm volatile("s_barrier" ::: "memory");
  STAMP(48);
  DMA1(1, 0); DMA1(1, 1); DMA1(1, 2); DMA1(1, 3); DMA1(1, 4);
  STAMP(1);
  WAITBAR(5);
  STAMP(2);
  RD1(wa, 0); RD1B(wa, 0); RD1C(wa, 0);
#define STATG(g) do { STATS(2 * ((g) - 12)); STATS(2 * ((g) - 12) + 1); } while (0)
  DMA1(2, 0); DMA1(2, 4);
#define STEP1(ks, WC, WN) do { constexpr int c_ = ((ks) + 1) >> 2, s_ = ((ks) + 1) & 3; \
    if (s_ == 0) { SBAR(); \
      if ((c_ == 1 || c_ == 2) && w < 3 && pos == c_ - 1) \
        colsum((lptr)((unsigned)(size_t)(w == 0 ? wp0 : w == 1 ? wp1 + U_G1 : wp2 + U_G1) ^ (half * 64)) + SL(c_ - 1), (half * 5 + w) * 1024); \
      if (c_ == 1) { const unsigned z_ = 0; ALD4(xr, l16, rx, srow * 1024); CLD("buffer_load_dwordx2 %0, %1, %2, %3 offen sc1", ep, z_, ws, WS_EPOCH, 1); } \
      if (c_ == X_TC && w == 0) { const unsigned v_ = l16 * 224 + p2 * 128;     \
        asm volatile("s_mov_b32 m0, %0\n\ts_nop 0\n\tbuffer_load_dword %1, %2, %3 offen lds" :: "s"(lds0 + LDS_BYTES - 256), "v"(v_), "s"(half ? rg4 : rg3), "s"(kpart * (64 * 3584)) : "memory"); } \
      if (c_ == 5) W2LOAD(); \
      if (c_ == 6 && w == 0) { LANE(n_); vf0 = WS_F0 + (n_ < 16 ? set * 16 + n_ : (n_ & 15) * 16 + rb) * FS; PIN(vf0); } \
      if (c_ == 7) WAITF(4); \
      STAMP(16 + 2 * c_); if (c_ == 5 && ks2 < 144) WAITV(7); else WAITV(c_ == 1 ? 6 : c_ < 7 ? 5 : c_ == 7 ? 4 : 8); LBAR(); STAMP(17 + 2 * c_); \
      if (c_ == 2) { PIN(ep); PIN(xr); e0 = __builtin_amdgcn_readfirstlane(ep[0]); e1 = __builtin_amdgcn_readfirstlane(ep[1]);     \
        tag = (unsigned long long)((e1 ^ 0x85EBCA6Bu) + e0) << 32 | (e0 * 0x9E3779B1u + 0x7F4A7C15u); }     \
      if (c_ == 4) FLAG(WS_F0); \
      if (c_ == 6) POLL(vf0); \
      if (c_ == 7) { const unsigned v_ = (l16 & 496) >> 2; ALD1C(stat, v_, ws, WS_STAT + (rb * 4 + tt) * 128); \
        ALD4C(cs0, l16, ws, csb + cg * 5120); ALD4C(cs1, l16, ws, csb + cg * 5120 + 1024); ALD4C(cs2, l16, ws, csb + cg * 5120 + 2048); } \
      if (c_ == 8) { wp1 = wp0 - U_G0 + (l16 & 256) * (UD / 256); wp2 = (lptr)((unsigned)(size_t)wp1 ^ 32); PIN(wp1); PIN(wp2); } } \
    SBAR(); \
    if ((ks) < 31) RD1(WN, ((ks) + 1) & 31); else RD2A(WN, 0); \
    if (c_ <= 5) M01(c_ + 2, s_); else M02(c_ + 2, s_); STATG(3 * ((ks) - 3) - 1); SBAR(); \
    acc0 = MFMA(WC[0], xf[ks], acc0); SBAR(); \
    if (c_ <= 5) GO1(c_ + 2, s_); else GO2(c_ + 2, s_); \
    if ((ks) < 31) RD1B(WN, ((ks) + 1) & 31); else RD2B(WN, 0); \
    if (c_ >= 1 && c_ <= 5 && s_ == 0) M01(c_ + 2, 4); if (c_ == 8) M02(10, 1); SBAR(); STATG(3 * ((ks) - 3)); SBAR(); \
    acc1 = MFMA(WC[1], xf[ks], acc1); SBAR(); \
    if (c_ >= 1 && c_ <= 5 && s_ == 0) GO1(c_ + 2, 4); if (c_ == 8) { GO2(10, 1); M02(10, 2); } \
    if ((ks) < 31) RD1C(WN, ((ks) + 1) & 31); SBAR(); STATG(3 * ((ks) - 3) + 1); SBAR(); \
    acc2 = MFMA(WC[2], xf[ks], acc2); SBAR(); if (c_ == 8) GO2(10, 2); SBAR(); } while (0)
#define STEP1x4(k) STEP1(k, wa, wb); STEP1(k + 1, wb, wa); STEP1(k + 2, wa, wb); STEP1(k + 3, wb, wa)
  STEP1x4(0); STEP1x4(4); STEP1x4(8); STEP1x4(12); STEP1x4(16); STEP1x4(20); STEP1x4(24); STEP1x4(28);
  DMA2(10, 3);
  asm volatile("s_waitcnt vmcnt(8)" : "+v"(stat), "+v"(cs0), "+v"(cs1), "+v"(cs2) :: "memory");
  STAMP(3);
  unsigned nm2 = (stat & 0xffffu) * 0x10001u;
  bf16x8 nmf = __builtin_bit_cast(bf16x8, (u32x4){nm2, nm2, nm2, nm2});
  acc0 = MFMA(__builtin_bit_cast(bf16x8, cs0), nmf, acc0); acc1 = MFMA(__builtin_bit_cast(bf16x8, cs1), nmf, acc1); acc2 = MFMA(__builtin_bit_cast(bf16x8, cs2), nmf, acc2);
  const float sd = bfhi(stat), rstd = __builtin_amdgcn_rcpf(sd), gsc = -1.4426950408889634f * rstd, qsc = rstd * (0.25f * 1.4426950408889634f), sc0 = sj == 1 ? qsc : rstd;
  const int tile = rb * 4 + tt, hd = 8 * Q4 + (tb_ ? 0 : 4) + 2 * cg;
  const unsigned sok = WS_KV + hd * 131072 + tile * 1024, soq = WS_Q + (hd * 64 + tile) * 1024, isv = (sj + 1) & 2;
  unsigned l16k = (l16 & 0x330) | (l16 & 64) * 2 | (l16 & 128) / 2; if (sj == 1) l16k = l16; PIN(l16k);
  SBAR();
  STAMP(4);
  u32x4 qf0, qf1, hp, hq; bf16x8 eye0, eye1;
  f32x16 acc3 = {}, acc4 = {};
  auto glu2 = [&](const f32x16& a, int i) {
    const float e0_ = __builtin_amdgcn_exp2f(a[i] * gsc), e1_ = __builtin_amdgcn_exp2f(a[i + 1] * gsc);
    return cvtpa(a[8 + i] * __builtin_amdgcn_rcpf(__builtin_fmaf(e0_, sd, sd)), a[9 + i] * __builtin_amdgcn_rcpf(__builtin_fmaf(e1_, sd, sd))); };
  const unsigned mk_ = lds0 + ((w + aq) & 7) * 1024, skv = WS_KV + ah * 131072 + ((w + aq) & 7) * 1024;
#define KVM(i) M0S(mk_, (i) * 8192 + ((i) >= 8 ? L_V - 65536 : 0), skv + (i) * 8192)
#define KVG() asm volatile("buffer_load_dwordx4 %0, %1, %2 offen sc1 lds" :: "v"(l16), "s"(ws), "s"(sd_) : "memory")
#define PRE2(ks) do { constexpr int c_ = 8 + (((ks) + 1) >> 3), s_ = ((ks) + 1) & 7; \
    if (s_ == 0 && c_ < 12) { SBAR(); \
      if (c_ == 9 && (w == 1 || w == 2)) colsum((lptr)((unsigned)(size_t)(w == 1 ? wp1 : wp2) ^ (half * 64)) + SL(8) + (pos ? U_XA : U_G1), (half * 5 + 2 + w) * 1024); \
      STAMP(16 + 2 * c_); if (c_ == 9) WAITV(6); else if (c_ == 10) WAITV2(1, 0); \
      LBAR(); STAMP(17 + 2 * c_); } \
    if ((ks) == K_F) { asm volatile("s_waitcnt vmcnt(4)\n\ts_barrier" ::: "memory"); FLAG(WS_F1); STAMP(51); } \
    if ((ks) == K_P) { vf0 = WS_F1 + Q4 * 64 * FS + l16 * (FS / 16); POLL(vf0); }     \
    if ((ks) == K_C) { WAITF(0); STAMP(52); asm volatile("s_barrier" ::: "memory"); STAMP(53); } \
    if ((ks) == 31) { WAITV(6); PIN(cs0); PIN(cs1); } \
    SBAR(); } while (0)
#define FILL2(ks) do { \
    if ((ks) == 0 || (ks) == 1) { const u32x4 p_ = pack8(acc0, ((ks) & 1) * 8, sc0); if ((ks) == 0) hp = p_; else hq = p_; \
      if (isv) st16(p_, ws, l16k, sj == 1 ? soq + ((ks) & 1) * 65536 : sok + ((ks) & 1) * 131072); } \
    if ((ks) == 1) { eye0 = LDS8(lds + L_EYE + l16); eye1 = LDS8(lds + L_EYE + 1024 + l16); } \
    if ((ks) == 2 && !isv) acc0 = MFMA(__builtin_bit_cast(bf16x8, hq), eye1, MFMA(__builtin_bit_cast(bf16x8, hp), eye0, (f32x16){})); \
    if ((ks) == 3 && !isv) { LANE(n_); const unsigned vvo = (n_ & 16) * 8192 + ((n_ >> 5) * 16 + (n_ & 15)) * 16; \
      st16((u32x4){cvtpk(acc0[0], acc0[1]), cvtpk(acc0[2], acc0[3]), cvtpk(acc0[4], acc0[5]), cvtpk(acc0[6], acc0[7])}, ws, vvo, sok + 65536); \
      st16((u32x4){cvtpk(acc0[8], acc0[9]), cvtpk(acc0[10], acc0[11]), cvtpk(acc0[12], acc0[13]), cvtpk(acc0[14], acc0[15])}, ws, vvo, sok + 65536 + 512); } \
    if ((ks) >= 4 && (ks) <= 6 && tb_) st16(pack8((ks) < 6 ? acc1 : acc2, ((ks) & 1) * 8, (ks) < 6 ? rstd : qsc), ws, (ks) < 6 ? l16k : l16, (ks) < 6 ? sok + ((ks) & 1) * 131072 : soq); \
    if ((ks) == 6 && tb_) st16(pack8(acc2, 8, qsc), ws, l16, soq + 65536); \
    if ((ks) >= 4 && (ks) <= 18 && !((ks) & 1) && !tb_) { \
      if ((ks) == 16) st16(hp, ws, l16, WS_H + (tile * 112 + 4 * p1 + 2 * cg) * 1024); \
      if ((ks) < 12) { hp[(((ks) >> 1) + 2) & 3] = glu2(acc1, ((ks) + 4) & 6); PIN(hp); } \
      else { hq[(((ks) >> 1) + 2) & 3] = glu2(acc2, ((ks) + 4) & 6); PIN(hq); } \
      if ((ks) == 18) st16(hq, ws, l16, WS_H + (tile * 112 + 4 * p1 + 2 * cg + 1) * 1024); } \
    if ((ks) == 26) { PIN(nm2); nmf = __builtin_bit_cast(bf16x8, (u32x4){nm2, nm2, nm2, nm2}); PIN(nmf); } \
    if ((ks) == 31) { acc3 = MFMA(__builtin_bit_cast(bf16x8, cs0), nmf, acc3); acc4 = MFMA(__builtin_bit_cast(bf16x8, cs1), nmf, acc4); } \
    } while (0)
#define STEP2(ks, WC, WN) do { constexpr int c_ = 8 + (((ks) + 1) >> 3), s_ = ((ks) + 1) & 7; \
    if ((ks) < 31) RD2A(WN, ((ks) + 1) & 31); \
    if (s_ == 7) RD2B(WN, ((ks) + 1) & 31); \
    if (c_ == 9 && s_ < 4) M02(11, s_); \
    if ((ks) == K_C + 1) KVM(0); if ((ks) == K_C + 2) KVM(8); SBAR(); \
    acc3 = MFMA(WC[0], xf[ks], acc3); SBAR(); \
    if (c_ == 9 && s_ < 4) GO2(11, s_); \
    if ((ks) == K_C) ALD4C(cs0, l16, ws, csb + cg * 5120 + 3072); \
    if ((ks) == K_C + 1 || (ks) == K_C + 2) KVG(); if ((ks) == K_C + 3) ALD4C(qf1, l16, ws, WS_Q + (ah * 64 + qown) * 1024); \
    if ((ks) < 31 && s_ != 7) RD2B(WN, ((ks) + 1) & 31); \
    SBAR(); FILL2(ks); \
    if ((ks) == K_C + 1) KVM(4); if ((ks) == K_C + 2) KVM(12); SBAR(); \
    acc4 = MFMA(WC[1], xf[ks], acc4); SBAR(); \
    if ((ks) == K_C) ALD4C(cs1, l16, ws, csb + cg * 5120 + 4096); \
    if ((ks) == K_C + 1 || (ks) == K_C + 2) KVG(); if ((ks) == K_C + 3) ALD4C(qf0, l16, ws, WS_Q + (ah * 64 + (qown ^ 1)) * 1024); \
    SBAR(); PRE2((ks) + 1); } while (0)
#define STEP2x4(k) STEP2(k, wa, wb); STEP2(k + 1, wb, wa); STEP2(k + 2, wa, wb); STEP2(k + 3, wb, wa)
  STEP2x4(0); STEP2x4(4); STEP2x4(8); STEP2x4(12); STEP2x4(16); STEP2x4(20); STEP2x4(24); STEP2x4(28);
  STAMP(5);
  {
    LANE(n_); const lptr s2_ = lds + L_PRIV + w * 2048 + (4 * (n_ >> 5) + ((n_ >> 2) & 3)) * 128 + (n_ & 1) * 8; const int pc_ = (((n_ >> 3) & 2) + ((n_ >> 1) & 1)) ^ ((n_ >> 1) & 4);
    hp = __builtin_bit_cast(u32x4, trfrag(s2_ + pc_ * 16, 1024)); hq = __builtin_bit_cast(u32x4, trfrag(s2_ + (pc_ ^ 4) * 16, 1024)); }
  st16(glu8(acc3, gsc, sd), ws, l16, WS_H + (tile * 112 + 4 * p2 + 2 * cg) * 1024);
  LANE(ln); const int r3 = ln & 31, hf = ln >> 5;
  const int tb = xcd * 4 + (bi & 3), n64 = bi >> 2;
  lptr kp = lds + kh * 32768 + l16, kq = lds + (kh ^ 1) * 32768 + l16;
  const bool vrow = r3 < 16;
  int vstep = vrow ? 4096 : 0;
  lptr vp = lds + (vrow ? L_V + kh * 32768 + hf * 256 : L_ONES - 256) + r3 * 16;
  int idx = 0xDDDDDDDD;
  float aone = hf ? 0.f : 1.f;
  PIN(kp); PIN(kq); PIN(vstep); PIN(vp); PIN(idx); PIN(aone);
  f32x16 nm0, nm1, o0, o1, sA, sB;
  const f32x16 z = {};
  { u32x4 zz = {}; PIN(zz); o0 = MFMA(__builtin_bit_cast(bf16x8, zz), __builtin_bit_cast(bf16x8, zz), z); PIN(o0); o1 = MFMA(__builtin_bit_cast(bf16x8, zz), __builtin_bit_cast(bf16x8, zz), o0); PIN(o1); }
  STAMP(6);
  WAITBAR(1);
  PIN(qf0); PIN(qf1);
  STAMP(7);
  const bf16x8 q0 = __builtin_bit_cast(bf16x8, qf0), q1 = __builtin_bit_cast(bf16x8, qf1);
  auto hmax = [&](float m) { auto rr = __builtin_amdgcn_permlane32_swap(__float_as_uint(m), __float_as_uint(m), false, false);
    return __builtin_fmaxf(__uint_as_float(rr[0]), __uint_as_float(rr[1])); };
  auto max16 = [&](float m, const f32x16& s) {
#pragma unroll
    for (int i = 0; i < 16; i += 2) m = __builtin_fmaxf(__builtin_fmaxf(m, s[i]), s[i + 1]);
    return m; };
  __builtin_amdgcn_s_setprio(1);
  bf16x8 k1 = LDS8(kp), va, vb;
  {
    const bf16x8 k0 = LDS8(kq);
    va = LDS8(vp); SBAR();
    if (ks2 < 144) { st16(hp, ws, l16, WS_W2 + (2 * xcd * 144 + ks2) * 1024); st16(hq, ws, l16, WS_W2 + ((2 * xcd + 1) * 144 + ks2) * 1024); }
    hp[0] = glu2(acc4, 0); PIN(hp); SBAR();
    const f32x16 a2 = MFMA(k1, q0, z), a0 = MFMA(k0, q0, z); SBAR();
    hp[1] = glu2(acc4, 2); PIN(hp); SBAR();
    const f32x16 a3 = MFMA(k1, q1, z), a1 = MFMA(k0, q1, z); SBAR();
    float x0 = a0[0], x2 = a2[0];
#pragma unroll
    for (int i = 1; i < 15; i += 2) { x2 = max3(x2, a2[i], a2[i + 1]); x0 = max3(x0, a0[i], a0[i + 1]); }
    nm0 = __builtin_amdgcn_mfma_f32_32x32x2f32(aone, -REF_OFF - hmax(max3(max3(x0, x2, a0[15]), a2[15], a2[15])), z, 0, 0, 0); SBAR();
    float x1 = a1[0], x3 = a3[0];
#pragma unroll
    for (int i = 1; i < 15; i += 2) { x3 = max3(x3, a3[i], a3[i + 1]); x1 = max3(x1, a1[i], a1[i + 1]); }
    float m1 = -REF_OFF - hmax(max3(max3(x1, x3, a1[15]), a3[15], a3[15])); PIN(m1); SBAR();
    sA = MFMA(k1, q0, nm0); SBAR();
    hp[2] = glu2(acc4, 4); PIN(hp); SBAR();
    nm1 = __builtin_amdgcn_mfma_f32_32x32x2f32(aone, m1, z, 0, 0, 0); SBAR();
    hp[3] = glu2(acc4, 6); PIN(hp);
    st16(hp, ws, l16, WS_H + (tile * 112 + 4 * p2 + 2 * cg + 1) * 1024); SBAR();
  }
  f32x8 pa, pb;
  f32x16 g00, g01, g10, g11;
  bf16x8 fa[4], fb[4];
  unsigned sow = WS_W2 + (2 * n64 * 144 + 14 * w) * 1024, soh = WS_H + ((2 * tb + kh) * 112 + 14 * w) * 1024;
  const unsigned dh = kh ? -112 * 1024 : 112 * 1024;
#define EX(P, S, i) P[(i) & 7] = __builtin_amdgcn_exp2f(S[i])
  unsigned so_;
#define UN_(...) __VA_ARGS__
#define AP_(ph, f, ...) f(ph, __VA_ARGS__)
#define AP2_(...) AP_(__VA_ARGS__)
#define MS(slot, M) do { AP2_(0, UN_ slot); SBAR(); M; SBAR(); AP2_(1, UN_ slot); SBAR(); } while (0)
#define NO(ph, x) do {} while (0)
#define DO(ph, x) do { if (ph) { x; } } while (0)
#define LD(ph, dst, off) do { if (ph) dst = ld16c(ws, l16, so_); else { so_ = (off); asm volatile("" :: "s"(so_)); } } while (0)
#define DM(ph, base, imm, off) do { if (ph) asm volatile("buffer_load_dwordx4 %0, %1, %2 offen sc1 lds" :: "v"(l16), "s"(ws), "s"(so_) : "memory"); \
    else { so_ = (off); asm volatile("s_add_i32 m0, %1, %2" :: "s"(so_), "s"(base), "i"(imm) : "memory", "scc"); } } while (0)
#define KV(ph, i) DM(ph, mk_, (i) * 8192 + ((i) >= 8 ? L_V - 65536 : 0), skv + (i) * 8192)
#define N_ (NO, 0)
#define T2(u, ...) T2X(o1 = SMF(vb, pb, o1, idx), u, __VA_ARGS__)
#define T2X(FIN, u, A3, MA, A4, B1, B2, MB, B4) do { \
    EX(pa, sA, 0); EX(pa, sA, 1); SBAR(); FIN; SBAR(); vb = LDS8(vp + (u) * 1024 + 512); SBAR(); \
    EX(pa, sA, 2); EX(pa, sA, 3); EX(pa, sA, 4); EX(pa, sA, 5); SBAR(); sB = MFMA(k1, q1, nm1); SBAR(); k1 = LDS8(kp + ((u) + 1) * 1024); SBAR(); \
    EX(pa, sA, 6); EX(pa, sA, 7); SBAR(); EX(pb, sA, 8); EX(pb, sA, 9); MS(A3, o0 = SMF(va, pa, o0, idx)); \
    EX(pb, sA, 10); EX(pb, sA, 11); EX(pb, sA, 12); EX(pb, sA, 13); MS(A4, MA); EX(pb, sA, 14); EX(pb, sA, 15); SBAR(); \
    EX(pa, sB, 0); EX(pa, sB, 1); MS(B1, o0 = SMF(vb, pb, o0, idx)); \
    EX(pa, sB, 2); EX(pa, sB, 3); EX(pa, sB, 4); EX(pa, sB, 5); MS(B2, sA = MFMA(k1, q0, nm0)); \
    EX(pa, sB, 6); EX(pa, sB, 7); SBAR(); EX(pb, sB, 8); EX(pb, sB, 9); SBAR(); o1 = SMF(va, pa, o1, idx); SBAR(); va = LDS8(vp + ((u) + 1) * 1024); SBAR(); \
    EX(pb, sB, 10); EX(pb, sB, 11); EX(pb, sB, 12); EX(pb, sB, 13); MS(B4, MB); EX(pb, sB, 14); EX(pb, sB, 15); SBAR(); } while (0)
#define NOP_ do {} while (0)
#define ADV() do { kp += 4096; vp += vstep; } while (0)
#define GM(g, a, b) do { g = MFMA(a, b, g); PIN(g); } while (0)
#define G2P(u, C, D, X0, X1, X2, X3, X4, X5) do { \
    T2(u, X0, GM(g00, C[0], C[2]), (LD, D[3], soh + dh - 1024), X1, X2, GM(g10, C[1], C[2]), (LD, C[2], soh)); \
    T2(u + 1, X3, GM(g11, C[1], C[3]), (LD, C[1], sow + 144 * 1024), X4, X5, GM(g01, C[0], C[3]), (LD, C[0], sow)); \
    sow += 1024; soh += 1024; } while (0)
#define PRIO_AT(k, n) asm volatile("s_cmp_lg_u32 %0, " #k "\n\ts_cbranch_scc1 1f\n\ts_setprio " #n "\n1:" :: "s"(it) : "scc")
#define PRIO(n) __builtin_amdgcn_s_setprio(n)
  T2X(NOP_, 0, (KV, 1), NOP_, N_, (KV, 5), (KV, 9), NOP_, N_); PRIO(0); T2(1, (KV, 13), NOP_, N_, (KV, 2), (KV, 6), NOP_, N_);
  STAMP(41);
  asm volatile("s_waitcnt vmcnt(6)\n\ts_barrier" ::: "memory");
  STAMP(42);
  FLAG(WS_F2);
  PUT((unsigned long long)e1 << 32 | (e0 + 1), WS_EPOCH, w | b);
  if (w == 0) vf0 = WS_F2 + (ln < 16 ? ln * 16 + (tb >> 1) : ((n64 >> 1) * 4 + ((ln >> 3) & 3)) * 16 + (n64 & 1) * 8 + (ln & 7)) * FS;
  PRIO(2); T2(2, (KV, 10), NOP_, N_, (KV, 14), (KV, 3), NOP_, N_); T2(3, (KV, 7), NOP_, N_, (KV, 11), (KV, 15), NOP_, N_); ADV();
  hp = (u32x4){~0u, ~0u, ~0u, ~0u}; PIN(hp);
  PRIO(1); T2(0, (DO, st16(hp, ws, l16, WS_O + (qown * 32 + ah) * 1024)), NOP_, N_, N_, (DO, POLL(vf0)), NOP_, N_); PRIO(0); T2(1, N_, NOP_, N_, N_, N_, NOP_, N_);
  STAMP(43);
  WAITF(0);
  STAMP(44);
  asm volatile("s_waitcnt vmcnt(0)\n\ts_barrier" ::: "memory");
  STAMP(8);
  PUT(tag, WS_F4 + b * FS, w);
  PRIO(3);
  { bf16x8 zz = {};
#define GZ(g) do { PIN(zz); g = MFMA(zz, zz, z); PIN(g); } while (0)
    T2(2, (LD, fa[2], soh), GZ(g00), (LD, fa[0], sow), (LD, fa[1], sow + 144 * 1024), (LD, fa[3], soh + dh), GZ(g10), N_);
    T2(3, (LD, fb[2], soh + 1024), GZ(g11), N_, (LD, fb[1], sow + 145 * 1024), N_, GZ(g01), (LD, fb[0], sow + 1024)); ADV(); }
  sow += 2048; soh += 2048;
#pragma unroll 1
  for (int it = 0; it < 4; ++it) { STAMPV(54 + it); PRIO_AT(2, 2); PRIO_AT(3, 1); G2P(0, fa, fb, N_, N_, N_, N_, N_, N_); PRIO_AT(3, 0); G2P(2, fb, fa, N_, N_, N_, N_, N_, N_); ADV(); }
  auto lwo_of = [](int g) { return (unsigned)(g < 3 ? g : g < 6 ? g + 1 : g + 3) * 8192; };
  const unsigned swo = WS_W2 + (2 * n64 * 144 + 112 + 4 * w) * 1024, lwo = lds0 + lwo_of(w);
#define WO(ph, i) DM(ph, lwo, (i) * 1024, swo + ((i) & 3) * 1024 + ((i) >> 2) * (144 * 1024))
  STAMPV(59);
  vf0 = WS_F4 + xcd * 32 * FS + (l16 & 496) * (FS / 16); PIN(vf0);
  asm volatile("s_barrier" ::: "memory");
  STAMPV(60);
  PRIO(3);
  G2P(0, fa, fb, (WO, 0), (DO, POLL(vf0)), (WO, 1), (WO, 2), N_, (WO, 3)); G2P(2, fb, fa, (WO, 4), N_, (WO, 5), (WO, 6), N_, (WO, 7)); ADV();
  PRIO(2);
  bf16x8 fc[4];
  typedef __attribute__((address_space(3))) f32x4* l4p;
  typedef __attribute__((address_space(3))) float* l1p;
  lptr xw = lds + L_PRIV + w * 2048 + l16, xl = lds + 65536 + w * 256 + l16 / 4; PIN(xw); PIN(xl);
  G2P(0, fa, fb, (LD, fc[0], sow + 1024), N_, (LD, fc[2], soh + 1024), (LD, fc[1], sow + 145 * 1024), N_, (LD, fc[3], soh + dh + 1024));
  PRIO(1);
  T2(2, (LD, fa[3], soh + dh - 1024), NOP_, N_, N_, N_, NOP_, N_);
  PRIO(0);
  auto xchg = [&]() { PIN(o0); *(l4p)(xw) = (f32x4){o0[0], o0[1], o0[2], o0[3]}; *(l4p)(xw + 1024) = (f32x4){o0[4], o0[5], o0[6], o0[7]}; *(l1p)(xl) = o0[8]; };
  T2(3, N_, NOP_, N_, N_, N_, xchg(), N_);
#undef GM
#define GM(g, a, b) do { g = MFMA(a, b, g); PIN(g); SBAR(); } while (0)
  o1 = SMF(vb, pb, o1, idx);
  STAMP(9);
  xw = (lptr)((unsigned)(size_t)(xw - L_PRIV) ^ 8192) + L_PRIV; xl = (lptr)((unsigned)(size_t)xl ^ 1024); PIN(xw); PIN(xl); SBAR();
  LBAR();
  float oo[9];
  {
    const f32x4 a = *(l4p)(xw), c = *(l4p)(xw + 1024); const float lp = *(l1p)(xl);
    SBAR(); asm volatile("" :: "v"(o1));
    oo[8] = o1[8] + lp;
    float sm = 0.f;
#pragma unroll
    for (int i = 0; i < 4; ++i) { oo[i] = o1[i] + a[i]; oo[4 + i] = o1[4 + i] + c[i]; }
#pragma unroll
    for (int i = 0; i < 8; ++i) sm += __builtin_fabsf(oo[i]);
    const bool ok = !FORCE_FB && __builtin_amdgcn_fmed3f(oo[8], 0x1p-104f, 0x1p120f) == oo[8] && __builtin_amdgcn_fmed3f(sm, 0x1p-110f, 0x1p127f) == sm;
    if (__builtin_expect(__builtin_amdgcn_ballot_w64(!ok) != 0, 0)) {
      const unsigned sk = WS_KV + ah * 131072, vv = l16 & 256 ? 0x7fffff00u : (l16 & 512) / 2 + (l16 & 240), one = (l16 & 432) == 256 ? 0x3f803f80u : 0u;
      float m = -INFINITY;
#pragma unroll 1
      for (int t = 0; t < 64; ++t) m = max16(m, MFMA(ld16c(ws, l16, sk + t * 1024), q1, z));
      m = -hmax(m);
#pragma unroll
      for (int i = 0; i < 16; ++i) nm0[i] = m;
      o0 = z;
#pragma unroll 1
      for (int t = 0; t < 64; ++t) {
        const f32x16 s_ = MFMA(ld16c(ws, l16, sk + t * 1024), q1, nm0);
        const u32x4 v0 = __builtin_bit_cast(u32x4, ld16c(ws, vv, sk + 65536 + t * 1024)) | one, v1 = __builtin_bit_cast(u32x4, ld16c(ws, vv, sk + 65536 + t * 1024 + 512)) | one;
#pragma unroll
        for (int i = 0; i < 8; ++i) { pa[i] = __builtin_amdgcn_exp2f(s_[i]); pb[i] = __builtin_amdgcn_exp2f(s_[8 + i]); }
        o0 = SMF(__builtin_bit_cast(bf16x8, v0), pa, o0, idx); o0 = SMF(__builtin_bit_cast(bf16x8, v1), pb, o0, idx);
      }
      float unit = 1.f; PIN(unit);
#pragma unroll
      for (int i = 0; i < 9; ++i) oo[i] = o0[i] * unit;
    }
  }
  { const float rl = __builtin_amdgcn_rcpf(oo[8]);
    u32x4 ov = {cvtpa(oo[0] * rl, oo[1] * rl), cvtpa(oo[2] * rl, oo[3] * rl), cvtpa(oo[4] * rl, oo[5] * rl), cvtpa(oo[6] * rl, oo[7] * rl)};
#pragma unroll
    for (int i = 0; i < 4; ++i) ov[i] = ov[i] < 0xfffffffeu ? ov[i] : 0xfffffffeu;
#pragma unroll
    for (int i = 0; i < 4; ++i) { PIN(fa[i]); PIN(fb[i]); PIN(fc[i]); }
    WAITV(0);
    st16(ov, ws, l16, WS_O + (qown * 32 + ah) * 1024); }
  WAITF(1);
  STAMP(45);
  asm volatile("s_barrier" ::: "memory");
  const unsigned vout = (tb * 64 + w * 8) * 1024 + n64 * 128 + (l16 & 896) * 8 + (l16 & 112);
  const u32x4 xres = __builtin_amdgcn_raw_buffer_load_b128(rx, vout, 0, 0);
  {
    lptr d = lds + L_PAIR + w * 8704 + l16; PIN(d);
    GM(g01, fb[0], fb[3]); GM(g11, fb[1], fb[3]); GM(g01, fa[0], fa[3]); GM(g11, fa[1], fa[3]); GM(g01, fc[0], fc[3]); GM(g11, fc[1], fc[3]);
    GM(g00, fb[0], fb[2]); GM(g10, fb[1], fb[2]);
#define QUAD(g, i) (f32x4){g[4 * (i)], g[4 * (i) + 1], g[4 * (i) + 2], g[4 * (i) + 3]}
    *(l4p)(d) = QUAD(g01, 0); *(l4p)(d + 1024) = QUAD(g01, 1); SBAR(); GM(g00, fa[0], fa[2]);
    *(l4p)(d + 2048) = QUAD(g01, 2); *(l4p)(d + 3072) = QUAD(g01, 3); SBAR(); GM(g10, fa[1], fa[2]);
    *(l4p)(d + 4096) = QUAD(g11, 0); *(l4p)(d + 5120) = QUAD(g11, 1); SBAR(); GM(g00, fc[0], fc[2]);
    *(l4p)(d + 6144) = QUAD(g11, 2); *(l4p)(d + 7168) = QUAD(g11, 3); SBAR(); GM(g10, fc[1], fc[2]);
  }
  LBAR();
  {
    const int hq = (w + n64) & 3;
    const unsigned soo = WS_O + ((2 * tb + kh) * 32 + 8 * hq) * 1024; unsigned spins; unsigned long long ret; u32x16 ofa, ofb;
#define OL0 "buffer_load_dwordx4 v[0:3], %[l], %[r], %[o0] offen sc1\n\t"
#define OL1 "buffer_load_dwordx4 v[4:7], %[l], %[r], %[o0] offen offset:1024 sc1\n\t"
#define OL2 "buffer_load_dwordx4 v[8:11], %[l], %[r], %[o0] offen offset:2048 sc1\n\t"
#define OL3 "buffer_load_dwordx4 v[12:15], %[l], %[r], %[o0] offen offset:3072 sc1\n\t"
#define OL4 "buffer_load_dwordx4 v[16:19], %[l], %[r], %[o1] offen sc1\n\t"
#define OL5 "buffer_load_dwordx4 v[20:23], %[l], %[r], %[o1] offen offset:1024 sc1\n\t"
#define OL6 "buffer_load_dwordx4 v[24:27], %[l], %[r], %[o1] offen offset:2048 sc1\n\t"
#define OL7 "buffer_load_dwordx4 v[28:31], %[l], %[r], %[o1] offen offset:3072 sc1\n\t"
    asm volatile(OL0 OL1 OL2 OL3 OL4 OL5 OL6 OL7 : "=&{v[0:15]}"(ofa), "=&{v[16:31]}"(ofb) : [l] "v"(l16), [r] "s"(ws), [o0] "s"(soo), [o1] "s"(soo + 4096) : "memory");
    const lptr s = lds + L_PAIR + (w ^ 4) * 8704 + l16, wl0 = lds + lwo_of(2 * hq) + l16, wl1 = lds + lwo_of(2 * hq + 1) + l16;
    f32x4 pr[8]; bf16x8 wa_[8], wb_[8];
#pragma unroll
    for (int i = 0; i < 8; ++i) pr[i] = *(l4p)(s + i * 1024);
#pragma unroll
    for (int i = 0; i < 8; ++i) { wa_[i] = LDS8((i < 4 ? wl0 : wl1) + (i & 3) * 1024); wb_[i] = LDS8((i < 4 ? wl0 : wl1) + (i & 3) * 1024 + 4096); }
    SBAR();
#pragma unroll
    for (int i = 0; i < 16; ++i) { g00[i] += pr[i >> 2][i & 3]; g10[i] += pr[4 + (i >> 2)][i & 3]; }
    PIN(g00); PIN(g10);
#define OCHK(r0, r1, r2, r3) "v_max3_u32 v32, v" #r0 ", v" #r1 ", v" #r2 "\n\tv_max_u32 v32, v32, v" #r3 "\n\tv_cmp_eq_u32 vcc, -1, v32\n\t"
#define OSTEP(i, n, r0, r1, r2, r3) \
    "s_getpc_b64 %[ret]\n\ts_waitcnt vmcnt(" #n ")\n\t" OCHK(r0, r1, r2, r3) "s_cbranch_vccnz 3f\n\t" \
    "v_mfma_f32_32x32x16_bf16 %[g0], %[a" #i "], v[" #r0 ":" #r3 "], %[g0]\n\tv_mfma_f32_32x32x16_bf16 %[g1], %[b" #i "], v[" #r0 ":" #r3 "], %[g1]\n\t"
#define ORE(r0, r1, r2, r3, load) OCHK(r0, r1, r2, r3) "s_cbranch_vccz 4f\n\t" load "4:\n\t"
    static_assert(SPIN_CAP == 0x8000, "literal below");
    asm volatile("s_mov_b32 %[sp], 0\n\t"
                 OSTEP(0, 7, 0, 1, 2, 3) OSTEP(1, 6, 4, 5, 6, 7) OSTEP(2, 5, 8, 9, 10, 11) OSTEP(3, 4, 12, 13, 14, 15)
                 OSTEP(4, 3, 16, 17, 18, 19) OSTEP(5, 2, 20, 21, 22, 23) OSTEP(6, 1, 24, 25, 26, 27) OSTEP(7, 0, 28, 29, 30, 31)
                 "s_branch 2f\n"
                 "3:\n\ts_add_u32 %[sp], %[sp], 1\n\ts_cmp_gt_u32 %[sp], 0x8000\n\ts_cbranch_scc1 2f\n\ts_waitcnt vmcnt(0)\n\t"
                 ORE(0, 1, 2, 3, OL0) ORE(4, 5, 6, 7, OL1) ORE(8, 9, 10, 11, OL2) ORE(12, 13, 14, 15, OL3)
                 ORE(16, 17, 18, 19, OL4) ORE(20, 21, 22, 23, OL5) ORE(24, 25, 26, 27, OL6) ORE(28, 29, 30, 31, OL7)
                 "s_waitcnt vmcnt(0)\n\ts_setpc_b64 %[ret]\n"
                 "2:\n\ts_waitcnt vmcnt(0)\n\ts_nop 11"
                 : [g0] "+v"(g00), [g1] "+v"(g10), "+{v[0:15]}"(ofa), "+{v[16:31]}"(ofb), [sp] "=&s"(spins), [ret] "=&s"(ret)
                 : [l] "v"(l16), [r] "s"(ws), [o0] "s"(soo), [o1] "s"(soo + 4096),
                   [a0] "v"(wa_[0]), [a1] "v"(wa_[1]), [a2] "v"(wa_[2]), [a3] "v"(wa_[3]), [a4] "v"(wa_[4]), [a5] "v"(wa_[5]), [a6] "v"(wa_[6]), [a7] "v"(wa_[7]),
                   [b0] "v"(wb_[0]), [b1] "v"(wb_[1]), [b2] "v"(wb_[2]), [b3] "v"(wb_[3]), [b4] "v"(wb_[4]), [b5] "v"(wb_[5]), [b6] "v"(wb_[6]), [b7] "v"(wb_[7])
                 : "memory", "scc", "vcc", "v32");
  }
  STAMP(12);
  { const lptr d = lds + L_PAIR + (w ^ 4) * 8704 + l16;
#pragma unroll
    for (int i = 0; i < 4; ++i) { *(l4p)(d + i * 1088) = QUAD(g00, i); *(l4p)(d + (4 + i) * 1088) = QUAD(g10, i); } }
  lptr rs = lds + L_PAIR + (kh ^ 1) * (4 * 8704) + (l16 & 112) * 68 + (w & 3) * 128 + (l16 & 896) / 8; PIN(rs);
  f32x4 ra = {bflo(xres[0]), bfhi(xres[0]), bflo(xres[1]), bfhi(xres[1])}, rc = {bflo(xres[2]), bfhi(xres[2]), bflo(xres[3]), bfhi(xres[3])}; PIN(ra); PIN(rc);
  LBAR();
  STAMP(13);
  {
    f32x4 pa_[4], pc_[4];
#pragma unroll
    for (int i = 0; i < 4; ++i) { pa_[i] = *(l4p)(rs + i * 8704); pc_[i] = *(l4p)(rs + i * 8704 + 512); }
    SBAR();
#pragma unroll
    for (int i = 0; i < 4; ++i) { ra += pa_[i]; rc += pc_[i]; PIN(ra); PIN(rc); }
    const u32x4 r = {cvtpk(ra[0], ra[1]), cvtpk(ra[2], ra[3]), cvtpk(rc[0], rc[1]), cvtpk(rc[2], rc[3])};
    __builtin_amdgcn_raw_buffer_store_b128(r, mk(pout, 2048 * 1024), vout, 0, 0);
  }
  STAMP(14);
  STAMP_END();
  asm volatile(".Lfb_end:");
}

extern "C" void kernel_launch(void* const* d_in, const int* in_sizes, int n_in, void* d_out, int out_size, void* d_ws, size_t ws_size, hipStream_t stream) {
  (void)hipFuncSetAttribute((const void*)fused_block, hipFuncAttributeMaxDynamicSharedMemorySize, LDS_BYTES);
  hipLaunchKernelGGL(fused_block, dim3(256), dim3(512), LDS_BYTES, stream, d_in[0], d_in[1], d_in[2], d_in[4], d_in[5], d_in[6], d_out, d_ws);
}
```

```cpp
#include <hip/hip_runtime.h>
#include <hip/hip_bf16.h>

typedef __bf16 bf16x8 __attribute__((ext_vector_type(8)));
typedef __bf16 bf16x16 __attribute__((ext_vector_type(16)));
typedef __bf16 bf16x2 __attribute__((ext_vector_type(2)));
typedef short s16x4 __attribute__((ext_vector_type(4)));
typedef float f32x16 __attribute__((ext_vector_type(16)));
typedef float f32x8 __attribute__((ext_vector_type(8)));
typedef float f32x4 __attribute__((ext_vector_type(4)));
typedef float f32x2 __attribute__((ext_vector_type(2)));
typedef unsigned u32x4 __attribute__((ext_vector_type(4)));
typedef unsigned u32x2 __attribute__((ext_vector_type(2)));
typedef unsigned u32x16 __attribute__((ext_vector_type(16)));
typedef __amdgpu_buffer_rsrc_t rsrc_t;
typedef __attribute__((address_space(3))) char* lptr;
#define DI __device__ __forceinline__
#define SBAR() __builtin_amdgcn_sched_barrier(0)
#define MFMA(a, b, c) __builtin_amdgcn_mfma_f32_32x32x16_bf16(a, b, c, 0, 0, 0)
#define SMF(a, b, c, idx) __builtin_amdgcn_smfmac_f32_32x32x32_bf16(a, __builtin_bit_cast(bf16x16, b), c, idx, 0, 0)
#define LDS8(p) (*(const __attribute__((address_space(3))) bf16x8*)(p))
#define PIN(x) asm volatile("" : "+v"(x))
#define LANE(n) unsigned n = l16; PIN(n); n >>= 4

constexpr unsigned WS_EPOCH = 0;
constexpr unsigned FS = 128;
constexpr unsigned WS_F0 = 262144, WS_F1 = WS_F0 + 256 * FS, WS_F2 = WS_F1 + 256 * FS;
constexpr unsigned WS_F3 = WS_F2 + 256 * FS;
constexpr unsigned WS_STAT = 32768;
constexpr unsigned WS_CS = 65536;
constexpr unsigned WS_Q = 1u << 20;
constexpr unsigned WS_KV = WS_Q + (2u << 20);
constexpr unsigned WS_O = WS_KV + (4u << 20);
constexpr unsigned WS_W2 = WS_O + (2u << 20);
constexpr unsigned WS_H = WS_W2 + (3u << 20);
constexpr unsigned WS_END = WS_H + (8u << 20);
constexpr int U_XA = 0, U_G1 = 24576, U_XB = 49152, U_G2 = 73728, U_G0 = 98304, UD = 49152;
constexpr int L_V = 73728, L_PRIV = 139264, L_ONES = 155648, L_EYE = L_ONES + 5120, LDS_BYTES = 163840;
constexpr unsigned SPIN_CAP = 1u << 15;
constexpr float REF_OFF = 100.f;
constexpr int K_F = 12, K_P = 19, K_C = 25;

DI rsrc_t mk(const void* p, unsigned bytes) { return __builtin_amdgcn_make_buffer_rsrc((void*)p, (short)0, (int)bytes, 0x00020000); }
DI unsigned cvtpk(float lo, float hi) { f32x2 v = {lo, hi}; return __builtin_bit_cast(unsigned, __builtin_convertvector(v, bf16x2)); }
DI unsigned cvtpa(float lo, float hi) { unsigned r; asm("v_cvt_pk_bf16_f32 %0, %1, %2" : "=v"(r) : "v"(lo), "v"(hi)); return r; }
DI float bflo(unsigned u) { return __uint_as_float(u << 16); }
DI float bfhi(unsigned u) { return __uint_as_float(u & 0xffff0000u); }
DI float rbf(float f) { return bflo(cvtpk(f, f)); }
DI float max3(float a, float b, float c) { return __builtin_fmaxf(__builtin_fmaxf(a, b), c); }
DI lptr lxor(lptr p, unsigned m) { return (lptr)((size_t)p ^ m); }
DI s16x4 trd(lptr p) { return __builtin_amdgcn_ds_read_tr16_b64_v4i16((__attribute__((address_space(3))) s16x4*)p); }
DI bf16x8 trfrag(lptr p, int step) { s16x4 a = trd(p), b = trd(p + step); return __builtin_bit_cast(bf16x8, __builtin_shufflevector(a, b, 0, 1, 2, 3, 4, 5, 6, 7)); }
#define dma(base, imm, voff, r, soff) asm volatile("s_add_i32 m0, %0, %4\n\ts_nop 0\n\tbuffer_load_dwordx4 %1, %2, %3 offen lds" :: "s"(base), "v"(voff), "s"(r), "s"(soff), "i"(imm) : "memory", "scc")
#define dma_go(voff, r, soff) asm volatile("buffer_load_dwordx4 %0, %1, %2 offen lds" :: "v"(voff), "s"(r), "s"(soff) : "memory")
#define dma_nt(base, imm, voff, r, soff) asm volatile("s_add_i32 m0, %0, %4\n\ts_nop 0\n\tbuffer_load_dwordx4 %1, %2, %3 offen nt lds" :: "s"(base), "v"(voff), "s"(r), "s"(soff), "i"(imm) : "memory", "scc")
#define ALD4(dst, voff, r, soff) asm volatile("buffer_load_dwordx4 %0, %1, %2, %3 offen" : "=v"(dst) : "v"(voff), "s"(r), "s"(soff) : "memory")
#define ALD4C(dst, voff, r, soff) asm volatile("buffer_load_dwordx4 %0, %1, %2, %3 offen sc1" : "=v"(dst) : "v"(voff), "s"(r), "s"(soff) : "memory")
#define ALD1C(dst, voff, r, soff) asm volatile("buffer_load_dword %0, %1, %2, %3 offen sc1" : "=v"(dst) : "v"(voff), "s"(r), "s"(soff) : "memory")
#define CLD(op, dst, voff, r, soff, lim) asm volatile("s_cmp_ge_u32 %4, %5\n\ts_cbranch_scc1 1f\n\t" op "\n1:" : "=v"(dst) : "v"(voff), "s"(r), "s"(soff), "s"(w), "i"(lim) : "memory", "scc")
#define POLL(voff) CLD("buffer_load_dwordx2 %0, %1, %2, %3 offen sc1", pl, voff, ws, 0, 1)
#define WAITV(n) asm volatile("s_waitcnt vmcnt(%0)" :: "i"(n) : "memory")
#define WAITBAR(n) asm volatile("s_waitcnt vmcnt(%0) lgkmcnt(0)\n\ts_barrier" :: "i"(n) : "memory")
#define LBAR() asm volatile("s_waitcnt lgkmcnt(0)\n\ts_barrier" ::: "memory")
DI void st16(u32x4 v, rsrc_t r, unsigned voff, unsigned soff) {
  asm volatile("buffer_store_dwordx4 %0, %1, %2, %3 offen sc1\n\ts_nop 1" :: "v"(v), "v"(voff), "s"(r), "s"(soff) : "memory"); }
DI void st4(unsigned v, rsrc_t r, unsigned voff, unsigned soff) { __builtin_amdgcn_raw_buffer_store_b32(v, r, voff, soff, 16); }
DI bf16x8 ld16c(rsrc_t r, unsigned voff, unsigned soff) { return __builtin_bit_cast(bf16x8, __builtin_amdgcn_raw_buffer_load_b128(r, voff, soff, 16)); }
template <int CTRL> DI float dppadd(float v) { return v + __int_as_float(__builtin_amdgcn_update_dpp(0, __float_as_int(v), CTRL, 0xf, 0xf, true)); }
#define WAITF(n) do { unsigned sp_; asm volatile("s_cmp_lg_u32 %[w], 0\n\ts_cbranch_scc1 9f\n\ts_mov_b32 %[sp], 0\n\ts_waitcnt vmcnt(%[c])\n" \
    "1:\n\tv_cmp_ne_u64 vcc, %[t], %[p]\n\ts_cbranch_vccz 9f\n\ts_add_u32 %[sp], %[sp], 1\n\ts_cmp_gt_u32 %[sp], %[cap]\n\ts_cbranch_scc1 9f\n\ts_sleep 2\n\t" \
    "buffer_load_dwordx2 %[p], %[v], %[r], 0 offen sc1\n\ts_waitcnt vmcnt(0)\n\ts_branch 1b\n9:" \
    : [p] "+v"(pl), [sp] "=&s"(sp_) : [v] "v"(vf0), [r] "s"(ws), [t] "s"(tag), [w] "s"(w), [c] "i"(n), [cap] "i"(SPIN_CAP) : "memory", "scc", "vcc"); } while (0)
#define PUT(val, soff, who) do { unsigned long long t_; asm volatile("s_cmp_lg_u32 %4, 0\n\ts_cbranch_scc1 9f\n\tv_mov_b64 %0, %1\n\ts_mov_b64 exec, 1\n\t" \
    "buffer_store_dwordx2 %0, off, %2, %3 sc1\n\ts_mov_b64 exec, -1\n9:" : "=&v"(t_) : "s"(val), "s"(ws), "s"(soff), "s"(who) : "memory", "scc"); } while (0)
#define WAITV2(n0, n) asm volatile("s_waitcnt vmcnt(%1)\n\ts_cmp_eq_u32 %0, 0\n\ts_cbranch_scc1 9f\n\ts_waitcnt vmcnt(%2)\n9:" :: "s"(w), "i"(n0), "i"(n) : "memory", "scc")

DI u32x4 pack8(const f32x16& a, int o, float s) {
  return (u32x4){cvtpa(a[o] * s, a[o + 1] * s), cvtpa(a[o + 2] * s, a[o + 3] * s), cvtpa(a[o + 4] * s, a[o + 5] * s), cvtpa(a[o + 6] * s, a[o + 7] * s)}; }
DI u32x4 glu8(const f32x16& a, float nc, float sd) {
  float h[8];
#pragma unroll
  for (int i = 0; i < 8; ++i) { const float e = __builtin_amdgcn_exp2f(a[i] * nc); h[i] = a[8 + i] * __builtin_amdgcn_rcpf(__builtin_fmaf(e, sd, sd)); }
  return (u32x4){cvtpa(h[0], h[1]), cvtpa(h[2], h[3]), cvtpa(h[4], h[5]), cvtpa(h[6], h[7])}; }

typedef unsigned long long u64x8 __attribute__((ext_vector_type(8)));
__global__ __launch_bounds__(512, 2) void fused_block(const void* a_x, const void* a_wqkv, const void* a_wo, const void* a_wg, const void* a_wu, const void* a_wd, void* a_out, void* a_ws) {
  extern __shared__ __attribute__((aligned(16))) char smem[];
  u64x8 arg;
  asm volatile("s_load_dwordx16 %0, %1, 0x0" : "=s"(arg) : "s"(__builtin_amdgcn_kernarg_segment_ptr()));
  const lptr lds = (lptr)smem;
  const unsigned lds0 = (unsigned)(size_t)lds;
  const int tid = threadIdx.x, lane = tid & 63, w = __builtin_amdgcn_readfirstlane(tid >> 6);
  const int b = blockIdx.x, xcd = b & 7, bi = b >> 3;
  const int r32 = lane & 31, hi = lane >> 5;
  if (w == 0) {
    unsigned long long pc; unsigned len; asm volatile("s_getpc_b64 %0\n.Lfb_pc:\n\ts_mov_b32 %1, .Lfb_end-.Lfb_pc" : "=s"(pc), "=s"(len));
    const rsrc_t rc = mk((const void*)pc, len);
    const unsigned v = lane < 8 ? (bi * 8 + lane) * 128 : 0x7fffff00u;
    asm volatile("s_mov_b32 m0, %0\n\ts_nop 0\n\tbuffer_load_dword %1, %2, 0 offen lds" :: "s"(lds0 + LDS_BYTES - 256), "v"(v), "s"(rc) : "memory"); }
  const int rb = (xcd & 1) * 8 + (bi & 7), Q4 = xcd >> 1, sj = bi >> 3, set = Q4 * 4 + sj;
  const int rot = 2 * (xcd & 1) + (bi & 4), pos = (bi >> 1) & 1, half = bi & 1, kpart = rot + pos;
  const unsigned l3 = lane >> 3, l7 = lane & 7, l16 = lane * 16;
  const int wx = (w + 2 * sj) & 7, ww = (w + 2 * bi) & 7;
  unsigned vx0 = (rb * 128 + wx * 16 + l3) * 1024 + ((l7 ^ (lane >> 4)) * 16), vx1 = (vx0 + 8192) ^ 64;
  unsigned mx = lds0 + (wx >> 2) * UD + (wx & 3) * 2048, mw = lds0 + ww * 1024;
  const bool tb_ = sj == 0;
  const int p1 = 7 * Q4 + 2 * sj - 1, p2 = p1 + 1;
  const unsigned col0 = 128 * Q4 + (tb_ ? 1024 : sj * 512 - 448);
  const unsigned str12 = tb_ ? 3072 : 3584, wr = ww * 8 + l3, wpc = (l7 ^ ((lane >> 2) & 4)) * 16;
  unsigned vg0 = wr * 3072 + wpc + col0 * 2, vg1 = wr * str12 + wpc + (tb_ ? 1024 + 256 * Q4 : p1 * 128), vg2 = (vg1 - (tb_ ? 1024 : 0)) ^ 32;
  unsigned so0 = rot * 128, so1 = rot * (64 * 3072), so2 = rot * 64 * str12;
  PIN(vx0); PIN(vx1); PIN(vg0); PIN(vg1); PIN(vg2); asm volatile("" : "+s"(mx), "+s"(mw), "+s"(so0), "+s"(so1), "+s"(so2));
  SBAR();
  asm volatile("s_waitcnt lgkmcnt(0)" : "+s"(arg) :: "memory");
  const void *px = (const void*)arg[0], *pwqkv = (const void*)arg[1], *pwo = (const void*)arg[2], *pwg = (const void*)arg[3], *pwu = (const void*)arg[4], *pwd = (const void*)arg[5];
  void *pout = (void*)arg[6], *pws = (void*)arg[7];
  rsrc_t ws = mk(pws, WS_END), rx = mk(px, 2048 * 1024), rg0 = mk(pwqkv, 512 * 3072);
  rsrc_t rg1 = mk(tb_ ? pwqkv : pwg, 512 * str12), rg2 = mk(tb_ ? pwqkv : pwu, 512 * str12);
  rsrc_t rg3 = mk(pwg, 512 * 3584), rg4 = mk(pwu, 512 * 3584);
#define KC(c) (unsigned)(((c) + rot) & 7)
#define SL(c) (((c) % 3) * 8192)
#define DMA1(c, i) do { \
    if ((i) == 0) dma(mx, SL(c), vx0, rx, KC(c) * 128); if ((i) == 1) dma(mx, SL(c) + 1024, vx1, rx, KC(c) * 128); \
    if ((i) == 2) dma(mw, SL(c) + U_G0, vg0, rg0, KC(c) * (64 * 3072)); if ((i) == 3) dma(mw, SL(c) + U_G1, vg1, rg1, KC(c) * 64 * str12); \
    if ((i) == 4) dma(mw, SL(c) + U_G2, vg2, rg2, KC(c) * 64 * str12); } while (0)
  unsigned sd_;
#define M0S(base, imm, off) do { SBAR(); sd_ = (off); asm volatile("s_add_i32 m0, %1, %2" :: "s"(sd_), "s"(base), "i"(imm) : "memory", "scc"); } while (0)
#define M01(c, i) M0S((i) < 2 ? mx : mw, SL(c) + ((i) == 0 ? 0 : (i) == 1 ? 1024 : (i) == 2 ? U_G0 : (i) == 3 ? U_G1 : U_G2), KC(c) * ((i) < 2 ? 128 : (i) == 2 ? 64 * 3072 : 64 * str12))
#define GO1(c, i) do { if ((i) == 0) dma_go(vx0, rx, sd_); if ((i) == 1) dma_go(vx1, rx, sd_); if ((i) == 2) dma_go(vg0, rg0, sd_); if ((i) == 3) dma_go(vg1, rg1, sd_); if ((i) == 4) dma_go(vg2, rg2, sd_); } while (0)
#define M02(c, i) M0S(mw, SL(c) + ((i) & 2 ? U_XA : U_G1) + ((i) & 1) * UD, KC(2 * ((c) - 8) + ((i) >> 1)) * (64 * 3584))
#define GO2(c, i) do { if ((i) & 1) dma_go(vg4, rg4, sd_); else dma_go(vg3, rg3, sd_); } while (0)
#define DMA2(c, i) do { constexpr int sl_ = SL(c) + ((i) & 2 ? U_XA : U_G1); const unsigned kc_ = KC(2 * ((c) - 8) + ((i) >> 1)) * (64 * 3584); \
    if ((i) & 1) dma(mw, sl_ + UD, vg4, rg4, kc_); else dma(mw, sl_, vg3, rg3, kc_); } while (0)
  dma(mx, 0, vx0, rx, so0); dma(mx, 1024, vx1, rx, so0); SBAR(); dma(mw, U_G0, vg0, rg0, so1); SBAR(); dma(mw, U_G1, vg1, rg1, so2); dma(mw, U_G2, vg2, rg2, so2); SBAR();
  asm volatile("" : "+s"(ws), "+s"(rx), "+s"(rg0), "+s"(rg1), "+s"(rg2), "+s"(rg3), "+s"(rg4));
  unsigned vg3 = wr * 3584 + wpc + p2 * 128, vg4 = vg3 ^ 32; PIN(vg3); PIN(vg4);
  const int tt = w & 3, cg = w >> 2;
  const int q4 = (lane & 15) >> 2, p4 = lane & 3, g16 = (lane >> 4) & 1, swr = (q4 >> 1) * 4;
  const int wrow = (8 * hi + q4) * 128 + (p4 & 1) * 8;
  const int pq = 4 * cg + 2 * g16 + (p4 >> 1);
  const int gl0 = g16 * UD + (pq ^ swr) * 16, gl1 = gl0 ^ 32;
  lptr wp0 = lds + U_G0 + wrow + (pq ^ swr) * 16;
  lptr wp1 = lds + wrow + (tb_ ? (pq ^ swr) * 16 : gl0);
  lptr wp2 = lds + wrow + (tb_ ? UD + (pq ^ swr ^ 2) * 16 : gl1);
  lptr xp[4];
#pragma unroll
  for (int s = 0; s < 4; ++s) { xp[s] = lds + (tt >> 1) * UD + (tt & 1) * 4096 + r32 * 128 + (((2 * s + hi) ^ ((r32 >> 1) & 7)) * 16); PIN(xp[s]); }
  PIN(wp0); PIN(wp1); PIN(wp2);
  const int ah = xcd * 4 + (bi & 3), aq = bi >> 2, kh = w >> 2;
  const int qown = aq * 8 + 2 * (w & 3) + kh;
  const unsigned csb = WS_CS + set * 10240;
  auto colsum = [&](lptr cp, unsigned so) {
    const __bf16 o1 = (__bf16)1.0f; const bf16x8 ones = {o1, o1, o1, o1, o1, o1, o1, o1};
    const bf16x8 f0 = trfrag(cp, 512), f1 = trfrag(cp + 2048, 512), f2 = trfrag(cp + 4096, 512), f3 = trfrag(cp + 6144, 512);
    const f32x16 cs = MFMA(ones, f3, MFMA(ones, f2, MFMA(ones, f1, MFMA(ones, f0, (f32x16){}))));
    const float ch = rbf(cs[0]);
    st4(cvtpk(ch, cs[0] - ch), ws, l16 < 512 ? l16 : 0x7fffff00u, csb + so + (kpart >> 2) * 512 + (kpart & 3) * 4);
  };
  unsigned vf0 = 0;
  u32x2 ep; u32x4 xr; unsigned e0, e1; unsigned long long tag;
  const unsigned srow = rb * 128 + set * 8 + w;
  float sv[8], sa, sb, smean;
  auto swapadd16 = [](float v) { auto r = __builtin_amdgcn_permlane16_swap(__float_as_uint(v), __float_as_uint(v), false, false); return __uint_as_float(r[0]) + __uint_as_float(r[1]); };
  auto swapadd32 = [](float v) { auto r = __builtin_amdgcn_permlane32_swap(__float_as_uint(v), __float_as_uint(v), false, false); return __uint_as_float(r[0]) + __uint_as_float(r[1]); };
#define STATS(i) do { \
    if ((i) == 0 || (i) == 1) { sv[4 * ((i) & 1)] = bflo(xr[2 * ((i) & 1)]); sv[4 * ((i) & 1) + 1] = bfhi(xr[2 * ((i) & 1)]); sv[4 * ((i) & 1) + 2] = bflo(xr[2 * ((i) & 1) + 1]); sv[4 * ((i) & 1) + 3] = bfhi(xr[2 * ((i) & 1) + 1]); \
      PIN(sv[4 * ((i) & 1)]); PIN(sv[4 * ((i) & 1) + 1]); PIN(sv[4 * ((i) & 1) + 2]); PIN(sv[4 * ((i) & 1) + 3]); } \
    if ((i) == 2) { sa = (sv[0] + sv[1]) + sv[2]; PIN(sa); sb = (sv[3] + sv[4]) + sv[5]; PIN(sb); } \
    if ((i) == 3) { sa = ((sa + sv[6]) + sv[7]) + sb; PIN(sa); } \
    if ((i) == 4 || (i) == 14) { sa = dppadd<0x121>(sa); PIN(sa); } if ((i) == 5 || (i) == 15) { sa = dppadd<0x122>(sa); PIN(sa); } \
    if ((i) == 6 || (i) == 16) { sa = dppadd<0x124>(sa); PIN(sa); } if ((i) == 7 || (i) == 17) { sa = dppadd<0x128>(sa); PIN(sa); } \
    if ((i) == 8 || (i) == 18) { sa = swapadd16(sa); PIN(sa); } \
    if ((i) == 9) { smean = swapadd32(sa) * (1.f / 512); PIN(smean); sa = 0.f; } \
    if ((i) >= 10 && (i) <= 13) { const float d_ = sv[(2 * (i) - 20) & 7] - smean, e_ = sv[(2 * (i) - 19) & 7] - smean; sa = __builtin_fmaf(e_, e_, __builtin_fmaf(d_, d_, sa)); PIN(sa); } \
    if ((i) == 19) { sa = rbf(swapadd32(sa) * (1.f / 512)); PIN(sa); } \
    if ((i) == 20) { sa = __builtin_amdgcn_sqrtf(rbf(sa + 1e-5f)); PIN(sa); } \
    if ((i) == 21) st4(cvtpk(-smean, sa), ws, lane ? 0x7fffff00u : 0u, WS_STAT + srow * 4); } while (0)
  bf16x8 xf[32], wa[3], wb[3];
  f32x16 acc0 = {}, acc1 = {}, acc2 = {};
  u32x2 pl; unsigned stat; u32x4 cs0, cs1, cs2;
#define RD1(W, ks) do { xf[ks] = LDS8(xp[(ks) & 3] + SL((ks) >> 2)); W[0] = trfrag(wp0 + SL((ks) >> 2) + ((ks) & 3) * 2048, 512); } while (0)
#define RD1B(W, ks) W[1] = trfrag(wp1 + U_G1 + SL((ks) >> 2) + ((ks) & 3) * 2048, 512)
#define RD1C(W, ks) W[2] = trfrag(wp2 + U_G1 + SL((ks) >> 2) + ((ks) & 3) * 2048, 512)
#define RD2A(W, ks) W[0] = trfrag(wp1 + SL(8 + ((ks) >> 3)) + ((ks) & 4 ? U_XA : U_G1) + ((ks) & 3) * 2048, 512)
#define RD2B(W, ks) W[1] = trfrag(wp2 + SL(8 + ((ks) >> 3)) + ((ks) & 4 ? U_XA : U_G1) + ((ks) & 3) * 2048, 512)
  const int ks2 = bi + 32 * ((w + 7) & 7);
#define W2LOAD() do { if (ks2 < 144) { const rsrc_t r_ = mk(ks2 < 112 ? pwd : pwo, ks2 < 112 ? 1792 * 1024 : 512 * 1024); const unsigned so_ = (ks2 < 112 ? ks2 : ks2 - 112) * 16384 + xcd * 128; \
      LANE(n_); const unsigned v_ = (n_ >> 3) * 1024 + ((n_ & 7) ^ ((n_ >> 2) & 4)) * 16; \
      dma_nt(lds0 + w * 2048, L_PRIV, v_, r_, so_); dma_nt(lds0 + w * 2048, L_PRIV + 1024, v_, r_, so_ + 8192); } } while (0)
#define FLAG(f) PUT(tag, (f) + (set * 16 + rb) * FS, w)
  if (w < 5) { const unsigned o_ = (tid & 11) == 0 ? 0x3f803f80u : 0u;
    *(__attribute__((address_space(3))) u32x4*)(lds + L_ONES + tid * 16) = (u32x4){o_, o_, o_, o_}; }
  else if (w < 7) {
    const unsigned one_ = hi == ((lane >> 2) & 1) ? 0x3f80u << ((lane & 1) * 16) : 0u, dw_ = ((lane >> 1) & 1) + ((lane >> 2) & 6) - 4 * (w - 5);
    *(__attribute__((address_space(3))) u32x4*)(lds + L_ONES + tid * 16) = (u32x4){dw_ == 0 ? one_ : 0u, dw_ == 1 ? one_ : 0u, dw_ == 2 ? one_ : 0u, dw_ == 3 ? one_ : 0u}; }
  SBAR();
  asm volatile("s_barrier" ::: "memory");
  DMA1(1, 0); DMA1(1, 1); DMA1(1, 2); DMA1(1, 3); DMA1(1, 4);
  WAITBAR(5);
  RD1(wa, 0); RD1B(wa, 0); RD1C(wa, 0);
#define STATG(g) do { STATS(2 * ((g) - 12)); STATS(2 * ((g) - 12) + 1); } while (0)
  DMA1(2, 0); DMA1(2, 4);
#define STEP1(ks, WC, WN) do { constexpr int c_ = ((ks) + 1) >> 2, s_ = ((ks) + 1) & 3; \
    if (s_ == 0) { SBAR(); \
      if ((c_ == 1 || c_ == 2) && w < 3 && pos == c_ - 1) \
        colsum(lxor(w == 0 ? wp0 : w == 1 ? wp1 + U_G1 : wp2 + U_G1, half * 64) + SL(c_ - 1), (half * 5 + w) * 1024); \
      if (c_ == 1) { const unsigned z_ = 0; ALD4(xr, l16, rx, srow * 1024); CLD("buffer_load_dwordx2 %0, %1, %2, %3 offen sc1", ep, z_, ws, WS_EPOCH, 1); } \
      if (c_ == 3 && w == 0) { const unsigned v_ = l16 * 224 + p2 * 128;     \
        asm volatile("s_mov_b32 m0, %0\n\ts_nop 0\n\tbuffer_load_dword %1, %2, %3 offen lds" :: "s"(lds0 + LDS_BYTES - 256), "v"(v_), "s"(half ? rg4 : rg3), "s"(kpart * (64 * 3584)) : "memory"); } \
      if (c_ == 5) W2LOAD(); \
      if (c_ == 6 && w == 0) { LANE(n_); vf0 = WS_F0 + (n_ < 16 ? set * 16 + n_ : (n_ & 15) * 16 + rb) * FS; PIN(vf0); } \
      if (c_ == 7) WAITF(4); \
      if (c_ == 5 && ks2 < 144) WAITV(7); else WAITV(c_ == 1 ? 6 : c_ < 7 ? 5 : c_ == 7 ? 4 : 8); LBAR(); \
      if (c_ == 2) { PIN(ep); PIN(xr); e0 = __builtin_amdgcn_readfirstlane(ep[0]); e1 = __builtin_amdgcn_readfirstlane(ep[1]);     \
        tag = (unsigned long long)((e1 ^ 0x85EBCA6Bu) + e0) << 32 | (e0 * 0x9E3779B1u + 0x7F4A7C15u); }     \
      if (c_ == 4) FLAG(WS_F0); \
      if (c_ == 6) POLL(vf0); \
      if (c_ == 7) { const unsigned v_ = (l16 & 496) >> 2; ALD1C(stat, v_, ws, WS_STAT + (rb * 4 + tt) * 128); \
        ALD4C(cs0, l16, ws, csb + cg * 5120); ALD4C(cs1, l16, ws, csb + cg * 5120 + 1024); ALD4C(cs2, l16, ws, csb + cg * 5120 + 2048); } \
      if (c_ == 8) { wp1 = wp0 - U_G0 + (l16 & 256) * (UD / 256); wp2 = lxor(wp1, 32); PIN(wp1); PIN(wp2); } } \
    SBAR(); \
    if ((ks) < 31) RD1(WN, ((ks) + 1) & 31); else RD2A(WN, 0); \
    if (c_ <= 5) M01(c_ + 2, s_); else M02(c_ + 2, s_); STATG(3 * ((ks) - 3) - 1); SBAR(); \
    acc0 = MFMA(WC[0], xf[ks], acc0); SBAR(); \
    if (c_ <= 5) GO1(c_ + 2, s_); else GO2(c_ + 2, s_); \
    if ((ks) < 31) RD1B(WN, ((ks) + 1) & 31); else RD2B(WN, 0); \
    if (c_ >= 1 && c_ <= 5 && s_ == 0) M01(c_ + 2, 4); if (c_ == 8) M02(10, 1); SBAR(); STATG(3 * ((ks) - 3)); SBAR(); \
    acc1 = MFMA(WC[1], xf[ks], acc1); SBAR(); \
    if (c_ >= 1 && c_ <= 5 && s_ == 0) GO1(c_ + 2, 4); if (c_ == 8) { GO2(10, 1); M02(10, 2); } \
    if ((ks) < 31) RD1C(WN, ((ks) + 1) & 31); SBAR(); STATG(3 * ((ks) - 3) + 1); SBAR(); \
    acc2 = MFMA(WC[2], xf[ks], acc2); SBAR(); if (c_ == 8) GO2(10, 2); SBAR(); } while (0)
#define STEP1x4(k) STEP1(k, wa, wb); STEP1(k + 1, wb, wa); STEP1(k + 2, wa, wb); STEP1(k + 3, wb, wa)
  STEP1x4(0); STEP1x4(4); STEP1x4(8); STEP1x4(12); STEP1x4(16); STEP1x4(20); STEP1x4(24); STEP1x4(28);
  DMA2(10, 3);
  asm volatile("s_waitcnt vmcnt(8)" : "+v"(stat), "+v"(cs0), "+v"(cs1), "+v"(cs2) :: "memory");
  unsigned nm2 = (stat & 0xffffu) * 0x10001u;
  bf16x8 nmf = __builtin_bit_cast(bf16x8, (u32x4){nm2, nm2, nm2, nm2});
  acc0 = MFMA(__builtin_bit_cast(bf16x8, cs0), nmf, acc0); acc1 = MFMA(__builtin_bit_cast(bf16x8, cs1), nmf, acc1); acc2 = MFMA(__builtin_bit_cast(bf16x8, cs2), nmf, acc2);
  const float sd = bfhi(stat), rstd = __builtin_amdgcn_rcpf(sd), gsc = -1.4426950408889634f * rstd, qsc = rstd * (0.25f * 1.4426950408889634f), sc0 = sj == 1 ? qsc : rstd;
  const int tile = rb * 4 + tt, hd = 8 * Q4 + (tb_ ? 0 : 4) + 2 * cg;
  const unsigned sok = WS_KV + hd * 131072 + tile * 1024, soq = WS_Q + (hd * 64 + tile) * 1024, isv = (sj + 1) & 2;
  unsigned l16k = (l16 & 0x330) | (l16 & 64) * 2 | (l16 & 128) / 2; if (sj == 1) l16k = l16; PIN(l16k);
  SBAR();
  u32x4 qf0, qf1, hp, hq; bf16x8 eye0, eye1;
  f32x16 acc3 = {}, acc4 = {};
  auto glu2 = [&](const f32x16& a, int i) {
    const float e0_ = __builtin_amdgcn_exp2f(a[i] * gsc), e1_ = __builtin_amdgcn_exp2f(a[i + 1] * gsc);
    return cvtpa(a[8 + i] * __builtin_amdgcn_rcpf(__builtin_fmaf(e0_, sd, sd)), a[9 + i] * __builtin_amdgcn_rcpf(__builtin_fmaf(e1_, sd, sd))); };
  const unsigned mk_ = lds0 + ((w + aq) & 7) * 1024, skv = WS_KV + ah * 131072 + ((w + aq) & 7) * 1024;
#define KVM(i) M0S(mk_, (i) * 8192 + ((i) >= 8 ? L_V - 65536 : 0), skv + (i) * 8192)
#define KVG() asm volatile("buffer_load_dwordx4 %0, %1, %2 offen sc1 lds" :: "v"(l16), "s"(ws), "s"(sd_) : "memory")
#define PRE2(ks) do { constexpr int c_ = 8 + (((ks) + 1) >> 3), s_ = ((ks) + 1) & 7; \
    if (s_ == 0 && c_ < 12) { SBAR(); \
      if (c_ == 9 && (w == 1 || w == 2)) colsum(lxor(w == 1 ? wp1 : wp2, half * 64) + SL(8) + (pos ? U_XA : U_G1), (half * 5 + 2 + w) * 1024); \
      if (c_ == 9) WAITV(6); else if (c_ == 10) WAITV2(1, 0); \
      LBAR(); } \
    if ((ks) == K_F) { asm volatile("s_waitcnt vmcnt(4)\n\ts_barrier" ::: "memory"); FLAG(WS_F1); } \
    if ((ks) == K_P) { vf0 = WS_F1 + Q4 * 64 * FS + l16 * (FS / 16); POLL(vf0); }     \
    if ((ks) == K_C) { WAITF(0); asm volatile("s_barrier" ::: "memory"); } \
    if ((ks) == 31) { WAITV(6); PIN(cs0); PIN(cs1); } \
    SBAR(); } while (0)
#define FILL2(ks) do { \
    if ((ks) == 0 || (ks) == 1) { const u32x4 p_ = pack8(acc0, ((ks) & 1) * 8, sc0); if ((ks) == 0) hp = p_; else hq = p_; \
      if (isv) st16(p_, ws, l16k, sj == 1 ? soq + ((ks) & 1) * 65536 : sok + ((ks) & 1) * 131072); } \
    if ((ks) == 1) { eye0 = LDS8(lds + L_EYE + l16); eye1 = LDS8(lds + L_EYE + 1024 + l16); } \
    if ((ks) == 2 && !isv) acc0 = MFMA(__builtin_bit_cast(bf16x8, hq), eye1, MFMA(__builtin_bit_cast(bf16x8, hp), eye0, (f32x16){})); \
    if ((ks) == 3 && !isv) { LANE(n_); const unsigned vvo = (n_ & 16) * 8192 + ((n_ >> 5) * 16 + (n_ & 15)) * 16; \
      st16((u32x4){cvtpk(acc0[0], acc0[1]), cvtpk(acc0[2], acc0[3]), cvtpk(acc0[4], acc0[5]), cvtpk(acc0[6], acc0[7])}, ws, vvo, sok + 65536); \
      st16((u32x4){cvtpk(acc0[8], acc0[9]), cvtpk(acc0[10], acc0[11]), cvtpk(acc0[12], acc0[13]), cvtpk(acc0[14], acc0[15])}, ws, vvo, sok + 65536 + 512); } \
    if ((ks) >= 4 && (ks) <= 6 && tb_) st16(pack8((ks) < 6 ? acc1 : acc2, ((ks) & 1) * 8, (ks) < 6 ? rstd : qsc), ws, (ks) < 6 ? l16k : l16, (ks) < 6 ? sok + ((ks) & 1) * 131072 : soq); \
    if ((ks) == 6 && tb_) st16(pack8(acc2, 8, qsc), ws, l16, soq + 65536); \
    if ((ks) >= 4 && (ks) <= 18 && !((ks) & 1) && !tb_) { \
      if ((ks) == 16) st16(hp, ws, l16, WS_H + (tile * 112 + 4 * p1 + 2 * cg) * 1024); \
      if ((ks) < 12) { hp[(((ks) >> 1) + 2) & 3] = glu2(acc1, ((ks) + 4) & 6); PIN(hp); } \
      else { hq[(((ks) >> 1) + 2) & 3] = glu2(acc2, ((ks) + 4) & 6); PIN(hq); } \
      if ((ks) == 18) st16(hq, ws, l16, WS_H + (tile * 112 + 4 * p1 + 2 * cg + 1) * 1024); } \
    if ((ks) == 26) { PIN(nm2); nmf = __builtin_bit_cast(bf16x8, (u32x4){nm2, nm2, nm2, nm2}); PIN(nmf); } \
    if ((ks) == 31) { acc3 = MFMA(__builtin_bit_cast(bf16x8, cs0), nmf, acc3); acc4 = MFMA(__builtin_bit_cast(bf16x8, cs1), nmf, acc4); } \
    } while (0)
#define STEP2(ks, WC, WN) do { constexpr int c_ = 8 + (((ks) + 1) >> 3), s_ = ((ks) + 1) & 7; \
    if ((ks) < 31) RD2A(WN, ((ks) + 1) & 31); \
    if (s_ == 7) RD2B(WN, ((ks) + 1) & 31); \
    if (c_ == 9 && s_ < 4) M02(11, s_); \
    if ((ks) == K_C + 1) KVM(0); if ((ks) == K_C + 2) KVM(8); SBAR(); \
    acc3 = MFMA(WC[0], xf[ks], acc3); SBAR(); \
    if (c_ == 9 && s_ < 4) GO2(11, s_); \
    if ((ks) == K_C) ALD4C(cs0, l16, ws, csb + cg * 5120 + 3072); \
    if ((ks) == K_C + 1 || (ks) == K_C + 2) KVG(); if ((ks) == K_C + 3) ALD4C(qf1, l16, ws, WS_Q + (ah * 64 + qown) * 1024); \
    if ((ks) < 31 && s_ != 7) RD2B(WN, ((ks) + 1) & 31); \
    SBAR(); FILL2(ks); \
    if ((ks) == K_C + 1) KVM(4); if ((ks) == K_C + 2) KVM(12); SBAR(); \
    acc4 = MFMA(WC[1], xf[ks], acc4); SBAR(); \
    if ((ks) == K_C) ALD4C(cs1, l16, ws, csb + cg * 5120 + 4096); \
    if ((ks) == K_C + 1 || (ks) == K_C + 2) KVG(); if ((ks) == K_C + 3) ALD4C(qf0, l16, ws, WS_Q + (ah * 64 + (qown ^ 1)) * 1024); \
    SBAR(); PRE2((ks) + 1); } while (0)
#define STEP2x4(k) STEP2(k, wa, wb); STEP2(k + 1, wb, wa); STEP2(k + 2, wa, wb); STEP2(k + 3, wb, wa)
  STEP2x4(0); STEP2x4(4); STEP2x4(8); STEP2x4(12); STEP2x4(16); STEP2x4(20); STEP2x4(24); STEP2x4(28);
  {
    LANE(n_); const lptr s2_ = lds + L_PRIV + w * 2048 + (4 * (n_ >> 5) + ((n_ >> 2) & 3)) * 128 + (n_ & 1) * 8; const int pc_ = (((n_ >> 3) & 2) + ((n_ >> 1) & 1)) ^ ((n_ >> 1) & 4);
    hp = __builtin_bit_cast(u32x4, trfrag(s2_ + pc_ * 16, 1024)); hq = __builtin_bit_cast(u32x4, trfrag(s2_ + (pc_ ^ 4) * 16, 1024)); }
  st16(glu8(acc3, gsc, sd), ws, l16, WS_H + (tile * 112 + 4 * p2 + 2 * cg) * 1024);
  LANE(ln); const int r3 = ln & 31, hf = ln >> 5;
  const int tb = xcd * 4 + (bi & 3), n64 = bi >> 2;
  lptr kp = lds + kh * 32768 + l16, kq = lds + (kh ^ 1) * 32768 + l16;
  const bool vrow = r3 < 16;
  int vstep = vrow ? 4096 : 0;
  lptr vp = lds + (vrow ? L_V + kh * 32768 + hf * 256 : L_ONES - 256) + r3 * 16;
  int idx = 0xDDDDDDDD;
  float aone = hf ? 0.f : 1.f;
  PIN(kp); PIN(kq); PIN(vstep); PIN(vp); PIN(idx); PIN(aone);
  f32x16 nm0, nm1, o0, o1, sA, sB;
  const f32x16 z = {};
  { u32x4 zz = {}; PIN(zz); o0 = MFMA(__builtin_bit_cast(bf16x8, zz), __builtin_bit_cast(bf16x8, zz), z); PIN(o0); o1 = MFMA(__builtin_bit_cast(bf16x8, zz), __builtin_bit_cast(bf16x8, zz), o0); PIN(o1); }
  WAITBAR(1);
  PIN(qf0); PIN(qf1);
  const bf16x8 q0 = __builtin_bit_cast(bf16x8, qf0), q1 = __builtin_bit_cast(bf16x8, qf1);
  auto hmax = [&](float m) { auto rr = __builtin_amdgcn_permlane32_swap(__float_as_uint(m), __float_as_uint(m), false, false);
    return __builtin_fmaxf(__uint_as_float(rr[0]), __uint_as_float(rr[1])); };
  auto max16 = [&](float m, const f32x16& s) {
#pragma unroll
    for (int i = 0; i < 16; i += 2) m = __builtin_fmaxf(__builtin_fmaxf(m, s[i]), s[i + 1]);
    return m; };
  __builtin_amdgcn_s_setprio(1);
  bf16x8 k1 = LDS8(kp), va, vb;
  {
    const bf16x8 k0 = LDS8(kq);
    va = LDS8(vp); SBAR();
    if (ks2 < 144) { st16(hp, ws, l16, WS_W2 + (2 * xcd * 144 + ks2) * 1024); st16(hq, ws, l16, WS_W2 + ((2 * xcd + 1) * 144 + ks2) * 1024); }
    hp[0] = glu2(acc4, 0); PIN(hp); SBAR();
    const f32x16 a2 = MFMA(k1, q0, z), a0 = MFMA(k0, q0, z); SBAR();
    hp[1] = glu2(acc4, 2); PIN(hp); SBAR();
    const f32x16 a3 = MFMA(k1, q1, z), a1 = MFMA(k0, q1, z); SBAR();
    float x0 = a0[0], x2 = a2[0];
#pragma unroll
    for (int i = 1; i < 15; i += 2) { x2 = max3(x2, a2[i], a2[i + 1]); x0 = max3(x0, a0[i], a0[i + 1]); }
    nm0 = __builtin_amdgcn_mfma_f32_32x32x2f32(aone, -REF_OFF - hmax(max3(max3(x0, x2, a0[15]), a2[15], a2[15])), z, 0, 0, 0); SBAR();
    float x1 = a1[0], x3 = a3[0];
#pragma unroll
    for (int i = 1; i < 15; i += 2) { x3 = max3(x3, a3[i], a3[i + 1]); x1 = max3(x1, a1[i], a1[i + 1]); }
    float m1 = -REF_OFF - hmax(max3(max3(x1, x3, a1[15]), a3[15], a3[15])); PIN(m1); SBAR();
    sA = MFMA(k1, q0, nm0); SBAR();
    hp[2] = glu2(acc4, 4); PIN(hp); SBAR();
    nm1 = __builtin_amdgcn_mfma_f32_32x32x2f32(aone, m1, z, 0, 0, 0); SBAR();
    hp[3] = glu2(acc4, 6); PIN(hp);
    st16(hp, ws, l16, WS_H + (tile * 112 + 4 * p2 + 2 * cg + 1) * 1024); SBAR();
  }
  f32x8 pa, pb;
  f32x16 g00, g01, g10, g11;
  bf16x8 fa[4], fb[4];
  unsigned sow = WS_W2 + (2 * n64 * 144 + 14 * w) * 1024, soh = WS_H + ((2 * tb + kh) * 112 + 14 * w) * 1024;
  const unsigned dh = kh ? -112 * 1024 : 112 * 1024;
#define EX(P, S, i) P[(i) & 7] = __builtin_amdgcn_exp2f(S[i])
  unsigned so_;
#define UN_(...) __VA_ARGS__
#define AP_(ph, f, ...) f(ph, __VA_ARGS__)
#define AP2_(...) AP_(__VA_ARGS__)
#define MS(slot, M) do { AP2_(0, UN_ slot); SBAR(); M; SBAR(); AP2_(1, UN_ slot); SBAR(); } while (0)
#define NO(ph, x) do {} while (0)
#define DO(ph, x) do { if (ph) { x; } } while (0)
#define LD(ph, dst, off) do { if (ph) dst = ld16c(ws, l16, so_); else { so_ = (off); asm volatile("" :: "s"(so_)); } } while (0)
#define DM(ph, base, imm, off) do { if (ph) asm volatile("buffer_load_dwordx4 %0, %1, %2 offen sc1 lds" :: "v"(l16), "s"(ws), "s"(so_) : "memory"); \
    else { so_ = (off); asm volatile("s_add_i32 m0, %1, %2" :: "s"(so_), "s"(base), "i"(imm) : "memory", "scc"); } } while (0)
#define KV(ph, i) DM(ph, mk_, (i) * 8192 + ((i) >= 8 ? L_V - 65536 : 0), skv + (i) * 8192)
#define N_ (NO, 0)
#define T2(u, ...) T2X(o1 = SMF(vb, pb, o1, idx), u, __VA_ARGS__)
#define T2X(FIN, u, A3, MA, A4, B1, B2, MB, B4) do { \
    EX(pa, sA, 0); EX(pa, sA, 1); SBAR(); FIN; SBAR(); vb = LDS8(vp + (u) * 1024 + 512); SBAR(); \
    EX(pa, sA, 2); EX(pa, sA, 3); EX(pa, sA, 4); EX(pa, sA, 5); SBAR(); sB = MFMA(k1, q1, nm1); SBAR(); k1 = LDS8(kp + ((u) + 1) * 1024); SBAR(); \
    EX(pa, sA, 6); EX(pa, sA, 7); SBAR(); EX(pb, sA, 8); EX(pb, sA, 9); MS(A3, o0 = SMF(va, pa, o0, idx)); \
    EX(pb, sA, 10); EX(pb, sA, 11); EX(pb, sA, 12); EX(pb, sA, 13); MS(A4, MA); EX(pb, sA, 14); EX(pb, sA, 15); SBAR(); \
    EX(pa, sB, 0); EX(pa, sB, 1); MS(B1, o0 = SMF(vb, pb, o0, idx)); \
    EX(pa, sB, 2); EX(pa, sB, 3); EX(pa, sB, 4); EX(pa, sB, 5); MS(B2, sA = MFMA(k1, q0, nm0)); \
    EX(pa, sB, 6); EX(pa, sB, 7); SBAR(); EX(pb, sB, 8); EX(pb, sB, 9); SBAR(); o1 = SMF(va, pa, o1, idx); SBAR(); va = LDS8(vp + ((u) + 1) * 1024); SBAR(); \
    EX(pb, sB, 10); EX(pb, sB, 11); EX(pb, sB, 12); EX(pb, sB, 13); MS(B4, MB); EX(pb, sB, 14); EX(pb, sB, 15); SBAR(); } while (0)
#define NOP_ do {} while (0)
#define ADV() do { kp += 4096; vp += vstep; } while (0)
#define GM(g, wf, hf_) do { g = MFMA(hf_, wf, g); PIN(g); } while (0)
#define G2P(u, C, D, X0, X1, X2, X3, X4, X5) do { \
    T2(u, X0, GM(g00, C[0], C[2]), (LD, D[3], soh + dh - 1024), X1, X2, GM(g10, C[1], C[2]), (LD, C[2], soh)); \
    T2(u + 1, X3, GM(g11, C[1], C[3]), (LD, C[1], sow + 144 * 1024), X4, X5, GM(g01, C[0], C[3]), (LD, C[0], sow)); \
    sow += 1024; soh += 1024; } while (0)
#define PRIO_AT(k, n) asm volatile("s_cmp_lg_u32 %0, " #k "\n\ts_cbranch_scc1 1f\n\ts_setprio " #n "\n1:" :: "s"(it) : "scc")
#define PRIO(n) __builtin_amdgcn_s_setprio(n)
  T2X(NOP_, 0, (KV, 1), NOP_, N_, (KV, 5), (KV, 9), NOP_, N_); PRIO(0); T2(1, (KV, 13), NOP_, N_, (KV, 2), (KV, 6), NOP_, N_);
  asm volatile("s_waitcnt vmcnt(6)\n\ts_barrier" ::: "memory");
  FLAG(WS_F2);
  PUT((unsigned long long)e1 << 32 | (e0 + 1), WS_EPOCH, w | b);
  if (w == 0) vf0 = WS_F2 + (ln < 16 ? ln * 16 + (tb >> 1) : ((n64 >> 1) * 4 + ((ln >> 3) & 3)) * 16 + (n64 & 1) * 8 + (ln & 7)) * FS;
  PRIO(2); T2(2, (KV, 10), NOP_, N_, (KV, 14), (KV, 3), NOP_, N_); T2(3, (KV, 7), NOP_, N_, (KV, 11), (KV, 15), NOP_, N_); ADV();
  hp = (u32x4){~0u, ~0u, ~0u, ~0u}; PIN(hp);
  PRIO(1); T2(0, (DO, st16(hp, ws, l16, WS_O + (qown * 32 + ah) * 1024)), NOP_, N_, N_, (DO, POLL(vf0)), NOP_, N_); PRIO(0); T2(1, N_, NOP_, N_, N_, N_, NOP_, N_);
  WAITF(0);
  asm volatile("s_waitcnt vmcnt(0)\n\ts_barrier" ::: "memory");
  PUT(tag, WS_F3 + b * FS, w);
  PRIO(3);
  { bf16x8 zz = {};
#define GZ(g) do { PIN(zz); g = MFMA(zz, zz, z); PIN(g); } while (0)
    T2(2, (LD, fa[2], soh), GZ(g00), (LD, fa[0], sow), (LD, fa[1], sow + 144 * 1024), (LD, fa[3], soh + dh), GZ(g10), N_);
    T2(3, (LD, fb[2], soh + 1024), GZ(g11), N_, (LD, fb[1], sow + 145 * 1024), N_, GZ(g01), (LD, fb[0], sow + 1024)); ADV(); }
  sow += 2048; soh += 2048;
#pragma unroll 1
  for (int it = 0; it < 4; ++it) { PRIO_AT(2, 2); PRIO_AT(3, 1); G2P(0, fa, fb, N_, N_, N_, N_, N_, N_); PRIO_AT(3, 0); G2P(2, fb, fa, N_, N_, N_, N_, N_, N_); ADV(); }
  auto lwo_of = [](int g) { return (unsigned)(g < 3 ? g : g < 6 ? g + 1 : g + 3) * 8192; };
  const unsigned swo = WS_W2 + (2 * n64 * 144 + 112 + 4 * w) * 1024, lwo = lds0 + lwo_of(w);
#define WO(ph, i) DM(ph, lwo, (i) * 1024, swo + ((i) & 3) * 1024 + ((i) >> 2) * (144 * 1024))
  vf0 = WS_F3 + xcd * 32 * FS + (l16 & 496) * (FS / 16); PIN(vf0);
  asm volatile("s_barrier" ::: "memory");
  PRIO(3);
  G2P(0, fa, fb, (WO, 0), (DO, POLL(vf0)), (WO, 1), (WO, 2), N_, (WO, 3)); G2P(2, fb, fa, (WO, 4), N_, (WO, 5), (WO, 6), N_, (WO, 7)); ADV();
  PRIO(2);
  bf16x8 fc[4];
  typedef __attribute__((address_space(3))) f32x4* l4p;
  typedef __attribute__((address_space(3))) float* l1p;
  lptr xw = lds + L_PRIV + w * 2048 + l16, xl = lds + 65536 + w * 256 + l16 / 4; PIN(xw); PIN(xl);
  G2P(0, fa, fb, (LD, fc[0], sow + 1024), N_, (LD, fc[2], soh + 1024), (LD, fc[1], sow + 145 * 1024), N_, (LD, fc[3], soh + dh + 1024));
  PRIO(1);
  T2(2, (LD, fa[3], soh + dh - 1024), NOP_, N_, N_, N_, NOP_, N_);
  PRIO(0);
  auto xchg = [&]() { PIN(o0); *(l4p)(xw) = (f32x4){o0[0], o0[1], o0[2], o0[3]}; *(l4p)(xw + 1024) = (f32x4){o0[4], o0[5], o0[6], o0[7]}; *(l1p)(xl) = o0[8]; };
  T2(3, N_, NOP_, N_, N_, N_, xchg(), N_);
#undef GM
#define GM(g, wf, hf_) do { g = MFMA(hf_, wf, g); PIN(g); SBAR(); } while (0)
  o1 = SMF(vb, pb, o1, idx);
  xw = lxor(xw - L_PRIV, 8192) + L_PRIV; xl = lxor(xl, 1024); PIN(xw); PIN(xl);
  const int hs = (w + n64) & 3;
  lptr wl0 = lds + lwo_of(2 * hs) + l16, wl1 = lds + lwo_of(2 * hs + 1) + l16; PIN(wl0); PIN(wl1); SBAR();
  LBAR();
  float oo[9];
  {
    const f32x4 a = *(l4p)(xw), c = *(l4p)(xw + 1024); const float lp = *(l1p)(xl);
    SBAR(); asm volatile("" :: "v"(o1));
    oo[8] = o1[8] + lp;
    float sm = 0.f;
#pragma unroll
    for (int i = 0; i < 4; ++i) { oo[i] = o1[i] + a[i]; oo[4 + i] = o1[4 + i] + c[i]; }
#pragma unroll
    for (int i = 0; i < 8; ++i) sm += __builtin_fabsf(oo[i]);
    const bool ok = __builtin_amdgcn_fmed3f(oo[8], 0x1p-104f, 0x1p120f) == oo[8] && __builtin_amdgcn_fmed3f(sm, 0x1p-106f, 0x1p127f) == sm;
    if (__builtin_expect(__builtin_amdgcn_ballot_w64(!ok) != 0, 0)) {
      const unsigned sk = WS_KV + ah * 131072, vv = l16 & 256 ? 0x7fffff00u : (l16 & 512) / 2 + (l16 & 240), one = (l16 & 432) == 256 ? 0x3f803f80u : 0u;
      float m = -INFINITY;
#pragma unroll 1
      for (int t = 0; t < 64; ++t) m = max16(m, MFMA(ld16c(ws, l16, sk + t * 1024), q1, z));
      m = -hmax(m);
#pragma unroll
      for (int i = 0; i < 16; ++i) nm0[i] = m;
      o0 = z;
#pragma unroll 1
      for (int t = 0; t < 64; ++t) {
        const f32x16 s_ = MFMA(ld16c(ws, l16, sk + t * 1024), q1, nm0);
        const u32x4 v0 = __builtin_bit_cast(u32x4, ld16c(ws, vv, sk + 65536 + t * 1024)) | one, v1 = __builtin_bit_cast(u32x4, ld16c(ws, vv, sk + 65536 + t * 1024 + 512)) | one;
#pragma unroll
        for (int i = 0; i < 8; ++i) { pa[i] = __builtin_amdgcn_exp2f(s_[i]); pb[i] = __builtin_amdgcn_exp2f(s_[8 + i]); }
        o0 = SMF(__builtin_bit_cast(bf16x8, v0), pa, o0, idx); o0 = SMF(__builtin_bit_cast(bf16x8, v1), pb, o0, idx);
      }
      float unit = 1.f; PIN(unit);
#pragma unroll
      for (int i = 0; i < 9; ++i) oo[i] = o0[i] * unit;
    }
  }
  { const float rl = __builtin_amdgcn_rcpf(oo[8]);
    u32x4 ov = {cvtpa(oo[0] * rl, oo[1] * rl), cvtpa(oo[2] * rl, oo[3] * rl), cvtpa(oo[4] * rl, oo[5] * rl), cvtpa(oo[6] * rl, oo[7] * rl)};
#pragma unroll
    for (int i = 0; i < 4; ++i) ov[i] = ov[i] < 0xfffffffeu ? ov[i] : 0xfffffffeu;
#pragma unroll
    for (int i = 0; i < 4; ++i) { PIN(fa[i]); PIN(fb[i]); PIN(fc[i]); }
    WAITV(0);
    st16(ov, ws, l16, WS_O + (qown * 32 + ah) * 1024); }
  WAITF(1);
  asm volatile("s_barrier" ::: "memory");
  unsigned vout; u32x2 xr0, xr1; lptr rs;
  f32x4 ra, rc;
#define PUTG(g, o) asm volatile("ds_write_addtid_b32 %0 offset:%c16\n\tds_write_addtid_b32 %1 offset:%c16+256\n\tds_write_addtid_b32 %2 offset:%c16+512\n\tds_write_addtid_b32 %3 offset:%c16+768\n\t" \
      "ds_write_addtid_b32 %4 offset:%c16+1024\n\tds_write_addtid_b32 %5 offset:%c16+1280\n\tds_write_addtid_b32 %6 offset:%c16+1536\n\tds_write_addtid_b32 %7 offset:%c16+1792\n\t" \
      "ds_write_addtid_b32 %8 offset:%c16+2048\n\tds_write_addtid_b32 %9 offset:%c16+2304\n\tds_write_addtid_b32 %10 offset:%c16+2560\n\tds_write_addtid_b32 %11 offset:%c16+2816\n\t" \
      "ds_write_addtid_b32 %12 offset:%c16+3072\n\tds_write_addtid_b32 %13 offset:%c16+3328\n\tds_write_addtid_b32 %14 offset:%c16+3584\n\tds_write_addtid_b32 %15 offset:%c16+3840" \
      :: "v"(g[0]), "v"(g[1]), "v"(g[2]), "v"(g[3]), "v"(g[4]), "v"(g[5]), "v"(g[6]), "v"(g[7]), "v"(g[8]), "v"(g[9]), "v"(g[10]), "v"(g[11]), "v"(g[12]), "v"(g[13]), "v"(g[14]), "v"(g[15]), "i"(o) : "memory")
  f32x4 pa_[4], pc_[4];
#define RD4(p) do { _Pragma("unroll") for (int i = 0; i < 4; ++i) { pa_[i] = *(l4p)((p) + i * 8192); pc_[i] = *(l4p)((p) + i * 8192 + 512); } SBAR(); } while (0)
#define SUM4() do { _Pragma("unroll") for (int i = 0; i < 4; ++i) { ra += pa_[i]; rc += pc_[i]; PIN(ra); PIN(rc); } } while (0)
  {
    const unsigned soo = WS_O + ((2 * tb + kh) * 32 + 8 * hs) * 1024; unsigned spins; unsigned long long ret; u32x16 ofa, ofb;
#define OL0 "buffer_load_dwordx4 v[0:3], %[l], %[r], %[o0] offen sc1\n\t"
#define OL1 "buffer_load_dwordx4 v[4:7], %[l], %[r], %[o0] offen offset:1024 sc1\n\t"
#define OL2 "buffer_load_dwordx4 v[8:11], %[l], %[r], %[o0] offen offset:2048 sc1\n\t"
#define OL3 "buffer_load_dwordx4 v[12:15], %[l], %[r], %[o0] offen offset:3072 sc1\n\t"
#define OL4 "buffer_load_dwordx4 v[16:19], %[l], %[r], %[o1] offen sc1\n\t"
#define OL5 "buffer_load_dwordx4 v[20:23], %[l], %[r], %[o1] offen offset:1024 sc1\n\t"
#define OL6 "buffer_load_dwordx4 v[24:27], %[l], %[r], %[o1] offen offset:2048 sc1\n\t"
#define OL7 "buffer_load_dwordx4 v[28:31], %[l], %[r], %[o1] offen offset:3072 sc1\n\t"
#define OLOADS(br, c) asm volatile("s_cmp_lt_u32 %[w], 4\n\t" br " 1f\n\t" OL0 OL1 OL2 OL3 OL4 OL5 OL6 OL7 "1:" : c "{v[0:15]}"(ofa), c "{v[16:31]}"(ofb) \
      : [l] "v"(l16), [r] "s"(ws), [o0] "s"(soo), [o1] "s"(soo + 4096), [w] "s"(w) : "memory", "scc")
    bf16x8 wa_[8], wb_[8];
#define WRD(i) do { wa_[i] = LDS8(((i) < 4 ? wl0 : wl1) + ((i) & 3) * 1024); wb_[i] = LDS8(((i) < 4 ? wl0 : wl1) + ((i) & 3) * 1024 + 4096); SBAR(); } while (0)
    SBAR(); GM(g01, fb[0], fb[3]); WRD(0); WRD(1); GM(g11, fb[1], fb[3]); WRD(2); WRD(3); GM(g01, fa[0], fa[3]); WRD(4); GM(g11, fa[1], fa[3]); WRD(5); GM(g01, fc[0], fc[3]); WRD(6); GM(g11, fc[1], fc[3]); WRD(7);
    vout = (tb * 64 + w * 8) * 1024 + n64 * 128 + (l16 & 256) * 16 + (l16 & 512) * 2 + (l16 & 240) / 2;
    xr0 = __builtin_amdgcn_raw_buffer_load_b64(rx, vout, 0, 0); xr1 = __builtin_amdgcn_raw_buffer_load_b64(rx, vout + 2048, 0, 0);
    rs = lds + (kh ^ 1) * 32768 + (l16 & 128) * 32 + (w & 3) * 1024 + (l16 & 512) / 2 + (l16 & 256) / 2 + (l16 & 112); PIN(rs); SBAR();
    asm volatile("s_mov_b32 m0, %0\n\ts_nop 7\n\ts_waitcnt lgkmcnt(0)\n\ts_barrier" :: "s"(lds0 + w * 8192) : "memory");
    GM(g00, fb[0], fb[2]); PUTG(g01, 0); GM(g10, fb[1], fb[2]); GM(g00, fa[0], fa[2]); PUTG(g11, 4096); GM(g10, fa[1], fa[2]);
    OLOADS("s_cbranch_scc0", "=&");
    GM(g00, fc[0], fc[2]); GM(g10, fc[1], fc[2]);
    LBAR();
    OLOADS("s_cbranch_scc1", "+");
    RD4(rs);
#define OCHK(r0, r1, r2, r3) "v_max3_u32 v32, v" #r0 ", v" #r1 ", v" #r2 "\n\tv_max_u32 v32, v32, v" #r3 "\n\tv_cmp_eq_u32 vcc, -1, v32\n\t"
#define OT1(n, r0, r1, r2, r3) "s_waitcnt vmcnt(" #n ")\n\tv_max3_u32 v32, v" #r0 ", v" #r1 ", v" #r2 "\n\tv_max_u32 v32, v32, v" #r3 "\n\t"
#define OT2 "v_cmp_eq_u32 vcc, -1, v32\n\ts_getpc_b64 %[ret]\n\ts_cbranch_vccnz 3f\n\t"
#define OM2(a, b, c, d) "v_max3_u32 v32, v32, v" #a ", v" #b "\n\tv_max3_u32 v33, v33, v" #c ", v" #d "\n\t"
#define OMA(i, r0, r3) "v_mfma_f32_32x32x16_bf16 %[g0], v[" #r0 ":" #r3 "], %[a" #i "], %[g0]\n\t"
#define OMB(i, r0, r3) "v_mfma_f32_32x32x16_bf16 %[g1], v[" #r0 ":" #r3 "], %[b" #i "], %[g1]\n\t"
#define ORE(r0, r1, r2, r3, load) OCHK(r0, r1, r2, r3) "s_cbranch_vccz 4f\n\t" load "4:\n\t"
    static_assert(SPIN_CAP == 0x8000, "literal below");
    asm volatile("s_mov_b32 %[sp], 0\n\t"
                 OT1(7, 0, 1, 2, 3) OT2 OMA(0, 0, 3) OT1(6, 4, 5, 6, 7) OMB(0, 0, 3) OT2 OMA(1, 4, 7) OT1(5, 8, 9, 10, 11) OMB(1, 4, 7) OT2 OMA(2, 8, 11) OT1(4, 12, 13, 14, 15) OMB(2, 8, 11) OT2
                 OMA(3, 12, 15) OT1(3, 16, 17, 18, 19) OMB(3, 12, 15) OT2 OMA(4, 16, 19) OT1(2, 20, 21, 22, 23) OMB(4, 16, 19) OT2 OMA(5, 20, 23) OT1(1, 24, 25, 26, 27) OMB(5, 20, 23) OT2
                 OMA(6, 24, 27) OT1(0, 28, 29, 30, 31) OMB(6, 24, 27) OT2 OMA(7, 28, 31) OMB(7, 28, 31)
                 "s_branch 2f\n"
                 "3:\n\ts_add_u32 %[sp], %[sp], 1\n\ts_cmp_gt_u32 %[sp], 0x8000\n\ts_cbranch_scc1 2f\n\ts_waitcnt vmcnt(0)\n\t"
                 ORE(0, 1, 2, 3, OL0) ORE(4, 5, 6, 7, OL1) ORE(8, 9, 10, 11, OL2) ORE(12, 13, 14, 15, OL3)
                 ORE(16, 17, 18, 19, OL4) ORE(20, 21, 22, 23, OL5) ORE(24, 25, 26, 27, OL6) ORE(28, 29, 30, 31, OL7)
                 "s_waitcnt vmcnt(0)\n\tv_max3_u32 v32, v0, v1, v2\n\tv_max3_u32 v33, v3, v4, v5\n\t" OM2(6, 7, 8, 9) OM2(10, 11, 12, 13) OM2(14, 15, 16, 17) OM2(18, 19, 20, 21)
                 OM2(22, 23, 24, 25) OM2(26, 27, 28, 29) "v_max3_u32 v32, v32, v30, v31\n\tv_max_u32 v32, v32, v33\n\tv_cmp_eq_u32 vcc, -1, v32\n\ts_setpc_b64 %[ret]\n"
                 "2:\n\ts_waitcnt vmcnt(0)\n\ts_nop 11"
                 : [g0] "+v"(g00), [g1] "+v"(g10), "+{v[0:15]}"(ofa), "+{v[16:31]}"(ofb), [sp] "=&s"(spins), [ret] "=&s"(ret)
                 : [l] "v"(l16), [r] "s"(ws), [o0] "s"(soo), [o1] "s"(soo + 4096),
                   [a0] "v"(wa_[0]), [a1] "v"(wa_[1]), [a2] "v"(wa_[2]), [a3] "v"(wa_[3]), [a4] "v"(wa_[4]), [a5] "v"(wa_[5]), [a6] "v"(wa_[6]), [a7] "v"(wa_[7]),
                   [b0] "v"(wb_[0]), [b1] "v"(wb_[1]), [b2] "v"(wb_[2]), [b3] "v"(wb_[3]), [b4] "v"(wb_[4]), [b5] "v"(wb_[5]), [b6] "v"(wb_[6]), [b7] "v"(wb_[7])
                 : "memory", "scc", "vcc", "v32", "v33");
  }
#pragma unroll
  for (int i = 0; i < 4; ++i) { PIN(pa_[i]); PIN(pc_[i]); }
  asm volatile("s_mov_b32 m0, %0\n\ts_nop 0" :: "s"(lds0 + w * 8192 + 8188) : "memory");
  PUTG(g00, 65536 - 8188); PUTG(g10, 65536 - 8188 + 4096);
  ra = (f32x4){bflo(xr0[0]), bfhi(xr0[0]), bflo(xr0[1]), bfhi(xr0[1])}; rc = (f32x4){bflo(xr1[0]), bfhi(xr1[0]), bflo(xr1[1]), bfhi(xr1[1])}; PIN(ra); PIN(rc);
  SUM4();
  rs = lxor(rs, 98304); PIN(rs);
  LBAR();
  RD4(rs); SUM4();
  { const rsrc_t ro = mk(pout, 2048 * 1024);
    __builtin_amdgcn_raw_buffer_store_b64((u32x2){cvtpk(ra[0], ra[1]), cvtpk(ra[2], ra[3])}, ro, vout, 0, 0);
    __builtin_amdgcn_raw_buffer_store_b64((u32x2){cvtpk(rc[0], rc[1]), cvtpk(rc[2], rc[3])}, ro, vout + 2048, 0, 0); }
  asm volatile(".Lfb_end:");
}

extern "C" void kernel_launch(void* const* d_in, const int* in_sizes, int n_in, void* d_out, int out_size, void* d_ws, size_t ws_size, hipStream_t stream) {
  (void)hipFuncSetAttribute((const void*)fused_block, hipFuncAttributeMaxDynamicSharedMemorySize, LDS_BYTES);
  hipLaunchKernelGGL(fused_block, dim3(256), dim3(512), LDS_BYTES, stream, d_in[0], d_in[1], d_in[2], d_in[4], d_in[5], d_in[6], d_out, d_ws);
}
```

```cpp
#include <hip/hip_runtime.h>
#include <hip/hip_bf16.h>

typedef __bf16 bf16x8 __attribute__((ext_vector_type(8)));
typedef __bf16 bf16x16 __attribute__((ext_vector_type(16)));
typedef __bf16 bf16x2 __attribute__((ext_vector_type(2)));
typedef short s16x4 __attribute__((ext_vector_type(4)));
typedef float f32x16 __attribute__((ext_vector_type(16)));
typedef float f32x8 __attribute__((ext_vector_type(8)));
typedef float f32x4 __attribute__((ext_vector_type(4)));
typedef float f32x2 __attribute__((ext_vector_type(2)));
typedef unsigned u32x4 __attribute__((ext_vector_type(4)));
typedef unsigned u32x2 __attribute__((ext_vector_type(2)));
typedef unsigned u32x16 __attribute__((ext_vector_type(16)));
typedef __amdgpu_buffer_rsrc_t rsrc_t;
typedef __attribute__((address_space(3))) char* lptr;
#define DBG_STAMPS 0
#define DI __device__ __forceinline__
#define SBAR() __builtin_amdgcn_sched_barrier(0)
#define MFMA(a, b, c) __builtin_amdgcn_mfma_f32_32x32x16_bf16(a, b, c, 0, 0, 0)
#define SMF(a, b, c, idx) __builtin_amdgcn_smfmac_f32_32x32x32_bf16(a, __builtin_bit_cast(bf16x16, b), c, idx, 0, 0)
#define LDS8(p) (*(const __attribute__((address_space(3))) bf16x8*)(p))
#define PIN(x) asm volatile("" : "+v"(x))
#define LANE(n) unsigned n = l16; PIN(n); n >>= 4

#define X_PT 2
#define X_SC " sc1"
#define X_FL 0
#define FORCE_FB 0

constexpr unsigned WS_EPOCH = 0;
constexpr unsigned FS = 128;
constexpr unsigned WS_F0 = 229376, WS_F1 = WS_F0 + 256 * FS, WS_F2 = WS_F1 + 256 * FS;
constexpr unsigned WS_F3 = WS_F2 + 256 * FS;
constexpr unsigned WS_STAT = 57344;
constexpr unsigned WS_CS = 65536;
constexpr unsigned WS_Q = 1u << 20;
constexpr unsigned WS_KV = WS_Q + (2u << 20);
constexpr unsigned WS_O = WS_KV + (4u << 20);
constexpr unsigned WS_W2 = WS_O + (2u << 20);
constexpr unsigned WS_H = WS_W2 + (3u << 20);
constexpr unsigned WS_DBG = WS_H + (7u << 20);
constexpr unsigned WS_END = WS_DBG + (1u << 20);
constexpr int U_XA = 0, U_G1 = 24576, U_XB = 49152, U_G2 = 73728, U_G0 = 98304, UD = 49152;
constexpr int L_V = 73728, L_PRIV = 139264, L_ONES = 155648, L_EYE = L_ONES + 5120, LDS_BYTES = 163840;
constexpr unsigned SPIN_CAP = 1u << 15;
constexpr float REF_OFF = 100.f;
constexpr int K_F = 12, K_P = 19, K_C = 25;

DI rsrc_t mk(const void* p, unsigned bytes) { return __builtin_amdgcn_make_buffer_rsrc((void*)p, (short)0, (int)bytes, 0x00020000); }
DI unsigned cvtpk(float lo, float hi) { f32x2 v = {lo, hi}; return __builtin_bit_cast(unsigned, __builtin_convertvector(v, bf16x2)); }
DI unsigned cvtpa(float lo, float hi) { unsigned r; asm("v_cvt_pk_bf16_f32 %0, %1, %2" : "=v"(r) : "v"(lo), "v"(hi)); return r; }
DI float bflo(unsigned u) { return __uint_as_float(u << 16); }
DI float bfhi(unsigned u) { return __uint_as_float(u & 0xffff0000u); }
DI float rbf(float f) { return bflo(cvtpk(f, f)); }
DI float max3(float a, float b, float c) { return __builtin_fmaxf(__builtin_fmaxf(a, b), c); }
DI lptr lxor(lptr p, unsigned m) { return (lptr)((size_t)p ^ m); }
DI s16x4 trd(lptr p) { return __builtin_amdgcn_ds_read_tr16_b64_v4i16((__attribute__((address_space(3))) s16x4*)p); }
DI bf16x8 trfrag(lptr p, int step) { s16x4 a = trd(p), b = trd(p + step); return __builtin_bit_cast(bf16x8, __builtin_shufflevector(a, b, 0, 1, 2, 3, 4, 5, 6, 7)); }
#define dma(base, imm, voff, r, soff) asm volatile("s_add_i32 m0, %0, %4\n\ts_nop 0\n\tbuffer_load_dwordx4 %1, %2, %3 offen lds" :: "s"(base), "v"(voff), "s"(r), "s"(soff), "i"(imm) : "memory", "scc")
#define dma_go(voff, r, soff) asm volatile("buffer_load_dwordx4 %0, %1, %2 offen lds" :: "v"(voff), "s"(r), "s"(soff) : "memory")
#define dma_nt(base, imm, voff, r, soff) asm volatile("s_add_i32 m0, %0, %4\n\ts_nop 0\n\tbuffer_load_dwordx4 %1, %2, %3 offen nt lds" :: "s"(base), "v"(voff), "s"(r), "s"(soff), "i"(imm) : "memory", "scc")
#define ALD4(dst, voff, r, soff) asm volatile("buffer_load_dwordx4 %0, %1, %2, %3 offen" : "=v"(dst) : "v"(voff), "s"(r), "s"(soff) : "memory")
#define ALD4C(dst, voff, r, soff) asm volatile("buffer_load_dwordx4 %0, %1, %2, %3 offen sc1" : "=v"(dst) : "v"(voff), "s"(r), "s"(soff) : "memory")
#define ALD4O(dst, soff, imm) asm volatile("buffer_load_dwordx4 %0, %1, %2, %3 offen offset:%4 sc1" : "=v"(dst) : "v"(l16), "s"(ws), "s"(soff), "i"(imm) : "memory")
#define ALD1C(dst, voff, r, soff) asm volatile("buffer_load_dword %0, %1, %2, %3 offen sc1" : "=v"(dst) : "v"(voff), "s"(r), "s"(soff) : "memory")
#define CLD(op, dst, voff, r, soff, lim) asm volatile("s_cmp_ge_u32 %4, %5\n\ts_cbranch_scc1 1f\n\t" op "\n1:" : "=v"(dst) : "v"(voff), "s"(r), "s"(soff), "s"(w), "i"(lim) : "memory", "scc")
#define POLL(voff) CLD("buffer_load_dwordx2 %0, %1, %2, %3 offen sc1", pl, voff, ws, 0, 1)
#define WAITV(n) asm volatile("s_waitcnt vmcnt(%0)" :: "i"(n) : "memory")
#define WAITBAR(n) asm volatile("s_waitcnt vmcnt(%0) lgkmcnt(0)\n\ts_barrier" :: "i"(n) : "memory")
#define LBAR() asm volatile("s_waitcnt lgkmcnt(0)\n\ts_barrier" ::: "memory")
DI void st16(u32x4 v, rsrc_t r, unsigned voff, unsigned soff) {
  asm volatile("buffer_store_dwordx4 %0, %1, %2, %3 offen sc1\n\ts_nop 1" :: "v"(v), "v"(voff), "s"(r), "s"(soff) : "memory"); }
DI void st4(unsigned v, rsrc_t r, unsigned voff, unsigned soff) { __builtin_amdgcn_raw_buffer_store_b32(v, r, voff, soff, 16); }
DI bf16x8 ld16c(rsrc_t r, unsigned voff, unsigned soff) { return __builtin_bit_cast(bf16x8, __builtin_amdgcn_raw_buffer_load_b128(r, voff, soff, 16)); }
template <int CTRL> DI float dppadd(float v) { return v + __int_as_float(__builtin_amdgcn_update_dpp(0, __float_as_int(v), CTRL, 0xf, 0xf, true)); }
#define WAITF(n) do { unsigned sp_; asm volatile("s_cmp_lg_u32 %[w], 0\n\ts_cbranch_scc1 9f\n\ts_mov_b32 %[sp], 0\n\ts_waitcnt vmcnt(%[c])\n" \
    "1:\n\tv_cmp_ne_u64 vcc, %[t], %[p]\n\ts_cbranch_vccz 9f\n\ts_add_u32 %[sp], %[sp], 1\n\ts_cmp_gt_u32 %[sp], %[cap]\n\ts_cbranch_scc1 9f\n\ts_sleep 2\n\t" \
    "buffer_load_dwordx2 %[p], %[v], %[r], 0 offen sc1\n\ts_waitcnt vmcnt(0)\n\ts_branch 1b\n9:" \
    : [p] "+v"(pl), [sp] "=&s"(sp_) : [v] "v"(vf0), [r] "s"(ws), [t] "s"(tag), [w] "s"(w), [c] "i"(n), [cap] "i"(SPIN_CAP) : "memory", "scc", "vcc"); } while (0)
#define PUT(val, soff, who) do { unsigned long long t_; asm volatile("s_cmp_lg_u32 %4, 0\n\ts_cbranch_scc1 9f\n\tv_mov_b64 %0, %1\n\ts_mov_b64 exec, 1\n\t" \
    "buffer_store_dwordx2 %0, off, %2, %3 sc1\n\ts_mov_b64 exec, -1\n9:" : "=&v"(t_) : "s"(val), "s"(ws), "s"(soff), "s"(who) : "memory", "scc"); } while (0)
#define WAITV2(n0, n) asm volatile("s_waitcnt vmcnt(%1)\n\ts_cmp_eq_u32 %0, 0\n\ts_cbranch_scc1 9f\n\ts_waitcnt vmcnt(%2)\n9:" :: "s"(w), "i"(n0), "i"(n) : "memory", "scc")
#define STAMP(i) do { if ((i) == 14) { unsigned long long zt_; SBAR(); asm volatile("s_memrealtime %0\n\ts_waitcnt lgkmcnt(0)" : "=&s"(zt_) :: "memory"); dg1 = (unsigned)zt_; SBAR(); } } while (0)
#define STAMPV(i)
#define STAMP_END() do { if (w == 7) { const unsigned long long e_ = dg0 + 4000 + (unsigned)((dg1 - (unsigned)dg0) * 8); while (__builtin_amdgcn_s_memrealtime() < e_) __builtin_amdgcn_s_sleep(1); } } while (0)

DI u32x4 pack8(const f32x16& a, int o, float s) {
  return (u32x4){cvtpa(a[o] * s, a[o + 1] * s), cvtpa(a[o + 2] * s, a[o + 3] * s), cvtpa(a[o + 4] * s, a[o + 5] * s), cvtpa(a[o + 6] * s, a[o + 7] * s)}; }
DI u32x4 glu8(const f32x16& a, float nc, float sd) {
  float h[8];
#pragma unroll
  for (int i = 0; i < 8; ++i) { const float e = __builtin_amdgcn_exp2f(a[i] * nc); h[i] = a[8 + i] * __builtin_amdgcn_rcpf(__builtin_fmaf(e, sd, sd)); }
  return (u32x4){cvtpa(h[0], h[1]), cvtpa(h[2], h[3]), cvtpa(h[4], h[5]), cvtpa(h[6], h[7])}; }

typedef unsigned long long u64x2 __attribute__((ext_vector_type(2)));
typedef unsigned long long u64x8 __attribute__((ext_vector_type(8)));
__global__ __launch_bounds__(512, 2) void fused_block(const void* a_x, const void* a_wqkv, const void* a_wo, const void* a_wg, const void* a_wu, const void* a_wd, void* a_out, void* a_ws) {
  extern __shared__ __attribute__((aligned(16))) char smem[];
  u64x8 arg;
  asm volatile("s_load_dwordx16 %0, %1, 0x0" : "=s"(arg) : "s"(__builtin_amdgcn_kernarg_segment_ptr()));
  unsigned long long dg0; unsigned dg1 = 0; asm volatile("s_memrealtime %0" : "=s"(dg0) :: "memory");
  const lptr lds = (lptr)smem;
  const unsigned lds0 = (unsigned)(size_t)lds;
  const int tid = threadIdx.x, lane = tid & 63, w = __builtin_amdgcn_readfirstlane(tid >> 6);
  const int b = blockIdx.x, xcd = b & 7, bi = b >> 3;
  const int r32 = lane & 31, hi = lane >> 5;
  if (w == 0) {
    unsigned long long pc; unsigned len; asm volatile("s_getpc_b64 %0\n.Lfb_pc:\n\ts_mov_b32 %1, .Lfb_end-.Lfb_pc" : "=s"(pc), "=s"(len));
    const rsrc_t rc = mk((const void*)pc, len);
    const unsigned v = lane < 8 ? (bi * 8 + lane) * 128 : 0x7fffff00u;
    asm volatile("s_mov_b32 m0, %0\n\ts_nop 0\n\tbuffer_load_dword %1, %2, 0 offen lds" :: "s"(lds0 + LDS_BYTES - 256), "v"(v), "s"(rc) : "memory"); }
  const int rb = (xcd & 1) * 8 + (bi & 7), Q4 = xcd >> 1, sj = bi >> 3, set = Q4 * 4 + sj;
  const int rot = 2 * (xcd & 1) + (bi & 4), pos = (bi >> 1) & 1, half = bi & 1, kpart = rot + pos;
  const unsigned l3 = lane >> 3, l7 = lane & 7, l16 = lane * 16;
  const int wx = (w + 2 * sj) & 7, ww = (w + 2 * bi) & 7;
  unsigned vx0 = (rb * 128 + wx * 16 + l3) * 1024 + ((l7 ^ (lane >> 4)) * 16), vx1 = (vx0 + 8192) ^ 64;
  unsigned mx = lds0 + (wx >> 2) * UD + (wx & 3) * 2048, mw = lds0 + ww * 1024;
  const bool tb_ = sj == 0;
  const int p1 = 7 * Q4 + 2 * sj - 1, p2 = p1 + 1;
  const unsigned col0 = 128 * Q4 + (tb_ ? 1024 : sj * 512 - 448);
  const unsigned str12 = tb_ ? 3072 : 3584, wr = ww * 8 + l3, wpc = (l7 ^ ((lane >> 2) & 4)) * 16;
  unsigned vg0 = wr * 3072 + wpc + col0 * 2, vg1 = wr * str12 + wpc + (tb_ ? 1024 + 256 * Q4 : p1 * 128), vg2 = (vg1 - (tb_ ? 1024 : 0)) ^ 32;
  unsigned so0 = rot * 128, so1 = rot * (64 * 3072), so2 = rot * 64 * str12;
  PIN(vx0); PIN(vx1); PIN(vg0); PIN(vg1); PIN(vg2); asm volatile("" : "+s"(mx), "+s"(mw), "+s"(so0), "+s"(so1), "+s"(so2));
  SBAR();
  asm volatile("s_waitcnt lgkmcnt(0)" : "+s"(arg), "+s"(dg0) :: "memory");
  const void *px = (const void*)arg[0], *pwqkv = (const void*)arg[1], *pwo = (const void*)arg[2], *pwg = (const void*)arg[3], *pwu = (const void*)arg[4], *pwd = (const void*)arg[5];
  void *pout = (void*)arg[6], *pws = (void*)arg[7];
  rsrc_t ws = mk(pws, WS_END), rx = mk(px, 2048 * 1024), rg0 = mk(pwqkv, 512 * 3072);
  rsrc_t rg1 = mk(tb_ ? pwqkv : pwg, 512 * str12), rg2 = mk(tb_ ? pwqkv : pwu, 512 * str12);
  rsrc_t rg3 = mk(pwg, 512 * 3584), rg4 = mk(pwu, 512 * 3584);
#define KC(c) (unsigned)(((c) + rot) & 7)
#define SL(c) (((c) % 3) * 8192)
#define DMA1(c, i) do { \
    if ((i) == 0) dma(mx, SL(c), vx0, rx, KC(c) * 128); if ((i) == 1) dma(mx, SL(c) + 1024, vx1, rx, KC(c) * 128); \
    if ((i) == 2) dma(mw, SL(c) + U_G0, vg0, rg0, KC(c) * (64 * 3072)); if ((i) == 3) dma(mw, SL(c) + U_G1, vg1, rg1, KC(c) * 64 * str12); \
    if ((i) == 4) dma(mw, SL(c) + U_G2, vg2, rg2, KC(c) * 64 * str12); } while (0)
  unsigned sd_;
#define M0S(base, imm, off) do { SBAR(); sd_ = (off); asm volatile("s_add_i32 m0, %1, %2" :: "s"(sd_), "s"(base), "i"(imm) : "memory", "scc"); } while (0)
#define M01(c, i) M0S((i) < 2 ? mx : mw, SL(c) + ((i) == 0 ? 0 : (i) == 1 ? 1024 : (i) == 2 ? U_G0 : (i) == 3 ? U_G1 : U_G2), KC(c) * ((i) < 2 ? 128 : (i) == 2 ? 64 * 3072 : 64 * str12))
#define GO1(c, i) do { if ((i) == 0) dma_go(vx0, rx, sd_); if ((i) == 1) dma_go(vx1, rx, sd_); if ((i) == 2) dma_go(vg0, rg0, sd_); if ((i) == 3) dma_go(vg1, rg1, sd_); if ((i) == 4) dma_go(vg2, rg2, sd_); } while (0)
#define M02(c, i) M0S(mw, SL(c) + ((i) & 2 ? U_XA : U_G1) + ((i) & 1) * UD, KC(2 * ((c) - 8) + ((i) >> 1)) * (64 * 3584))
#define GO2(c, i) do { if ((i) & 1) dma_go(vg4, rg4, sd_); else dma_go(vg3, rg3, sd_); } while (0)
  dma(mx, 0, vx0, rx, so0); dma(mx, 1024, vx1, rx, so0); SBAR(); dma(mw, U_G0, vg0, rg0, so1); SBAR(); dma(mw, U_G1, vg1, rg1, so2); dma(mw, U_G2, vg2, rg2, so2); SBAR();
  STAMP(47);
  asm volatile("" : "+s"(ws), "+s"(rx), "+s"(rg0), "+s"(rg1), "+s"(rg2), "+s"(rg3), "+s"(rg4));
  unsigned vg3 = wr * 3584 + wpc + p2 * 128, vg4 = vg3 ^ 32; PIN(vg3); PIN(vg4);
  const int tt = w & 3, cg = w >> 2;
  const int q4 = (lane & 15) >> 2, p4 = lane & 3, g16 = (lane >> 4) & 1, swr = (q4 >> 1) * 4;
  const int wrow = (8 * hi + q4) * 128 + (p4 & 1) * 8;
  const int pq = 4 * cg + 2 * g16 + (p4 >> 1);
  const int gl0 = g16 * UD + (pq ^ swr) * 16, gl1 = gl0 ^ 32;
  lptr wp0 = lds + U_G0 + wrow + (pq ^ swr) * 16;
  lptr wp1 = lds + wrow + (tb_ ? (pq ^ swr) * 16 : gl0);
  lptr wp2 = lds + wrow + (tb_ ? UD + (pq ^ swr ^ 2) * 16 : gl1);
  lptr xp[4];
#pragma unroll
  for (int s = 0; s < 4; ++s) { xp[s] = lds + (tt >> 1) * UD + (tt & 1) * 4096 + r32 * 128 + (((2 * s + hi) ^ ((r32 >> 1) & 7)) * 16); PIN(xp[s]); }
  PIN(wp0); PIN(wp1); PIN(wp2);
  const int ah = xcd * 4 + (bi & 3), aq = bi >> 2, kh = w >> 2;
  const int qown = aq * 8 + 2 * (w & 3) + kh;
  const unsigned csb = WS_CS + set * 10240;
  auto colsum = [&](lptr cp, unsigned so) {
    const __bf16 o1 = (__bf16)1.0f; const bf16x8 ones = {o1, o1, o1, o1, o1, o1, o1, o1};
    const bf16x8 f0 = trfrag(cp, 512), f1 = trfrag(cp + 2048, 512), f2 = trfrag(cp + 4096, 512), f3 = trfrag(cp + 6144, 512);
    const f32x16 cs = MFMA(ones, f3, MFMA(ones, f2, MFMA(ones, f1, MFMA(ones, f0, (f32x16){}))));
    const float ch = rbf(cs[0]);
    st4(cvtpk(ch, cs[0] - ch), ws, l16 < 512 ? l16 : 0x7fffff00u, csb + so + (kpart >> 2) * 512 + (kpart & 3) * 4);
  };
  unsigned vf0 = 0, scs, sst;
  u32x2 ep; u32x4 xr; unsigned e0, e1; unsigned long long tag;
  const unsigned srow = rb * 128 + set * 8 + w;
  float sv[8], sa, sb, smean;
  auto swapadd16 = [](float v) { auto r = __builtin_amdgcn_permlane16_swap(__float_as_uint(v), __float_as_uint(v), false, false); return __uint_as_float(r[0]) + __uint_as_float(r[1]); };
  auto swapadd32 = [](float v) { auto r = __builtin_amdgcn_permlane32_swap(__float_as_uint(v), __float_as_uint(v), false, false); return __uint_as_float(r[0]) + __uint_as_float(r[1]); };
#define STATS(i) do { \
    if ((i) == 0 || (i) == 1) { sv[4 * ((i) & 1)] = bflo(xr[2 * ((i) & 1)]); sv[4 * ((i) & 1) + 1] = bfhi(xr[2 * ((i) & 1)]); sv[4 * ((i) & 1) + 2] = bflo(xr[2 * ((i) & 1) + 1]); sv[4 * ((i) & 1) + 3] = bfhi(xr[2 * ((i) & 1) + 1]); \
      PIN(sv[4 * ((i) & 1)]); PIN(sv[4 * ((i) & 1) + 1]); PIN(sv[4 * ((i) & 1) + 2]); PIN(sv[4 * ((i) & 1) + 3]); } \
    if ((i) == 2) { sa = (sv[0] + sv[1]) + sv[2]; PIN(sa); sb = (sv[3] + sv[4]) + sv[5]; PIN(sb); } \
    if ((i) == 3) { sa = ((sa + sv[6]) + sv[7]) + sb; PIN(sa); } \
    if ((i) == 4 || (i) == 14) { sa = dppadd<0x121>(sa); PIN(sa); } if ((i) == 5 || (i) == 15) { sa = dppadd<0x122>(sa); PIN(sa); } \
    if ((i) == 6 || (i) == 16) { sa = dppadd<0x124>(sa); PIN(sa); } if ((i) == 7 || (i) == 17) { sa = dppadd<0x128>(sa); PIN(sa); } \
    if ((i) == 8 || (i) == 18) { sa = swapadd16(sa); PIN(sa); } \
    if ((i) == 9) { smean = swapadd32(sa) * (1.f / 512); PIN(smean); sa = 0.f; } \
    if ((i) >= 10 && (i) <= 13) { const float d_ = sv[(2 * (i) - 20) & 7] - smean, e_ = sv[(2 * (i) - 19) & 7] - smean; sa = __builtin_fmaf(e_, e_, __builtin_fmaf(d_, d_, sa)); PIN(sa); } \
    if ((i) == 19) { sa = rbf(swapadd32(sa) * (1.f / 512)); PIN(sa); } \
    if ((i) == 20) { sa = __builtin_amdgcn_sqrtf(rbf(sa + 1e-5f)); PIN(sa); } \
    if ((i) == 21) st4(cvtpk(-smean, sa), ws, lane ? 0x7fffff00u : 0u, WS_STAT + srow * 4); } while (0)
  bf16x8 xf[32], wa[3], wb[3];
  f32x16 acc0 = {}, acc1 = {}, acc2 = {};
  u32x2 pl; unsigned stat; u32x4 cs0, cs1, cs2;
#define RD1(W, ks) do { xf[ks] = LDS8(xp[(ks) & 3] + SL((ks) >> 2)); W[0] = trfrag(wp0 + SL((ks) >> 2) + ((ks) & 3) * 2048, 512); } while (0)
#define RD1B(W, ks) W[1] = trfrag(wp1 + U_G1 + SL((ks) >> 2) + ((ks) & 3) * 2048, 512)
#define RD1C(W, ks) W[2] = trfrag(wp2 + U_G1 + SL((ks) >> 2) + ((ks) & 3) * 2048, 512)
#define RD2A(W, ks) W[0] = trfrag(wp1 + SL(8 + ((ks) >> 3)) + ((ks) & 4 ? U_XA : U_G1) + ((ks) & 3) * 2048, 512)
#define RD2B(W, ks) W[1] = trfrag(wp2 + SL(8 + ((ks) >> 3)) + ((ks) & 4 ? U_XA : U_G1) + ((ks) & 3) * 2048, 512)
  const int n2 = rb >> 1, ks2 = (rb & 1) * 16 + set + 32 * ((w + 7) & 7);
#define W2LOAD() do { if (ks2 < 144) { const rsrc_t r_ = mk(ks2 < 112 ? pwd : pwo, ks2 < 112 ? 1792 * 1024 : 512 * 1024); const unsigned so_ = (ks2 < 112 ? ks2 : ks2 - 112) * 16384 + n2 * 128; \
      LANE(n_); const unsigned v_ = (n_ >> 3) * 1024 + ((n_ & 7) ^ ((n_ >> 2) & 4)) * 16; \
      dma_nt(lds0 + w * 2048, L_PRIV, v_, r_, so_); dma_nt(lds0 + w * 2048, L_PRIV + 1024, v_, r_, so_ + 8192); } } while (0)
#define PUTL(val, soff) do { unsigned long long t_; unsigned a_; asm volatile("s_cmp_lg_u32 %6, 0\n\ts_cbranch_scc1 9f\n\tv_mov_b64 %0, %2\n\tv_lshrrev_b32 %1, 1, %3\n\ts_mov_b64 exec, 0xffff\n\t" \
    "buffer_store_dwordx2 %0, %1, %4, %5 offen sc1\n\ts_mov_b64 exec, -1\n9:" : "=&v"(t_), "=&v"(a_) : "s"(val), "v"(l16), "s"(ws), "s"(soff), "s"(w) : "memory", "scc"); } while (0)
#define FLAG(f) do { if (X_FL) PUTL(tag, (f) + (set * 16 + rb) * FS); else PUT(tag, (f) + (set * 16 + rb) * FS, w); } while (0)
  if (w < 5) { const unsigned o_ = (tid & 11) == 0 ? 0x3f803f80u : 0u;
    *(__attribute__((address_space(3))) u32x4*)(lds + L_ONES + tid * 16) = (u32x4){o_, o_, o_, o_}; }
  else if (w < 7) {
    const unsigned one_ = hi == ((lane >> 2) & 1) ? 0x3f80u << ((lane & 1) * 16) : 0u, dw_ = ((lane >> 1) & 1) + ((lane >> 2) & 6) - 4 * (w - 5);
    *(__attribute__((address_space(3))) u32x4*)(lds + L_ONES + tid * 16) = (u32x4){dw_ == 0 ? one_ : 0u, dw_ == 1 ? one_ : 0u, dw_ == 2 ? one_ : 0u, dw_ == 3 ? one_ : 0u}; }
  unsigned vep = lane == 0 || lane > (X_FL ? 6 : 10) || !X_PT || (X_FL && b >= 224) ? WS_EPOCH : WS_STAT - 128 + b * (X_FL ? 768 : 1280) + l16 * 8; PIN(vep);
  if (X_PT == 1) CLD("buffer_load_dwordx2 %0, %1, %2, %3 offen" X_SC, ep, vep, ws, 0, 1);
  SBAR();
  asm volatile("s_barrier" ::: "memory");
  STAMP(48);
  DMA1(1, 0); DMA1(1, 1); DMA1(1, 2); DMA1(1, 3); DMA1(1, 4);
  STAMP(1);
  WAITBAR(5);
  STAMP(2);
  RD1(wa, 0); RD1B(wa, 0); RD1C(wa, 0);
  if (X_PT == 3) CLD("buffer_load_dwordx2 %0, %1, %2, %3 offen" X_SC, ep, vep, ws, 0, 1);
#define STATG(g) do { STATS(2 * ((g) - 12)); STATS(2 * ((g) - 12) + 1); } while (0)
#define STEP1(ks, WC, WN) do { constexpr int c_ = ((ks) + 1) >> 2, s_ = ((ks) + 1) & 3; \
    if (s_ == 0) { SBAR(); \
      if ((c_ == 1 || c_ == 2) && w < 3 && pos == c_ - 1) \
        colsum(lxor(w == 0 ? wp0 : w == 1 ? wp1 + U_G1 : wp2 + U_G1, half * 64) + SL(c_ - 1), (half * 5 + w) * 1024); \
      if (c_ == 1 && X_PT != 1 && X_PT != 3) CLD("buffer_load_dwordx2 %0, %1, %2, %3 offen" X_SC, ep, vep, ws, 0, 1); \
      if (c_ == 3 && w == 0) { const unsigned v_ = l16 * 224 + p2 * 128;     \
        asm volatile("s_mov_b32 m0, %0\n\ts_nop 0\n\tbuffer_load_dword %1, %2, %3 offen lds" :: "s"(lds0 + LDS_BYTES - 256), "v"(v_), "s"(half ? rg4 : rg3), "s"(kpart * (64 * 3584)) : "memory"); } \
      if (c_ == 5) W2LOAD(); \
      if (c_ == 6 && w == 0) { LANE(n_); vf0 = WS_F0 + (n_ < 16 ? set * 16 + n_ : (n_ & 15) * 16 + rb) * FS; PIN(vf0); } \
      if (c_ == 7) WAITF(4); \
      STAMP(16 + 2 * c_); if (c_ == 5 && ks2 < 144) WAITV(7); else WAITV(c_ == 1 ? 6 : c_ < 7 ? 5 : c_ == 7 ? 4 : 8); LBAR(); STAMP(17 + 2 * c_); \
      if (c_ == 2) { PIN(ep); PIN(xr); e0 = __builtin_amdgcn_readfirstlane(ep[0]); e1 = __builtin_amdgcn_readfirstlane(ep[1]);     \
        tag = (unsigned long long)((e1 ^ 0x85EBCA6Bu) + e0) << 32 | (e0 * 0x9E3779B1u + 0x7F4A7C15u); }     \
      if (c_ == 4) FLAG(WS_F0); \
      if (c_ == 6) POLL(vf0); \
      if (c_ == 8) { wp1 = wp0 - U_G0 + (l16 & 256) * (UD / 256); wp2 = lxor(wp1, 32); PIN(wp1); PIN(wp2); } } \
    SBAR(); \
    if ((ks) < 31) RD1(WN, ((ks) + 1) & 31); else RD2A(WN, 0); \
    if (c_ <= 5) M01(c_ + 2, s_); else M02(c_ + 2, s_); STATG(3 * ((ks) - 3) - 1); SBAR(); \
    acc0 = MFMA(WC[0], xf[ks], acc0); SBAR(); \
    if (c_ <= 5) GO1(c_ + 2, s_); else GO2(c_ + 2, s_); \
    if ((ks) < 31) RD1B(WN, ((ks) + 1) & 31); else RD2B(WN, 0); \
    if (c_ >= 1 && c_ <= 5 && s_ == 0) M01(c_ + 2, 4); if ((ks) < 2) M01(2, (ks) * 4); if (c_ == 8) M02(10, 1); \
    if ((ks) == 27) { vf0 = (l16 & 496) >> 2; PIN(vf0); scs = csb + cg * 5120; sst = WS_STAT + (rb * 4 + tt) * 128; asm volatile("" :: "s"(scs), "s"(sst)); } SBAR(); STATG(3 * ((ks) - 3)); SBAR(); \
    acc1 = MFMA(WC[1], xf[ks], acc1); SBAR(); \
    if (c_ >= 1 && c_ <= 5 && s_ == 0) GO1(c_ + 2, 4); if ((ks) < 2) GO1(2, (ks) * 4); if (c_ == 8) { GO2(10, 1); M02(10, 2); } \
    if ((ks) == 27) ALD1C(stat, vf0, ws, sst); if ((ks) == 28) ALD4O(cs1, scs, 1024); \
    if ((ks) < 31) RD1C(WN, ((ks) + 1) & 31); SBAR(); STATG(3 * ((ks) - 3) + 1); SBAR(); \
    acc2 = MFMA(WC[2], xf[ks], acc2); SBAR(); if (c_ == 8) GO2(10, 2); \
    if ((ks) == 2) ALD4(xr, l16, rx, srow * 1024); if ((ks) == 27) ALD4O(cs0, scs, 0); if ((ks) == 28) ALD4O(cs2, scs, 2048); SBAR(); } while (0)
#define STEP1x4(k) STEP1(k, wa, wb); STEP1(k + 1, wb, wa); STEP1(k + 2, wa, wb); STEP1(k + 3, wb, wa)
  STEP1x4(0); STEP1x4(4); STEP1x4(8); STEP1x4(12); STEP1x4(16); STEP1x4(20); STEP1x4(24); STEP1x4(28);
  M02(10, 3);
  asm volatile("s_waitcnt vmcnt(5)" : "+v"(stat), "+v"(cs0), "+v"(cs1), "+v"(cs2) :: "memory");
  STAMP(3);
  unsigned nm2 = (stat & 0xffffu) * 0x10001u;
  bf16x8 nmf = __builtin_bit_cast(bf16x8, (u32x4){nm2, nm2, nm2, nm2});
  PIN(nmf); SBAR(); acc0 = MFMA(__builtin_bit_cast(bf16x8, cs0), nmf, acc0); SBAR(); GO2(10, 3); acc1 = MFMA(__builtin_bit_cast(bf16x8, cs1), nmf, acc1); acc2 = MFMA(__builtin_bit_cast(bf16x8, cs2), nmf, acc2);
  const float sd = bfhi(stat), rstd = __builtin_amdgcn_rcpf(sd), gsc = -1.4426950408889634f * rstd, qsc = rstd * (0.25f * 1.4426950408889634f), sc0 = sj == 1 ? qsc : rstd;
  const int tile = rb * 4 + tt, hd = 8 * Q4 + (tb_ ? 0 : 4) + 2 * cg;
  const unsigned sok = WS_KV + hd * 131072 + tile * 1024, soq = WS_Q + (hd * 64 + tile) * 1024, isv = (sj + 1) & 2;
  unsigned l16k = (l16 & 0x330) | (l16 & 64) * 2 | (l16 & 128) / 2; if (sj == 1) l16k = l16; PIN(l16k);
  SBAR();
  STAMP(4);
  u32x4 qf0, qf1, hp, hq; bf16x8 eye0, eye1;
  f32x16 acc3 = {}, acc4 = {};
  auto glu2 = [&](const f32x16& a, int i) {
    const float e0_ = __builtin_amdgcn_exp2f(a[i] * gsc), e1_ = __builtin_amdgcn_exp2f(a[i + 1] * gsc);
    return cvtpa(a[8 + i] * __builtin_amdgcn_rcpf(__builtin_fmaf(e0_, sd, sd)), a[9 + i] * __builtin_amdgcn_rcpf(__builtin_fmaf(e1_, sd, sd))); };
  const unsigned mk_ = lds0 + ((w + aq) & 7) * 1024, skv = WS_KV + ah * 131072 + ((w + aq) & 7) * 1024;
#define KVM(i) M0S(mk_, (i) * 8192 + ((i) >= 8 ? L_V - 65536 : 0), skv + (i) * 8192)
#define KVG() asm volatile("buffer_load_dwordx4 %0, %1, %2 offen sc1 lds" :: "v"(l16), "s"(ws), "s"(sd_) : "memory")
#define PRE2(ks) do { constexpr int c_ = 8 + (((ks) + 1) >> 3), s_ = ((ks) + 1) & 7; \
    if (s_ == 0 && c_ < 12) { SBAR(); \
      if (c_ == 9 && (w == 1 || w == 2)) colsum(lxor(w == 1 ? wp1 : wp2, half * 64) + SL(8) + (pos ? U_XA : U_G1), (half * 5 + 2 + w) * 1024); \
      STAMP(16 + 2 * c_); if (c_ == 9) WAITV(6); else if (c_ == 10) WAITV2(1, 0); \
      LBAR(); STAMP(17 + 2 * c_); } \
    if ((ks) == K_F) { asm volatile("s_waitcnt vmcnt(4)\n\ts_barrier" ::: "memory"); FLAG(WS_F1); STAMP(51); } \
    if ((ks) == K_P) { vf0 = WS_F1 + Q4 * 64 * FS + l16 * (FS / 16); POLL(vf0); }     \
    if ((ks) == K_C) { WAITF(0); STAMP(52); asm volatile("s_barrier" ::: "memory"); STAMP(53); } \
    if ((ks) == 31) { WAITV(6); PIN(cs0); PIN(cs1); } \
    SBAR(); } while (0)
#define FILL2(ks) do { \
    if ((ks) == 0 || (ks) == 1) { const u32x4 p_ = pack8(acc0, ((ks) & 1) * 8, sc0); if ((ks) == 0) hp = p_; else hq = p_; \
      if (isv) st16(p_, ws, l16k, sj == 1 ? soq + ((ks) & 1) * 65536 : sok + ((ks) & 1) * 131072); } \
    if ((ks) == 1) { eye0 = LDS8(lds + L_EYE + l16); eye1 = LDS8(lds + L_EYE + 1024 + l16); } \
    if ((ks) == 2 && !isv) acc0 = MFMA(__builtin_bit_cast(bf16x8, hq), eye1, MFMA(__builtin_bit_cast(bf16x8, hp), eye0, (f32x16){})); \
    if ((ks) == 3 && !isv) { LANE(n_); const unsigned vvo = (n_ & 16) * 8192 + ((n_ >> 5) * 16 + (n_ & 15)) * 16; \
      st16((u32x4){cvtpk(acc0[0], acc0[1]), cvtpk(acc0[2], acc0[3]), cvtpk(acc0[4], acc0[5]), cvtpk(acc0[6], acc0[7])}, ws, vvo, sok + 65536); \
      st16((u32x4){cvtpk(acc0[8], acc0[9]), cvtpk(acc0[10], acc0[11]), cvtpk(acc0[12], acc0[13]), cvtpk(acc0[14], acc0[15])}, ws, vvo, sok + 65536 + 512); } \
    if ((ks) >= 4 && (ks) <= 6 && tb_) st16(pack8((ks) < 6 ? acc1 : acc2, ((ks) & 1) * 8, (ks) < 6 ? rstd : qsc), ws, (ks) < 6 ? l16k : l16, (ks) < 6 ? sok + ((ks) & 1) * 131072 : soq); \
    if ((ks) == 6 && tb_) st16(pack8(acc2, 8, qsc), ws, l16, soq + 65536); \
    if ((ks) >= 4 && (ks) <= 18 && !((ks) & 1) && !tb_) { \
      if ((ks) == 16) st16(hp, ws, l16, WS_H + (tile * 112 + 4 * p1 + 2 * cg) * 1024); \
      if ((ks) < 12) { hp[(((ks) >> 1) + 2) & 3] = glu2(acc1, ((ks) + 4) & 6); PIN(hp); } \
      else { hq[(((ks) >> 1) + 2) & 3] = glu2(acc2, ((ks) + 4) & 6); PIN(hq); } \
      if ((ks) == 18) st16(hq, ws, l16, WS_H + (tile * 112 + 4 * p1 + 2 * cg + 1) * 1024); } \
    if ((ks) == 26) { PIN(nm2); nmf = __builtin_bit_cast(bf16x8, (u32x4){nm2, nm2, nm2, nm2}); PIN(nmf); } \
    if ((ks) == 31) { acc3 = MFMA(__builtin_bit_cast(bf16x8, cs0), nmf, acc3); acc4 = MFMA(__builtin_bit_cast(bf16x8, cs1), nmf, acc4); } \
    } while (0)
#define STEP2(ks, WC, WN) do { constexpr int c_ = 8 + (((ks) + 1) >> 3), s_ = ((ks) + 1) & 7; \
    if ((ks) < 31) RD2A(WN, ((ks) + 1) & 31); \
    if (s_ == 7) RD2B(WN, ((ks) + 1) & 31); \
    if (c_ == 9 && s_ < 4) M02(11, s_); \
    if ((ks) == K_C + 1) KVM(0); if ((ks) == K_C + 2) KVM(8); SBAR(); \
    acc3 = MFMA(WC[0], xf[ks], acc3); SBAR(); \
    if (c_ == 9 && s_ < 4) GO2(11, s_); \
    if ((ks) == K_C) ALD4O(cs0, scs, 3072); \
    if ((ks) == K_C + 1 || (ks) == K_C + 2) KVG(); if ((ks) == K_C + 3) ALD4C(qf1, l16, ws, WS_Q + (ah * 64 + qown) * 1024); \
    if ((ks) < 31 && s_ != 7) RD2B(WN, ((ks) + 1) & 31); \
    SBAR(); FILL2(ks); \
    if ((ks) == K_C + 1) KVM(4); if ((ks) == K_C + 2) KVM(12); SBAR(); \
    acc4 = MFMA(WC[1], xf[ks], acc4); SBAR(); \
    if ((ks) == K_C) ALD4O(cs1, scs + 4096, 0); \
    if ((ks) == K_C + 1 || (ks) == K_C + 2) KVG(); if ((ks) == K_C + 3) ALD4C(qf0, l16, ws, WS_Q + (ah * 64 + (qown ^ 1)) * 1024); \
    SBAR(); PRE2((ks) + 1); } while (0)
#define STEP2x4(k) STEP2(k, wa, wb); STEP2(k + 1, wb, wa); STEP2(k + 2, wa, wb); STEP2(k + 3, wb, wa)
  STEP2x4(0); STEP2x4(4); STEP2x4(8); STEP2x4(12); STEP2x4(16); STEP2x4(20); STEP2x4(24); STEP2x4(28);
  STAMP(5);
  {
    LANE(n_); const lptr s2_ = lds + L_PRIV + w * 2048 + (4 * (n_ >> 5) + ((n_ >> 2) & 3)) * 128 + (n_ & 1) * 8; const int pc_ = (((n_ >> 3) & 2) + ((n_ >> 1) & 1)) ^ ((n_ >> 1) & 4);
    hp = __builtin_bit_cast(u32x4, trfrag(s2_ + pc_ * 16, 1024)); hq = __builtin_bit_cast(u32x4, trfrag(s2_ + (pc_ ^ 4) * 16, 1024)); }
  st16(glu8(acc3, gsc, sd), ws, l16, WS_H + (tile * 112 + 4 * p2 + 2 * cg) * 1024);
  LANE(ln); const int r3 = ln & 31, hf = ln >> 5;
  const int tb = xcd * 4 + (bi & 3), n64 = bi >> 2;
  lptr kp = lds + kh * 32768 + l16, kq = lds + (kh ^ 1) * 32768 + l16;
  const bool vrow = r3 < 16;
  int vstep = vrow ? 4096 : 0;
  lptr vp = lds + (vrow ? L_V + kh * 32768 + hf * 256 : L_ONES - 256) + r3 * 16;
  int idx = 0xDDDDDDDD;
  float aone = hf ? 0.f : 1.f;
  PIN(kp); PIN(kq); PIN(vstep); PIN(vp); PIN(idx); PIN(aone);
  f32x16 nm0, nm1, o0, o1, sA, sB;
  const f32x16 z = {};
  { u32x4 zz = {}; PIN(zz); o0 = MFMA(__builtin_bit_cast(bf16x8, zz), __builtin_bit_cast(bf16x8, zz), z); PIN(o0); o1 = MFMA(__builtin_bit_cast(bf16x8, zz), __builtin_bit_cast(bf16x8, zz), o0); PIN(o1); }
  STAMP(6);
  WAITBAR(1);
  PIN(qf0); PIN(qf1);
  STAMP(7);
  const bf16x8 q0 = __builtin_bit_cast(bf16x8, qf0), q1 = __builtin_bit_cast(bf16x8, qf1);
  auto hmax = [&](float m) { auto rr = __builtin_amdgcn_permlane32_swap(__float_as_uint(m), __float_as_uint(m), false, false);
    return __builtin_fmaxf(__uint_as_float(rr[0]), __uint_as_float(rr[1])); };
  auto max16 = [&](float m, const f32x16& s) {
#pragma unroll
    for (int i = 0; i < 16; i += 2) m = __builtin_fmaxf(__builtin_fmaxf(m, s[i]), s[i + 1]);
    return m; };
  __builtin_amdgcn_s_setprio(1);
  bf16x8 k1 = LDS8(kp), va, vb;
  {
    const bf16x8 k0 = LDS8(kq);
    va = LDS8(vp); SBAR();
    if (ks2 < 144) { st16(hp, ws, l16, WS_W2 + (2 * n2 * 144 + ks2) * 1024); st16(hq, ws, l16, WS_W2 + ((2 * n2 + 1) * 144 + ks2) * 1024); }
    hp[0] = glu2(acc4, 0); PIN(hp); SBAR();
    const f32x16 a2 = MFMA(k1, q0, z), a0 = MFMA(k0, q0, z); SBAR();
    hp[1] = glu2(acc4, 2); PIN(hp); SBAR();
    const f32x16 a3 = MFMA(k1, q1, z), a1 = MFMA(k0, q1, z); SBAR();
    float x0 = a0[0], x2 = a2[0];
#pragma unroll
    for (int i = 1; i < 15; i += 2) { x2 = max3(x2, a2[i], a2[i + 1]); x0 = max3(x0, a0[i], a0[i + 1]); }
    nm0 = __builtin_amdgcn_mfma_f32_32x32x2f32(aone, -REF_OFF - hmax(max3(max3(x0, x2, a0[15]), a2[15], a2[15])), z, 0, 0, 0); SBAR();
    float x1 = a1[0], x3 = a3[0];
#pragma unroll
    for (int i = 1; i < 15; i += 2) { x3 = max3(x3, a3[i], a3[i + 1]); x1 = max3(x1, a1[i], a1[i + 1]); }
    float m1 = -REF_OFF - hmax(max3(max3(x1, x3, a1[15]), a3[15], a3[15])); PIN(m1); SBAR();
    sA = MFMA(k1, q0, nm0); SBAR();
    hp[2] = glu2(acc4, 4); PIN(hp); SBAR();
    nm1 = __builtin_amdgcn_mfma_f32_32x32x2f32(aone, m1, z, 0, 0, 0); SBAR();
    hp[3] = glu2(acc4, 6); PIN(hp);
    st16(hp, ws, l16, WS_H + (tile * 112 + 4 * p2 + 2 * cg + 1) * 1024); SBAR();
  }
  f32x8 pa, pb;
  f32x16 g00, g01, g10, g11;
  bf16x8 fa[4], fb[4];
  unsigned sw0 = WS_W2 + (2 * n64 * 144 + 14 * w) * 1024, sw1 = sw0 + 144 * 1024, sh0 = WS_H + ((2 * tb + kh) * 112 + 14 * w) * 1024, sh1 = WS_H + ((2 * tb + 1 - kh) * 112 + 14 * w - 1) * 1024;
#define KADV() do { sw0 += 2048; sw1 += 2048; sh0 += 2048; sh1 += 2048; asm volatile("" : "+s"(sw0), "+s"(sw1), "+s"(sh0), "+s"(sh1)); } while (0)
#define EX(P, S, i) P[(i) & 7] = __builtin_amdgcn_exp2f(S[i])
  unsigned so_;
#define UN_(...) __VA_ARGS__
#define AP_(ph, f, ...) f(ph, __VA_ARGS__)
#define AP2_(...) AP_(__VA_ARGS__)
#define MS(slot, M) do { AP2_(0, UN_ slot); SBAR(); M; SBAR(); AP2_(1, UN_ slot); SBAR(); } while (0)
#define NO(ph, x) do {} while (0)
#define DO(ph, x) do { if (ph) { x; } } while (0)
#define LD(ph, dst, base, imm) do { if (ph) dst = ld16c(ws, l16 + (imm), base); } while (0)
#define DM(ph, base, imm, off) do { if (ph) asm volatile("buffer_load_dwordx4 %0, %1, %2 offen sc1 lds" :: "v"(l16), "s"(ws), "s"(so_) : "memory"); \
    else { so_ = (off); asm volatile("s_add_i32 m0, %1, %2" :: "s"(so_), "s"(base), "i"(imm) : "memory", "scc"); } } while (0)
#define KV(ph, i) DM(ph, mk_, (i) * 8192 + ((i) >= 8 ? L_V - 65536 : 0), skv + (i) * 8192)
#define N_ (NO, 0)
#define T2(u, ...) T2X(o1 = SMF(vb, pb, o1, idx), u, __VA_ARGS__)
#define T2X(FIN, u, A3, MA, A4, B1, B2, MB, B4) do { \
    EX(pa, sA, 0); EX(pa, sA, 1); SBAR(); FIN; SBAR(); vb = LDS8(vp + (u) * 1024 + 512); SBAR(); \
    EX(pa, sA, 2); EX(pa, sA, 3); EX(pa, sA, 4); EX(pa, sA, 5); SBAR(); sB = MFMA(k1, q1, nm1); SBAR(); k1 = LDS8(kp + ((u) + 1) * 1024); SBAR(); \
    EX(pa, sA, 6); EX(pa, sA, 7); SBAR(); EX(pb, sA, 8); EX(pb, sA, 9); MS(A3, o0 = SMF(va, pa, o0, idx)); \
    EX(pb, sA, 10); EX(pb, sA, 11); EX(pb, sA, 12); EX(pb, sA, 13); MS(A4, MA); EX(pb, sA, 14); EX(pb, sA, 15); SBAR(); \
    EX(pa, sB, 0); EX(pa, sB, 1); MS(B1, o0 = SMF(vb, pb, o0, idx)); \
    EX(pa, sB, 2); EX(pa, sB, 3); EX(pa, sB, 4); EX(pa, sB, 5); MS(B2, sA = MFMA(k1, q0, nm0)); \
    EX(pa, sB, 6); EX(pa, sB, 7); SBAR(); EX(pb, sB, 8); EX(pb, sB, 9); SBAR(); o1 = SMF(va, pa, o1, idx); SBAR(); va = LDS8(vp + ((u) + 1) * 1024); SBAR(); \
    EX(pb, sB, 10); EX(pb, sB, 11); EX(pb, sB, 12); EX(pb, sB, 13); MS(B4, MB); EX(pb, sB, 14); EX(pb, sB, 15); SBAR(); } while (0)
#define NOP_ do {} while (0)
#define ADV() do { kp += 4096; vp += vstep; } while (0)
#define GM(g, wf, hf_) do { g = MFMA(hf_, wf, g); PIN(g); } while (0)
#define G2P(u, C, D, X0, X1, X2, X3, X4, X5) do { \
    T2(u, X0, GM(g00, C[0], C[2]), (LD, D[3], sh1, (u) * 512), X1, X2, GM(g10, C[1], C[2]), (LD, C[2], sh0, (u) * 512)); \
    T2(u + 1, X3, GM(g11, C[1], C[3]), (LD, C[1], sw1, (u) * 512), X4, X5, GM(g01, C[0], C[3]), (LD, C[0], sw0, (u) * 512)); } while (0)
#define PRIO_AT(k, n) asm volatile("s_cmp_lg_u32 %0, " #k "\n\ts_cbranch_scc1 1f\n\ts_setprio " #n "\n1:" :: "s"(it) : "scc")
#define PRIO(n) __builtin_amdgcn_s_setprio(n)
  T2X(NOP_, 0, (KV, 1), NOP_, N_, (KV, 5), (KV, 9), NOP_, N_); PRIO(0); T2(1, (KV, 13), NOP_, N_, (KV, 2), (KV, 6), NOP_, N_);
  STAMP(41);
  asm volatile("s_waitcnt vmcnt(6)\n\ts_barrier" ::: "memory");
  STAMP(42);
  FLAG(WS_F2);
  auto side0 = [&]() { vf0 = WS_F2 + ((l16 & 240) + __builtin_amdgcn_ubfe(n64 * 0x02020200u + 0x01010000u + (tb >> 1), (l16 >> 5) & 24, 4)) * FS; PIN(vf0); };
  auto side1 = [&]() { hp = (u32x4){~0u, ~0u, ~0u, ~0u}; PIN(hp); };
  bf16x8 zz;
  auto side2 = [&]() { zz = (bf16x8){}; PIN(zz); };
#define EPOCH() PUT((unsigned long long)e1 << 32 | (e0 + 1), WS_EPOCH, w | b)
#define KPIN() asm volatile("" : "+s"(sw0), "+s"(sw1), "+s"(sh0), "+s"(sh1))
  PRIO(2); T2(2, (KV, 10), EPOCH(), N_, (KV, 14), (KV, 3), side0(), N_); T2(3, (KV, 7), KPIN(), N_, (KV, 11), (KV, 15), side1(), N_); ADV();
  PRIO(1); T2(0, (DO, st16(hp, ws, l16, WS_O + (qown * 32 + ah) * 1024)), side2(), N_, N_, (DO, POLL(vf0)), NOP_, N_); PRIO(0); T2(1, N_, NOP_, N_, N_, N_, NOP_, N_);
  STAMP(43);
  WAITF(0);
  STAMP(44);
  asm volatile("s_waitcnt vmcnt(0)\n\ts_barrier" ::: "memory");
  STAMP(8);
  auto lwo_of = [](int g) { return (unsigned)(g < 3 ? g : g < 6 ? g + 1 : g + 3) * 8192; };
  unsigned swo = WS_W2 + (2 * n64 * 144 + 112 + 4 * w) * 1024, lwo = lds0 + lwo_of(w);
  PRIO(3);
  {
#define WPIN(ph, x) do { if (ph) { vf0 = WS_F3 + xcd * 32 * FS + (l16 & 496) * (FS / 16); PIN(vf0); asm volatile("" : "+s"(swo), "+s"(lwo)); } } while (0)
#define GZ(g) do { PIN(zz); g = MFMA(zz, zz, z); PIN(g); } while (0)
    T2(2, (LD, fa[2], sh0, 0), GZ(g00), (LD, fa[0], sw0, 0), (LD, fa[1], sw1, 0), (LD, fa[3], sh1, 1024), GZ(g10), (DO, if (X_FL) PUTL(tag, WS_F3 + b * FS); else PUT(tag, WS_F3 + b * FS, w)));
    T2(3, (LD, fb[2], sh0, 1024), GZ(g11), (WPIN, 0), (LD, fb[1], sw1, 1024), N_, GZ(g01), (LD, fb[0], sw0, 1024)); ADV(); KADV(); }
#pragma unroll 1
  for (int it = 0; it < 4; ++it) { STAMPV(54 + it); PRIO_AT(2, 2); PRIO_AT(3, 1); G2P(0, fa, fb, N_, N_, N_, N_, N_, N_); PRIO_AT(3, 0); G2P(2, fb, fa, N_, N_, N_, N_, N_, N_); ADV(); KADV(); }
#define WO(ph, i) do { if (ph) asm volatile("buffer_load_dwordx4 %0, %1, %2 offen offset:%3 sc1 lds" :: "v"(l16), "s"(ws), "s"(so_), "i"(((i) & 3) * 1024) : "memory"); \
    else if (((i) & 3) == 0) DM(0, lwo, (i) * 1024, swo + ((i) >> 2) * (144 * 1024)); } while (0)
  STAMPV(59);
  asm volatile("s_barrier" ::: "memory");
  STAMPV(60);
  PRIO(3);
  G2P(0, fa, fb, (WO, 0), (DO, POLL(vf0)), (WO, 1), (WO, 2), N_, (WO, 3)); G2P(2, fb, fa, (WO, 4), N_, (WO, 5), (WO, 6), N_, (WO, 7)); ADV(); KADV();
  PRIO(2);
  bf16x8 fc[4];
  typedef __attribute__((address_space(3))) f32x4* l4p;
  typedef __attribute__((address_space(3))) float* l1p;
  lptr xw = lds + L_PRIV + w * 2048 + l16, xl = lds + 65536 + w * 256 + l16 / 4; PIN(xw); PIN(xl);
  G2P(0, fa, fb, (LD, fc[0], sw0, 1024), N_, (LD, fc[2], sh0, 1024), (LD, fc[1], sw1, 1024), N_, (LD, fc[3], sh1, 2048));
  PRIO(1);
  T2(2, (LD, fa[3], sh1, 1024), NOP_, N_, N_, N_, NOP_, N_);
  PRIO(0);
  auto xchg = [&]() { PIN(o0); *(l4p)(xw) = (f32x4){o0[0], o0[1], o0[2], o0[3]}; *(l4p)(xw + 1024) = (f32x4){o0[4], o0[5], o0[6], o0[7]}; *(l1p)(xl) = o0[8]; };
  T2(3, N_, NOP_, N_, N_, N_, xchg(), N_);
#undef GM
#define GM(g, wf, hf_) do { g = MFMA(hf_, wf, g); PIN(g); SBAR(); } while (0)
  o1 = SMF(vb, pb, o1, idx);
  STAMP(9);
  xw = lxor(xw - L_PRIV, 8192) + L_PRIV; xl = lxor(xl, 1024); PIN(xw); PIN(xl);
  const int hs = (w + n64) & 3;
  lptr wl0 = lds + lwo_of(2 * hs) + l16, wl1 = lds + lwo_of(2 * hs + 1) + l16; PIN(wl0); PIN(wl1); SBAR();
  LBAR();
  float oo[9];
  {
    const f32x4 a = *(l4p)(xw), c = *(l4p)(xw + 1024); const float lp = *(l1p)(xl);
    SBAR(); asm volatile("" :: "v"(o1));
    oo[8] = o1[8] + lp;
    float sm = 0.f;
#pragma unroll
    for (int i = 0; i < 4; ++i) { oo[i] = o1[i] + a[i]; oo[4 + i] = o1[4 + i] + c[i]; }
#pragma unroll
    for (int i = 0; i < 8; ++i) sm += __builtin_fabsf(oo[i]);
    const bool ok = !FORCE_FB && __builtin_amdgcn_fmed3f(oo[8], 0x1p-104f, 0x1p120f) == oo[8] && __builtin_amdgcn_fmed3f(sm, 0x1p-106f, 0x1p127f) == sm;
    if (__builtin_expect(__builtin_amdgcn_ballot_w64(!ok) != 0, 0)) {
      const unsigned sk = WS_KV + ah * 131072, vv = l16 & 256 ? 0x7fffff00u : (l16 & 512) / 2 + (l16 & 240), one = (l16 & 432) == 256 ? 0x3f803f80u : 0u;
      float m = -INFINITY;
#pragma unroll 1
      for (int t = 0; t < 64; ++t) m = max16(m, MFMA(ld16c(ws, l16, sk + t * 1024), q1, z));
      m = -hmax(m);
#pragma unroll
      for (int i = 0; i < 16; ++i) nm0[i] = m;
      o0 = z;
#pragma unroll 1
      for (int t = 0; t < 64; ++t) {
        const f32x16 s_ = MFMA(ld16c(ws, l16, sk + t * 1024), q1, nm0);
        const u32x4 v0 = __builtin_bit_cast(u32x4, ld16c(ws, vv, sk + 65536 + t * 1024)) | one, v1 = __builtin_bit_cast(u32x4, ld16c(ws, vv, sk + 65536 + t * 1024 + 512)) | one;
#pragma unroll
        for (int i = 0; i < 8; ++i) { pa[i] = __builtin_amdgcn_exp2f(s_[i]); pb[i] = __builtin_amdgcn_exp2f(s_[8 + i]); }
        o0 = SMF(__builtin_bit_cast(bf16x8, v0), pa, o0, idx); o0 = SMF(__builtin_bit_cast(bf16x8, v1), pb, o0, idx);
      }
      float unit = 1.f; PIN(unit);
#pragma unroll
      for (int i = 0; i < 9; ++i) oo[i] = o0[i] * unit;
    }
  }
  { const float rl = __builtin_amdgcn_rcpf(oo[8]);
    u32x4 ov = {cvtpa(oo[0] * rl, oo[1] * rl), cvtpa(oo[2] * rl, oo[3] * rl), cvtpa(oo[4] * rl, oo[5] * rl), cvtpa(oo[6] * rl, oo[7] * rl)};
#pragma unroll
    for (int i = 0; i < 4; ++i) ov[i] = ov[i] < 0xfffffffeu ? ov[i] : 0xfffffffeu;
#pragma unroll
    for (int i = 0; i < 4; ++i) { PIN(fa[i]); PIN(fb[i]); PIN(fc[i]); }
    WAITV(0);
    st16(ov, ws, l16, WS_O + (qown * 32 + ah) * 1024); }
  WAITF(1);
  STAMP(45);
  asm volatile("s_barrier" ::: "memory");
  unsigned vout; u32x2 xr0, xr1; lptr rs;
  f32x4 ra, rc;
#define PUTG(g, o) asm volatile("ds_write_addtid_b32 %0 offset:%c16\n\tds_write_addtid_b32 %1 offset:%c16+256\n\tds_write_addtid_b32 %2 offset:%c16+512\n\tds_write_addtid_b32 %3 offset:%c16+768\n\t" \
      "ds_write_addtid_b32 %4 offset:%c16+1024\n\tds_write_addtid_b32 %5 offset:%c16+1280\n\tds_write_addtid_b32 %6 offset:%c16+1536\n\tds_write_addtid_b32 %7 offset:%c16+1792\n\t" \
      "ds_write_addtid_b32 %8 offset:%c16+2048\n\tds_write_addtid_b32 %9 offset:%c16+2304\n\tds_write_addtid_b32 %10 offset:%c16+2560\n\tds_write_addtid_b32 %11 offset:%c16+2816\n\t" \
      "ds_write_addtid_b32 %12 offset:%c16+3072\n\tds_write_addtid_b32 %13 offset:%c16+3328\n\tds_write_addtid_b32 %14 offset:%c16+3584\n\tds_write_addtid_b32 %15 offset:%c16+3840" \
      :: "v"(g[0]), "v"(g[1]), "v"(g[2]), "v"(g[3]), "v"(g[4]), "v"(g[5]), "v"(g[6]), "v"(g[7]), "v"(g[8]), "v"(g[9]), "v"(g[10]), "v"(g[11]), "v"(g[12]), "v"(g[13]), "v"(g[14]), "v"(g[15]), "i"(o) : "memory")
  f32x4 pa_[4], pc_[4];
#define RD4(p) do { _Pragma("unroll") for (int i = 0; i < 4; ++i) { pa_[i] = *(l4p)((p) + i * 8192); pc_[i] = *(l4p)((p) + i * 8192 + 512); } SBAR(); } while (0)
#define SUM4() do { _Pragma("unroll") for (int i = 0; i < 4; ++i) { ra += pa_[i]; rc += pc_[i]; PIN(ra); PIN(rc); } } while (0)
  {
    const unsigned soo = WS_O + ((2 * tb + kh) * 32 + 8 * hs) * 1024; unsigned spins; unsigned long long ret; u32x16 ofa, ofb;
#define OL0 "buffer_load_dwordx4 v[0:3], %[l], %[r], %[o0] offen sc1\n\t"
#define OL1 "buffer_load_dwordx4 v[4:7], %[l], %[r], %[o0] offen offset:1024 sc1\n\t"
#define OL2 "buffer_load_dwordx4 v[8:11], %[l], %[r], %[o0] offen offset:2048 sc1\n\t"
#define OL3 "buffer_load_dwordx4 v[12:15], %[l], %[r], %[o0] offen offset:3072 sc1\n\t"
#define OL4 "buffer_load_dwordx4 v[16:19], %[l], %[r], %[o1] offen sc1\n\t"
#define OL5 "buffer_load_dwordx4 v[20:23], %[l], %[r], %[o1] offen offset:1024 sc1\n\t"
#define OL6 "buffer_load_dwordx4 v[24:27], %[l], %[r], %[o1] offen offset:2048 sc1\n\t"
#define OL7 "buffer_load_dwordx4 v[28:31], %[l], %[r], %[o1] offen offset:3072 sc1\n\t"
#define OLOADS(br, c) asm volatile("s_cmp_lt_u32 %[w], 4\n\t" br " 1f\n\t" OL0 OL1 OL2 OL3 OL4 OL5 OL6 OL7 "1:" : c "{v[0:15]}"(ofa), c "{v[16:31]}"(ofb) \
      : [l] "v"(l16), [r] "s"(ws), [o0] "s"(soo), [o1] "s"(soo + 4096), [w] "s"(w) : "memory", "scc")
    bf16x8 wa_[8], wb_[8];
#define WRD(i) do { wa_[i] = LDS8(((i) < 4 ? wl0 : wl1) + ((i) & 3) * 1024); wb_[i] = LDS8(((i) < 4 ? wl0 : wl1) + ((i) & 3) * 1024 + 4096); SBAR(); } while (0)
    SBAR(); GM(g01, fb[0], fb[3]); WRD(0); WRD(1); GM(g11, fb[1], fb[3]); WRD(2); WRD(3); GM(g01, fa[0], fa[3]); WRD(4); GM(g11, fa[1], fa[3]); WRD(5); GM(g01, fc[0], fc[3]); WRD(6); GM(g11, fc[1], fc[3]); WRD(7);
    vout = (tb * 64 + w * 8) * 1024 + n64 * 128 + (l16 & 256) * 16 + (l16 & 512) * 2 + (l16 & 240) / 2;
    xr0 = __builtin_amdgcn_raw_buffer_load_b64(rx, vout, 0, 0); xr1 = __builtin_amdgcn_raw_buffer_load_b64(rx, vout + 2048, 0, 0);
    rs = lds + (kh ^ 1) * 32768 + (l16 & 128) * 32 + (w & 3) * 1024 + (l16 & 512) / 2 + (l16 & 256) / 2 + (l16 & 112); PIN(rs); SBAR();
    asm volatile("s_mov_b32 m0, %0\n\ts_nop 7\n\ts_waitcnt lgkmcnt(0)\n\ts_barrier" :: "s"(lds0 + w * 8192) : "memory");
    GM(g00, fb[0], fb[2]); PUTG(g01, 0); GM(g10, fb[1], fb[2]); GM(g00, fa[0], fa[2]); PUTG(g11, 4096); GM(g10, fa[1], fa[2]);
    OLOADS("s_cbranch_scc0", "=&");
    GM(g00, fc[0], fc[2]); GM(g10, fc[1], fc[2]);
    LBAR();
    OLOADS("s_cbranch_scc1", "+");
    RD4(rs);
#define OCHK(r0, r1, r2, r3) "v_max3_u32 v32, v" #r0 ", v" #r1 ", v" #r2 "\n\tv_max_u32 v32, v32, v" #r3 "\n\tv_cmp_eq_u32 vcc, -1, v32\n\t"
#define OT1(n, r0, r1, r2, r3) "s_waitcnt vmcnt(" #n ")\n\tv_max3_u32 v32, v" #r0 ", v" #r1 ", v" #r2 "\n\tv_max_u32 v32, v32, v" #r3 "\n\t"
#define OT2 "v_cmp_eq_u32 vcc, -1, v32\n\ts_getpc_b64 %[ret]\n\ts_cbranch_vccnz 3f\n\t"
#define OM2(a, b, c, d) "v_max3_u32 v32, v32, v" #a ", v" #b "\n\tv_max3_u32 v33, v33, v" #c ", v" #d "\n\t"
#define OMA(i, r0, r3) "v_mfma_f32_32x32x16_bf16 %[g0], v[" #r0 ":" #r3 "], %[a" #i "], %[g0]\n\t"
#define OMB(i, r0, r3) "v_mfma_f32_32x32x16_bf16 %[g1], v[" #r0 ":" #r3 "], %[b" #i "], %[g1]\n\t"
#define ORE(r0, r1, r2, r3, load) OCHK(r0, r1, r2, r3) "s_cbranch_vccz 4f\n\t" load "4:\n\t"
    static_assert(SPIN_CAP == 0x8000, "literal below");
    asm volatile("s_mov_b32 %[sp], 0\n\t"
                 OT1(7, 0, 1, 2, 3) OT2 OMA(0, 0, 3) OT1(6, 4, 5, 6, 7) OMB(0, 0, 3) OT2 OMA(1, 4, 7) OT1(5, 8, 9, 10, 11) OMB(1, 4, 7) OT2 OMA(2, 8, 11) OT1(4, 12, 13, 14, 15) OMB(2, 8, 11) OT2
                 OMA(3, 12, 15) OT1(3, 16, 17, 18, 19) OMB(3, 12, 15) OT2 OMA(4, 16, 19) OT1(2, 20, 21, 22, 23) OMB(4, 16, 19) OT2 OMA(5, 20, 23) OT1(1, 24, 25, 26, 27) OMB(5, 20, 23) OT2
                 OMA(6, 24, 27) OT1(0, 28, 29, 30, 31) OMB(6, 24, 27) OT2 OMA(7, 28, 31) OMB(7, 28, 31)
                 "s_branch 2f\n"
                 "3:\n\ts_add_u32 %[sp], %[sp], 1\n\ts_cmp_gt_u32 %[sp], 0x8000\n\ts_cbranch_scc1 2f\n\ts_waitcnt vmcnt(0)\n\t"
                 ORE(0, 1, 2, 3, OL0) ORE(4, 5, 6, 7, OL1) ORE(8, 9, 10, 11, OL2) ORE(12, 13, 14, 15, OL3)
                 ORE(16, 17, 18, 19, OL4) ORE(20, 21, 22, 23, OL5) ORE(24, 25, 26, 27, OL6) ORE(28, 29, 30, 31, OL7)
                 "s_waitcnt vmcnt(0)\n\tv_max3_u32 v32, v0, v1, v2\n\tv_max3_u32 v33, v3, v4, v5\n\t" OM2(6, 7, 8, 9) OM2(10, 11, 12, 13) OM2(14, 15, 16, 17) OM2(18, 19, 20, 21)
                 OM2(22, 23, 24, 25) OM2(26, 27, 28, 29) "v_max3_u32 v32, v32, v30, v31\n\tv_max_u32 v32, v32, v33\n\tv_cmp_eq_u32 vcc, -1, v32\n\ts_setpc_b64 %[ret]\n"
                 "2:\n\ts_waitcnt vmcnt(0)\n\ts_nop 11"
                 : [g0] "+v"(g00), [g1] "+v"(g10), "+{v[0:15]}"(ofa), "+{v[16:31]}"(ofb), [sp] "=&s"(spins), [ret] "=&s"(ret)
                 : [l] "v"(l16), [r] "s"(ws), [o0] "s"(soo), [o1] "s"(soo + 4096),
                   [a0] "v"(wa_[0]), [a1] "v"(wa_[1]), [a2] "v"(wa_[2]), [a3] "v"(wa_[3]), [a4] "v"(wa_[4]), [a5] "v"(wa_[5]), [a6] "v"(wa_[6]), [a7] "v"(wa_[7]),
                   [b0] "v"(wb_[0]), [b1] "v"(wb_[1]), [b2] "v"(wb_[2]), [b3] "v"(wb_[3]), [b4] "v"(wb_[4]), [b5] "v"(wb_[5]), [b6] "v"(wb_[6]), [b7] "v"(wb_[7])
                 : "memory", "scc", "vcc", "v32", "v33");
  }
  STAMP(12);
#pragma unroll
  for (int i = 0; i < 4; ++i) { PIN(pa_[i]); PIN(pc_[i]); }
  asm volatile("s_mov_b32 m0, %0\n\ts_nop 0" :: "s"(lds0 + w * 8192 + 8188) : "memory");
  PUTG(g00, 65536 - 8188); PUTG(g10, 65536 - 8188 + 4096);
  ra = (f32x4){bflo(xr0[0]), bfhi(xr0[0]), bflo(xr0[1]), bfhi(xr0[1])}; rc = (f32x4){bflo(xr1[0]), bfhi(xr1[0]), bflo(xr1[1]), bfhi(xr1[1])}; PIN(ra); PIN(rc);
  SUM4();
  rs = lxor(rs, 98304); PIN(rs);
  LBAR();
  STAMP(13);
  RD4(rs); SUM4();
  { const rsrc_t ro = mk(pout, 2048 * 1024);
    __builtin_amdgcn_raw_buffer_store_b64((u32x2){cvtpk(ra[0], ra[1]), cvtpk(ra[2], ra[3])}, ro, vout, 0, 0);
    __builtin_amdgcn_raw_buffer_store_b64((u32x2){cvtpk(rc[0], rc[1]), cvtpk(rc[2], rc[3])}, ro, vout + 2048, 0, 0); }
  STAMP(14);
  STAMP_END();
  asm volatile(".Lfb_end:");
}

extern "C" void kernel_launch(void* const* d_in, const int* in_sizes, int n_in, void* d_out, int out_size, void* d_ws, size_t ws_size, hipStream_t stream) {
  (void)hipFuncSetAttribute((const void*)fused_block, hipFuncAttributeMaxDynamicSharedMemorySize, LDS_BYTES);
  hipLaunchKernelGGL(fused_block, dim3(256), dim3(512), LDS_BYTES, stream, d_in[0], d_in[1], d_in[2], d_in[4], d_in[5], d_in[6], d_out, d_ws);
}
```

```cpp
#include <hip/hip_runtime.h>
#include <hip/hip_bf16.h>

typedef __bf16 bf16x8 __attribute__((ext_vector_type(8)));
typedef __bf16 bf16x16 __attribute__((ext_vector_type(16)));
typedef __bf16 bf16x2 __attribute__((ext_vector_type(2)));
typedef short s16x4 __attribute__((ext_vector_type(4)));
typedef float f32x16 __attribute__((ext_vector_type(16)));
typedef float f32x8 __attribute__((ext_vector_type(8)));
typedef float f32x4 __attribute__((ext_vector_type(4)));
typedef float f32x2 __attribute__((ext_vector_type(2)));
typedef unsigned u32x4 __attribute__((ext_vector_type(4)));
typedef unsigned u32x2 __attribute__((ext_vector_type(2)));
typedef unsigned u32x16 __attribute__((ext_vector_type(16)));
typedef __amdgpu_buffer_rsrc_t rsrc_t;
typedef __attribute__((address_space(3))) char* lptr;
#define DI __device__ __forceinline__
#define SBAR() __builtin_amdgcn_sched_barrier(0)
#define MFMA(a, b, c) __builtin_amdgcn_mfma_f32_32x32x16_bf16(a, b, c, 0, 0, 0)
#define SMF(a, b, c, idx) __builtin_amdgcn_smfmac_f32_32x32x32_bf16(a, __builtin_bit_cast(bf16x16, b), c, idx, 0, 0)
#define LDS8(p) (*(const __attribute__((address_space(3))) bf16x8*)(p))
#define PIN(x) asm volatile("" : "+v"(x))
#define LANE(n) unsigned n = l16; PIN(n); n >>= 4

constexpr unsigned WS_EPOCH = 0;
constexpr unsigned WS_STAT = 57344;
constexpr unsigned WS_CS = WS_STAT + 8192;
constexpr unsigned FS = 128;
constexpr unsigned WS_F0 = WS_CS + 163840, WS_F1 = WS_F0 + 256 * FS, WS_F2 = WS_F1 + 256 * FS;
constexpr unsigned WS_F3 = WS_F2 + 256 * FS;
constexpr unsigned WS_Q = 1u << 20;
constexpr unsigned WS_KV = WS_Q + (2u << 20);
constexpr unsigned WS_O = WS_KV + (4u << 20);
constexpr unsigned WS_W2 = WS_O + (2u << 20);
constexpr unsigned WS_H = WS_W2 + (3u << 20);
constexpr unsigned WS_END = WS_H + (8u << 20);
constexpr int U_XA = 0, U_G1 = 24576, U_XB = 49152, U_G2 = 73728, U_G0 = 98304, UD = 49152;
constexpr int L_V = 73728, L_PRIV = 139264, L_ONES = 155648, L_EYE = L_ONES + 5120, LDS_BYTES = 163840;
constexpr unsigned SPIN_CAP = 1u << 15;
constexpr float REF_OFF = 100.f;
constexpr int K_F = 12, K_P = 19, K_C = 25;

DI rsrc_t mk(const void* p, unsigned bytes) { return __builtin_amdgcn_make_buffer_rsrc((void*)p, (short)0, (int)bytes, 0x00020000); }
DI unsigned cvtpk(float lo, float hi) { f32x2 v = {lo, hi}; return __builtin_bit_cast(unsigned, __builtin_convertvector(v, bf16x2)); }
DI unsigned cvtpa(float lo, float hi) { unsigned r; asm("v_cvt_pk_bf16_f32 %0, %1, %2" : "=v"(r) : "v"(lo), "v"(hi)); return r; }
DI float bflo(unsigned u) { return __uint_as_float(u << 16); }
DI float bfhi(unsigned u) { return __uint_as_float(u & 0xffff0000u); }
DI float rbf(float f) { return bflo(cvtpk(f, f)); }
DI float max3(float a, float b, float c) { return __builtin_fmaxf(__builtin_fmaxf(a, b), c); }
DI lptr lxor(lptr p, unsigned m) { return (lptr)((size_t)p ^ m); }
DI s16x4 trd(lptr p) { return __builtin_amdgcn_ds_read_tr16_b64_v4i16((__attribute__((address_space(3))) s16x4*)p); }
DI bf16x8 trfrag(lptr p, int step) { s16x4 a = trd(p), b = trd(p + step); return __builtin_bit_cast(bf16x8, __builtin_shufflevector(a, b, 0, 1, 2, 3, 4, 5, 6, 7)); }
#define dma(base, imm, voff, r, soff) asm volatile("s_add_i32 m0, %0, %4\n\ts_nop 0\n\tbuffer_load_dwordx4 %1, %2, %3 offen lds" :: "s"(base), "v"(voff), "s"(r), "s"(soff), "i"(imm) : "memory", "scc")
#define dma_go(voff, r, soff) asm volatile("buffer_load_dwordx4 %0, %1, %2 offen lds" :: "v"(voff), "s"(r), "s"(soff) : "memory")
#define dma_nt(base, imm, voff, r, soff) asm volatile("s_add_i32 m0, %0, %4\n\ts_nop 0\n\tbuffer_load_dwordx4 %1, %2, %3 offen nt lds" :: "s"(base), "v"(voff), "s"(r), "s"(soff), "i"(imm) : "memory", "scc")
#define ALD4(dst, voff, r, soff) asm volatile("buffer_load_dwordx4 %0, %1, %2, %3 offen" : "=v"(dst) : "v"(voff), "s"(r), "s"(soff) : "memory")
#define ALD4C(dst, voff, r, soff) asm volatile("buffer_load_dwordx4 %0, %1, %2, %3 offen sc1" : "=v"(dst) : "v"(voff), "s"(r), "s"(soff) : "memory")
#define ALD4O(dst, soff, imm) asm volatile("buffer_load_dwordx4 %0, %1, %2, %3 offen offset:%4 sc1" : "=v"(dst) : "v"(l16), "s"(ws), "s"(soff), "i"(imm) : "memory")
#define ALD1C(dst, voff, r, soff) asm volatile("buffer_load_dword %0, %1, %2, %3 offen sc1" : "=v"(dst) : "v"(voff), "s"(r), "s"(soff) : "memory")
#define CLD(op, dst, voff, r, soff, lim) asm volatile("s_cmp_ge_u32 %4, %5\n\ts_cbranch_scc1 1f\n\t" op "\n1:" : "=v"(dst) : "v"(voff), "s"(r), "s"(soff), "s"(w), "i"(lim) : "memory", "scc")
#define POLL(voff) CLD("buffer_load_dwordx2 %0, %1, %2, %3 offen sc1", pl, voff, ws, 0, 1)
#define WAITV(n) asm volatile("s_waitcnt vmcnt(%0)" :: "i"(n) : "memory")
#define WAITBAR(n) asm volatile("s_waitcnt vmcnt(%0) lgkmcnt(0)\n\ts_barrier" :: "i"(n) : "memory")
#define LBAR() asm volatile("s_waitcnt lgkmcnt(0)\n\ts_barrier" ::: "memory")
DI void st16(u32x4 v, rsrc_t r, unsigned voff, unsigned soff) {
  asm volatile("buffer_store_dwordx4 %0, %1, %2, %3 offen sc1\n\ts_nop 1" :: "v"(v), "v"(voff), "s"(r), "s"(soff) : "memory"); }
DI void st4(unsigned v, rsrc_t r, unsigned voff, unsigned soff) { __builtin_amdgcn_raw_buffer_store_b32(v, r, voff, soff, 16); }
DI bf16x8 ld16c(rsrc_t r, unsigned voff, unsigned soff) { return __builtin_bit_cast(bf16x8, __builtin_amdgcn_raw_buffer_load_b128(r, voff, soff, 16)); }
template <int CTRL> DI float dppadd(float v) { return v + __int_as_float(__builtin_amdgcn_update_dpp(0, __float_as_int(v), CTRL, 0xf, 0xf, true)); }
#define WAITF(n) do { unsigned sp_; asm volatile("s_cmp_lg_u32 %[w], 0\n\ts_cbranch_scc1 9f\n\ts_mov_b32 %[sp], 0\n\ts_waitcnt vmcnt(%[c])\n" \
    "1:\n\tv_cmp_ne_u64 vcc, %[t], %[p]\n\ts_cbranch_vccz 9f\n\ts_add_u32 %[sp], %[sp], 1\n\ts_cmp_gt_u32 %[sp], %[cap]\n\ts_cbranch_scc1 9f\n\ts_sleep 2\n\t" \
    "buffer_load_dwordx2 %[p], %[v], %[r], 0 offen sc1\n\ts_waitcnt vmcnt(0)\n\ts_branch 1b\n9:" \
    : [p] "+v"(pl), [sp] "=&s"(sp_) : [v] "v"(vf0), [r] "s"(ws), [t] "s"(tag), [w] "s"(w), [c] "i"(n), [cap] "i"(SPIN_CAP) : "memory", "scc", "vcc"); } while (0)
#define PUT(val, soff, who) do { unsigned long long t_; asm volatile("s_cmp_lg_u32 %4, 0\n\ts_cbranch_scc1 9f\n\tv_mov_b64 %0, %1\n\ts_mov_b64 exec, 1\n\t" \
    "buffer_store_dwordx2 %0, off, %2, %3 sc1\n\ts_mov_b64 exec, -1\n9:" : "=&v"(t_) : "s"(val), "s"(ws), "s"(soff), "s"(who) : "memory", "scc"); } while (0)
#define WAITV2(n0, n) asm volatile("s_waitcnt vmcnt(%1)\n\ts_cmp_eq_u32 %0, 0\n\ts_cbranch_scc1 9f\n\ts_waitcnt vmcnt(%2)\n9:" :: "s"(w), "i"(n0), "i"(n) : "memory", "scc")

DI u32x4 pack8(const f32x16& a, int o, float s) {
  return (u32x4){cvtpa(a[o] * s, a[o + 1] * s), cvtpa(a[o + 2] * s, a[o + 3] * s), cvtpa(a[o + 4] * s, a[o + 5] * s), cvtpa(a[o + 6] * s, a[o + 7] * s)}; }
DI u32x4 glu8(const f32x16& a, float nc, float sd) {
  float h[8];
#pragma unroll
  for (int i = 0; i < 8; ++i) { const float e = __builtin_amdgcn_exp2f(a[i] * nc); h[i] = a[8 + i] * __builtin_amdgcn_rcpf(__builtin_fmaf(e, sd, sd)); }
  return (u32x4){cvtpa(h[0], h[1]), cvtpa(h[2], h[3]), cvtpa(h[4], h[5]), cvtpa(h[6], h[7])}; }

typedef unsigned long long u64x8 __attribute__((ext_vector_type(8)));
__global__ __launch_bounds__(512, 2) void fused_block(const void* a_x, const void* a_wqkv, const void* a_wo, const void* a_wg, const void* a_wu, const void* a_wd, void* a_out, void* a_ws) {
  extern __shared__ __attribute__((aligned(16))) char smem[];
  u64x8 arg;
  asm volatile("s_load_dwordx16 %0, %1, 0x0" : "=s"(arg) : "s"(__builtin_amdgcn_kernarg_segment_ptr()));
  const lptr lds = (lptr)smem;
  const unsigned lds0 = (unsigned)(size_t)lds;
  const int tid = threadIdx.x, lane = tid & 63, w = __builtin_amdgcn_readfirstlane(tid >> 6);
  const int b = blockIdx.x, xcd = b & 7, bi = b >> 3;
  const int r32 = lane & 31, hi = lane >> 5;
  if (w == 0) {
    unsigned long long pc; unsigned len; asm volatile("s_getpc_b64 %0\n.Lfb_pc:\n\ts_mov_b32 %1, .Lfb_end-.Lfb_pc" : "=s"(pc), "=s"(len));
    const rsrc_t rc = mk((const void*)pc, len);
    const unsigned v = lane < 8 ? (bi * 8 + lane) * 128 : 0x7fffff00u;
    asm volatile("s_mov_b32 m0, %0\n\ts_nop 0\n\tbuffer_load_dword %1, %2, 0 offen lds" :: "s"(lds0 + LDS_BYTES - 256), "v"(v), "s"(rc) : "memory"); }
  const int rb = (xcd & 1) * 8 + (bi & 7), Q4 = xcd >> 1, sj = bi >> 3, set = Q4 * 4 + sj;
  const int rot = 2 * (xcd & 1) + (bi & 4), pos = (bi >> 1) & 1, half = bi & 1, kpart = rot + pos;
  const unsigned l3 = lane >> 3, l7 = lane & 7, l16 = lane * 16;
  const int wx = (w + 2 * sj) & 7, ww = (w + 2 * bi) & 7;
  unsigned vx0 = (rb * 128 + wx * 16 + l3) * 1024 + ((l7 ^ (lane >> 4)) * 16), vx1 = (vx0 + 8192) ^ 64;
  unsigned mx = lds0 + (wx >> 2) * UD + (wx & 3) * 2048, mw = lds0 + ww * 1024;
  const bool tb_ = sj == 0;
  const int p1 = 7 * Q4 + 2 * sj - 1, p2 = p1 + 1;
  const unsigned col0 = 128 * Q4 + (tb_ ? 1024 : sj * 512 - 448);
  const unsigned str12 = tb_ ? 3072 : 3584, wr = ww * 8 + l3, wpc = (l7 ^ ((lane >> 2) & 4)) * 16;
  unsigned vg0 = wr * 3072 + wpc + col0 * 2, vg1 = wr * str12 + wpc + (tb_ ? 1024 + 256 * Q4 : p1 * 128), vg2 = (vg1 - (tb_ ? 1024 : 0)) ^ 32;
  unsigned so0 = rot * 128, so1 = rot * (64 * 3072), so2 = rot * 64 * str12;
  PIN(vx0); PIN(vx1); PIN(vg0); PIN(vg1); PIN(vg2); asm volatile("" : "+s"(mx), "+s"(mw), "+s"(so0), "+s"(so1), "+s"(so2));
  SBAR();
  asm volatile("s_waitcnt lgkmcnt(0)" : "+s"(arg) :: "memory");
  const void *px = (const void*)arg[0], *pwqkv = (const void*)arg[1], *pwo = (const void*)arg[2], *pwg = (const void*)arg[3], *pwu = (const void*)arg[4], *pwd = (const void*)arg[5];
  void *pout = (void*)arg[6], *pws = (void*)arg[7];
  rsrc_t ws = mk(pws, WS_END), rx = mk(px, 2048 * 1024), rg0 = mk(pwqkv, 512 * 3072);
  rsrc_t rg1 = mk(tb_ ? pwqkv : pwg, 512 * str12), rg2 = mk(tb_ ? pwqkv : pwu, 512 * str12);
  rsrc_t rg3 = mk(pwg, 512 * 3584), rg4 = mk(pwu, 512 * 3584);
#define KC(c) (unsigned)(((c) + rot) & 7)
#define SL(c) (((c) % 3) * 8192)
#define DMA1(c, i) do { \
    if ((i) == 0) dma(mx, SL(c), vx0, rx, KC(c) * 128); if ((i) == 1) dma(mx, SL(c) + 1024, vx1, rx, KC(c) * 128); \
    if ((i) == 2) dma(mw, SL(c) + U_G0, vg0, rg0, KC(c) * (64 * 3072)); if ((i) == 3) dma(mw, SL(c) + U_G1, vg1, rg1, KC(c) * 64 * str12); \
    if ((i) == 4) dma(mw, SL(c) + U_G2, vg2, rg2, KC(c) * 64 * str12); } while (0)
  unsigned sd_;
#define M0S(base, imm, off) do { SBAR(); sd_ = (off); asm volatile("s_add_i32 m0, %1, %2" :: "s"(sd_), "s"(base), "i"(imm) : "memory", "scc"); } while (0)
#define M01(c, i) M0S((i) < 2 ? mx : mw, SL(c) + ((i) == 0 ? 0 : (i) == 1 ? 1024 : (i) == 2 ? U_G0 : (i) == 3 ? U_G1 : U_G2), KC(c) * ((i) < 2 ? 128 : (i) == 2 ? 64 * 3072 : 64 * str12))
#define GO1(c, i) do { if ((i) == 0) dma_go(vx0, rx, sd_); if ((i) == 1) dma_go(vx1, rx, sd_); if ((i) == 2) dma_go(vg0, rg0, sd_); if ((i) == 3) dma_go(vg1, rg1, sd_); if ((i) == 4) dma_go(vg2, rg2, sd_); } while (0)
#define M02(c, i) M0S(mw, SL(c) + ((i) & 2 ? U_XA : U_G1) + ((i) & 1) * UD, KC(2 * ((c) - 8) + ((i) >> 1)) * (64 * 3584))
#define GO2(c, i) do { if ((i) & 1) dma_go(vg4, rg4, sd_); else dma_go(vg3, rg3, sd_); } while (0)
  dma(mx, 0, vx0, rx, so0); dma(mx, 1024, vx1, rx, so0); SBAR(); dma(mw, U_G0, vg0, rg0, so1); SBAR(); dma(mw, U_G1, vg1, rg1, so2); dma(mw, U_G2, vg2, rg2, so2); SBAR();
  asm volatile("" : "+s"(ws), "+s"(rx), "+s"(rg0), "+s"(rg1), "+s"(rg2), "+s"(rg3), "+s"(rg4));
  unsigned vg3 = wr * 3584 + wpc + p2 * 128, vg4 = vg3 ^ 32; PIN(vg3); PIN(vg4);
  const int tt = w & 3, cg = w >> 2;
  const int q4 = (lane & 15) >> 2, p4 = lane & 3, g16 = (lane >> 4) & 1, swr = (q4 >> 1) * 4;
  const int wrow = (8 * hi + q4) * 128 + (p4 & 1) * 8;
  const int pq = 4 * cg + 2 * g16 + (p4 >> 1);
  const int gl0 = g16 * UD + (pq ^ swr) * 16, gl1 = gl0 ^ 32;
  lptr wp0 = lds + U_G0 + wrow + (pq ^ swr) * 16;
  lptr wp1 = lds + wrow + (tb_ ? (pq ^ swr) * 16 : gl0);
  lptr wp2 = lds + wrow + (tb_ ? UD + (pq ^ swr ^ 2) * 16 : gl1);
  lptr xp[4];
#pragma unroll
  for (int s = 0; s < 4; ++s) { xp[s] = lds + (tt >> 1) * UD + (tt & 1) * 4096 + r32 * 128 + (((2 * s + hi) ^ ((r32 >> 1) & 7)) * 16); PIN(xp[s]); }
  PIN(wp0); PIN(wp1); PIN(wp2);
  const int ah = xcd * 4 + (bi & 3), aq = bi >> 2, kh = w >> 2;
  const int qown = aq * 8 + 2 * (w & 3) + kh;
  const unsigned csb = WS_CS + set * 10240;
  auto colsum = [&](lptr cp, unsigned so) {
    const __bf16 o1 = (__bf16)1.0f; const bf16x8 ones = {o1, o1, o1, o1, o1, o1, o1, o1};
    const bf16x8 f0 = trfrag(cp, 512), f1 = trfrag(cp + 2048, 512), f2 = trfrag(cp + 4096, 512), f3 = trfrag(cp + 6144, 512);
    const f32x16 cs = MFMA(ones, f3, MFMA(ones, f2, MFMA(ones, f1, MFMA(ones, f0, (f32x16){}))));
    const float ch = rbf(cs[0]);
    st4(cvtpk(ch, cs[0] - ch), ws, l16 < 512 ? l16 : 0x7fffff00u, csb + so + (kpart >> 2) * 512 + (kpart & 3) * 4);
  };
  unsigned vf0 = 0, scs, sst;
  u32x2 ep; u32x4 xr; unsigned e0, e1; unsigned long long tag;
  const unsigned srow = rb * 128 + set * 8 + w;
  float sv[8], sa, sb, smean;
  auto swapadd16 = [](float v) { auto r = __builtin_amdgcn_permlane16_swap(__float_as_uint(v), __float_as_uint(v), false, false); return __uint_as_float(r[0]) + __uint_as_float(r[1]); };
  auto swapadd32 = [](float v) { auto r = __builtin_amdgcn_permlane32_swap(__float_as_uint(v), __float_as_uint(v), false, false); return __uint_as_float(r[0]) + __uint_as_float(r[1]); };
#define STATS(i) do { \
    if ((i) == 0 || (i) == 1) { sv[4 * ((i) & 1)] = bflo(xr[2 * ((i) & 1)]); sv[4 * ((i) & 1) + 1] = bfhi(xr[2 * ((i) & 1)]); sv[4 * ((i) & 1) + 2] = bflo(xr[2 * ((i) & 1) + 1]); sv[4 * ((i) & 1) + 3] = bfhi(xr[2 * ((i) & 1) + 1]); \
      PIN(sv[4 * ((i) & 1)]); PIN(sv[4 * ((i) & 1) + 1]); PIN(sv[4 * ((i) & 1) + 2]); PIN(sv[4 * ((i) & 1) + 3]); } \
    if ((i) == 2) { sa = (sv[0] + sv[1]) + sv[2]; PIN(sa); sb = (sv[3] + sv[4]) + sv[5]; PIN(sb); } \
    if ((i) == 3) { sa = ((sa + sv[6]) + sv[7]) + sb; PIN(sa); } \
    if ((i) == 4 || (i) == 14) { sa = dppadd<0x121>(sa); PIN(sa); } if ((i) == 5 || (i) == 15) { sa = dppadd<0x122>(sa); PIN(sa); } \
    if ((i) == 6 || (i) == 16) { sa = dppadd<0x124>(sa); PIN(sa); } if ((i) == 7 || (i) == 17) { sa = dppadd<0x128>(sa); PIN(sa); } \
    if ((i) == 8 || (i) == 18) { sa = swapadd16(sa); PIN(sa); } \
    if ((i) == 9) { smean = swapadd32(sa) * (1.f / 512); PIN(smean); sa = 0.f; } \
    if ((i) >= 10 && (i) <= 13) { const float d_ = sv[(2 * (i) - 20) & 7] - smean, e_ = sv[(2 * (i) - 19) & 7] - smean; sa = __builtin_fmaf(e_, e_, __builtin_fmaf(d_, d_, sa)); PIN(sa); } \
    if ((i) == 19) { sa = rbf(swapadd32(sa) * (1.f / 512)); PIN(sa); } \
    if ((i) == 20) { sa = __builtin_amdgcn_sqrtf(rbf(sa + 1e-5f)); PIN(sa); } \
    if ((i) == 21) st4(cvtpk(-smean, sa), ws, lane ? 0x7fffff00u : 0u, WS_STAT + srow * 4); } while (0)
  bf16x8 xf[32], wa[3], wb[3];
  f32x16 acc0 = {}, acc1 = {}, acc2 = {};
  u32x2 pl; unsigned stat; u32x4 cs0, cs1, cs2;
#define RD1(W, ks) do { xf[ks] = LDS8(xp[(ks) & 3] + SL((ks) >> 2)); W[0] = trfrag(wp0 + SL((ks) >> 2) + ((ks) & 3) * 2048, 512); } while (0)
#define RD1B(W, ks) W[1] = trfrag(wp1 + U_G1 + SL((ks) >> 2) + ((ks) & 3) * 2048, 512)
#define RD1C(W, ks) W[2] = trfrag(wp2 + U_G1 + SL((ks) >> 2) + ((ks) & 3) * 2048, 512)
#define RD2A(W, ks) W[0] = trfrag(wp1 + SL(8 + ((ks) >> 3)) + ((ks) & 4 ? U_XA : U_G1) + ((ks) & 3) * 2048, 512)
#define RD2B(W, ks) W[1] = trfrag(wp2 + SL(8 + ((ks) >> 3)) + ((ks) & 4 ? U_XA : U_G1) + ((ks) & 3) * 2048, 512)
  const int n2 = rb >> 1, ks2 = (rb & 1) * 16 + set + 32 * ((w + 7) & 7);
  const rsrc_t rw2 = mk(ks2 < 112 ? pwd : pwo, ks2 < 112 ? 1792 * 1024 : 512 * 1024);
  const unsigned vw2 = (ks2 < 112 ? ks2 : ks2 - 112) * 16384 + n2 * 128 + l3 * 1024 + wpc;
#define W2LOAD() do { if (ks2 < 144) { dma_nt(lds0 + w * 2048, L_PRIV, vw2, rw2, 0); dma_nt(lds0 + w * 2048, L_PRIV + 1024, vw2, rw2, 8192); } } while (0)
#define FLAG(f) PUT(tag, (f) + (set * 16 + rb) * FS, w)
  if (w < 5) { const unsigned o_ = (tid & 11) == 0 ? 0x3f803f80u : 0u;
    *(__attribute__((address_space(3))) u32x4*)(lds + L_ONES + tid * 16) = (u32x4){o_, o_, o_, o_}; }
  else if (w < 7) {
    const unsigned one_ = hi == ((lane >> 2) & 1) ? 0x3f80u << ((lane & 1) * 16) : 0u, dw_ = ((lane >> 1) & 1) + ((lane >> 2) & 6) - 4 * (w - 5);
    *(__attribute__((address_space(3))) u32x4*)(lds + L_ONES + tid * 16) = (u32x4){dw_ == 0 ? one_ : 0u, dw_ == 1 ? one_ : 0u, dw_ == 2 ? one_ : 0u, dw_ == 3 ? one_ : 0u}; }
  unsigned vep = lane == 0 || lane > 10 ? WS_EPOCH : WS_STAT - 128 + b * 1280 + l16 * 8; PIN(vep);
  SBAR();
  asm volatile("s_barrier" ::: "memory");
  DMA1(1, 0); DMA1(1, 1); DMA1(1, 2); DMA1(1, 3); DMA1(1, 4);
  WAITBAR(5);
  RD1(wa, 0); RD1B(wa, 0); RD1C(wa, 0);
#define STATG(g) do { STATS(2 * ((g) - 12)); STATS(2 * ((g) - 12) + 1); } while (0)
#define STEP1(ks, WC, WN) do { constexpr int c_ = ((ks) + 1) >> 2, s_ = ((ks) + 1) & 3; \
    if (s_ == 0) { SBAR(); \
      if ((c_ == 1 || c_ == 2) && w < 3 && pos == c_ - 1) \
        colsum(lxor(w == 0 ? wp0 : w == 1 ? wp1 + U_G1 : wp2 + U_G1, half * 64) + SL(c_ - 1), (half * 5 + w) * 1024); \
      if (c_ == 1) CLD("buffer_load_dwordx2 %0, %1, %2, %3 offen sc1", ep, vep, ws, 0, 1); \
      if (c_ == 3 && w == 0) { const unsigned v_ = l16 * 224 + p2 * 128;     \
        asm volatile("s_mov_b32 m0, %0\n\ts_nop 0\n\tbuffer_load_dword %1, %2, %3 offen lds" :: "s"(lds0 + LDS_BYTES - 256), "v"(v_), "s"(half ? rg4 : rg3), "s"(kpart * (64 * 3584)) : "memory"); } \
      if (c_ == 5) W2LOAD(); \
      if (c_ == 6 && w == 0) { LANE(n_); vf0 = WS_F0 + (n_ < 16 ? set * 16 + n_ : (n_ & 15) * 16 + rb) * FS; PIN(vf0); } \
      if (c_ == 7) WAITF(4); \
      if (c_ == 5 && ks2 < 144) WAITV(7); else WAITV(c_ == 1 ? 6 : c_ < 7 ? 5 : c_ == 7 ? 4 : 8); LBAR(); \
      if (c_ == 2) { PIN(ep); PIN(xr); e0 = __builtin_amdgcn_readfirstlane(ep[0]); e1 = __builtin_amdgcn_readfirstlane(ep[1]);     \
        tag = (unsigned long long)((e1 ^ 0x85EBCA6Bu) + e0) << 32 | (e0 * 0x9E3779B1u + 0x7F4A7C15u); }     \
      if (c_ == 4) FLAG(WS_F0); \
      if (c_ == 6) POLL(vf0); \
      if (c_ == 8) { wp1 = wp0 - U_G0 + (l16 & 256) * (UD / 256); wp2 = lxor(wp1, 32); PIN(wp1); PIN(wp2); } } \
    SBAR(); \
    if ((ks) < 31) RD1(WN, ((ks) + 1) & 31); else RD2A(WN, 0); \
    if (c_ <= 5) M01(c_ + 2, s_); else M02(c_ + 2, s_); STATG(3 * ((ks) - 3) - 1); SBAR(); \
    acc0 = MFMA(WC[0], xf[ks], acc0); SBAR(); \
    if (c_ <= 5) GO1(c_ + 2, s_); else GO2(c_ + 2, s_); \
    if ((ks) < 31) RD1B(WN, ((ks) + 1) & 31); else RD2B(WN, 0); \
    if (c_ >= 1 && c_ <= 5 && s_ == 0) M01(c_ + 2, 4); if ((ks) < 2) M01(2, (ks) * 4); if (c_ == 8) M02(10, 1); \
    if ((ks) == 27) { vf0 = (l16 & 496) >> 2; PIN(vf0); scs = csb + cg * 5120; sst = WS_STAT + (rb * 4 + tt) * 128; asm volatile("" :: "s"(scs), "s"(sst)); } SBAR(); STATG(3 * ((ks) - 3)); SBAR(); \
    acc1 = MFMA(WC[1], xf[ks], acc1); SBAR(); \
    if (c_ >= 1 && c_ <= 5 && s_ == 0) GO1(c_ + 2, 4); if ((ks) < 2) GO1(2, (ks) * 4); if (c_ == 8) { GO2(10, 1); M02(10, 2); } \
    if ((ks) == 27) ALD1C(stat, vf0, ws, sst); if ((ks) == 28) ALD4O(cs1, scs, 1024); \
    if ((ks) < 31) RD1C(WN, ((ks) + 1) & 31); SBAR(); STATG(3 * ((ks) - 3) + 1); SBAR(); \
    acc2 = MFMA(WC[2], xf[ks], acc2); SBAR(); if (c_ == 8) GO2(10, 2); \
    if ((ks) == 2) ALD4(xr, l16, rx, srow * 1024); if ((ks) == 27) ALD4O(cs0, scs, 0); if ((ks) == 28) ALD4O(cs2, scs, 2048); SBAR(); } while (0)
#define STEP1x4(k) STEP1(k, wa, wb); STEP1(k + 1, wb, wa); STEP1(k + 2, wa, wb); STEP1(k + 3, wb, wa)
  STEP1x4(0); STEP1x4(4); STEP1x4(8); STEP1x4(12); STEP1x4(16); STEP1x4(20); STEP1x4(24); STEP1x4(28);
  M02(10, 3);
  asm volatile("s_waitcnt vmcnt(5)" : "+v"(stat), "+v"(cs0), "+v"(cs1), "+v"(cs2) :: "memory");
  unsigned nm2 = (stat & 0xffffu) * 0x10001u;
  bf16x8 nmf = __builtin_bit_cast(bf16x8, (u32x4){nm2, nm2, nm2, nm2});
  PIN(nmf); SBAR(); acc0 = MFMA(__builtin_bit_cast(bf16x8, cs0), nmf, acc0); SBAR(); GO2(10, 3); acc1 = MFMA(__builtin_bit_cast(bf16x8, cs1), nmf, acc1); acc2 = MFMA(__builtin_bit_cast(bf16x8, cs2), nmf, acc2);
  const float sd = bfhi(stat), rstd = __builtin_amdgcn_rcpf(sd), gsc = -1.4426950408889634f * rstd, qsc = rstd * (0.25f * 1.4426950408889634f), sc0 = sj == 1 ? qsc : rstd;
  const int tile = rb * 4 + tt, hd = 8 * Q4 + (tb_ ? 0 : 4) + 2 * cg;
  const unsigned sok = WS_KV + hd * 131072 + tile * 1024, soq = WS_Q + (hd * 64 + tile) * 1024, isv = (sj + 1) & 2;
  unsigned l16k = (l16 & 0x330) | (l16 & 64) * 2 | (l16 & 128) / 2; if (sj == 1) l16k = l16; PIN(l16k);
  SBAR();
  u32x4 qf0, qf1, hp, hq; bf16x8 eye0, eye1;
  f32x16 acc3 = {}, acc4 = {};
  auto glu2 = [&](const f32x16& a, int i) {
    const float e0_ = __builtin_amdgcn_exp2f(a[i] * gsc), e1_ = __builtin_amdgcn_exp2f(a[i + 1] * gsc);
    return cvtpa(a[8 + i] * __builtin_amdgcn_rcpf(__builtin_fmaf(e0_, sd, sd)), a[9 + i] * __builtin_amdgcn_rcpf(__builtin_fmaf(e1_, sd, sd))); };
  const unsigned mk_ = lds0 + ((w + aq) & 7) * 1024, skv = WS_KV + ah * 131072 + ((w + aq) & 7) * 1024;
#define KVM(i) M0S(mk_, (i) * 8192 + ((i) >= 8 ? L_V - 65536 : 0), skv + (i) * 8192)
#define KVG() asm volatile("buffer_load_dwordx4 %0, %1, %2 offen sc1 lds" :: "v"(l16), "s"(ws), "s"(sd_) : "memory")
#define PRE2(ks) do { constexpr int c_ = 8 + (((ks) + 1) >> 3), s_ = ((ks) + 1) & 7; \
    if (s_ == 0 && c_ < 12) { SBAR(); \
      if (c_ == 9 && (w == 1 || w == 2)) colsum(lxor(w == 1 ? wp1 : wp2, half * 64) + SL(8) + (pos ? U_XA : U_G1), (half * 5 + 2 + w) * 1024); \
      if (c_ == 9) WAITV(6); else if (c_ == 10) WAITV2(1, 0); \
      LBAR(); } \
    if ((ks) == K_F) { asm volatile("s_waitcnt vmcnt(4)\n\ts_barrier" ::: "memory"); FLAG(WS_F1); } \
    if ((ks) == K_P) { vf0 = WS_F1 + Q4 * 64 * FS + l16 * (FS / 16); POLL(vf0); }     \
    if ((ks) == K_C) { WAITF(0); asm volatile("s_barrier" ::: "memory"); } \
    if ((ks) == 31) { WAITV(6); PIN(cs0); PIN(cs1); } \
    SBAR(); } while (0)
#define FILL2(ks) do { \
    if ((ks) == 0 || (ks) == 1) { const u32x4 p_ = pack8(acc0, ((ks) & 1) * 8, sc0); if ((ks) == 0) hp = p_; else hq = p_; \
      if (isv) st16(p_, ws, l16k, sj == 1 ? soq + ((ks) & 1) * 65536 : sok + ((ks) & 1) * 131072); } \
    if ((ks) == 1) { eye0 = LDS8(lds + L_EYE + l16); eye1 = LDS8(lds + L_EYE + 1024 + l16); } \
    if ((ks) == 2 && !isv) acc0 = MFMA(__builtin_bit_cast(bf16x8, hq), eye1, MFMA(__builtin_bit_cast(bf16x8, hp), eye0, (f32x16){})); \
    if ((ks) == 3 && !isv) { LANE(n_); const unsigned vvo = (n_ & 16) * 8192 + ((n_ >> 5) * 16 + (n_ & 15)) * 16; \
      st16((u32x4){cvtpk(acc0[0], acc0[1]), cvtpk(acc0[2], acc0[3]), cvtpk(acc0[4], acc0[5]), cvtpk(acc0[6], acc0[7])}, ws, vvo, sok + 65536); \
      st16((u32x4){cvtpk(acc0[8], acc0[9]), cvtpk(acc0[10], acc0[11]), cvtpk(acc0[12], acc0[13]), cvtpk(acc0[14], acc0[15])}, ws, vvo, sok + 65536 + 512); } \
    if ((ks) >= 4 && (ks) <= 6 && tb_) st16(pack8((ks) < 6 ? acc1 : acc2, ((ks) & 1) * 8, (ks) < 6 ? rstd : qsc), ws, (ks) < 6 ? l16k : l16, (ks) < 6 ? sok + ((ks) & 1) * 131072 : soq); \
    if ((ks) == 6 && tb_) st16(pack8(acc2, 8, qsc), ws, l16, soq + 65536); \
    if ((ks) >= 4 && (ks) <= 18 && !((ks) & 1) && !tb_) { \
      if ((ks) == 16) st16(hp, ws, l16, WS_H + (tile * 112 + 4 * p1 + 2 * cg) * 1024); \
      if ((ks) < 12) { hp[(((ks) >> 1) + 2) & 3] = glu2(acc1, ((ks) + 4) & 6); PIN(hp); } \
      else { hq[(((ks) >> 1) + 2) & 3] = glu2(acc2, ((ks) + 4) & 6); PIN(hq); } \
      if ((ks) == 18) st16(hq, ws, l16, WS_H + (tile * 112 + 4 * p1 + 2 * cg + 1) * 1024); } \
    if ((ks) == 26) { PIN(nm2); nmf = __builtin_bit_cast(bf16x8, (u32x4){nm2, nm2, nm2, nm2}); PIN(nmf); } \
    if ((ks) == 31) { acc3 = MFMA(__builtin_bit_cast(bf16x8, cs0), nmf, acc3); acc4 = MFMA(__builtin_bit_cast(bf16x8, cs1), nmf, acc4); } \
    } while (0)
#define STEP2(ks, WC, WN) do { constexpr int c_ = 8 + (((ks) + 1) >> 3), s_ = ((ks) + 1) & 7; \
    if ((ks) < 31) RD2A(WN, ((ks) + 1) & 31); \
    if (s_ == 7) RD2B(WN, ((ks) + 1) & 31); \
    if (c_ == 9 && s_ < 4) M02(11, s_); \
    if ((ks) == K_C + 1) KVM(0); if ((ks) == K_C + 2) KVM(8); SBAR(); \
    acc3 = MFMA(WC[0], xf[ks], acc3); SBAR(); \
    if (c_ == 9 && s_ < 4) GO2(11, s_); \
    if ((ks) == K_C) ALD4O(cs0, scs, 3072); \
    if ((ks) == K_C + 1 || (ks) == K_C + 2) KVG(); if ((ks) == K_C + 3) ALD4C(qf1, l16, ws, WS_Q + (ah * 64 + qown) * 1024); \
    if ((ks) < 31 && s_ != 7) RD2B(WN, ((ks) + 1) & 31); \
    SBAR(); FILL2(ks); \
    if ((ks) == K_C + 1) KVM(4); if ((ks) == K_C + 2) KVM(12); SBAR(); \
    acc4 = MFMA(WC[1], xf[ks], acc4); SBAR(); \
    if ((ks) == K_C) ALD4O(cs1, scs + 4096, 0); \
    if ((ks) == K_C + 1 || (ks) == K_C + 2) KVG(); if ((ks) == K_C + 3) ALD4C(qf0, l16, ws, WS_Q + (ah * 64 + (qown ^ 1)) * 1024); \
    SBAR(); PRE2((ks) + 1); } while (0)
#define STEP2x4(k) STEP2(k, wa, wb); STEP2(k + 1, wb, wa); STEP2(k + 2, wa, wb); STEP2(k + 3, wb, wa)
  STEP2x4(0); STEP2x4(4); STEP2x4(8); STEP2x4(12); STEP2x4(16); STEP2x4(20); STEP2x4(24); STEP2x4(28);
  {
    LANE(n_); const lptr s2_ = lds + L_PRIV + w * 2048 + (4 * (n_ >> 5) + ((n_ >> 2) & 3)) * 128 + (n_ & 1) * 8; const int pc_ = (((n_ >> 3) & 2) + ((n_ >> 1) & 1)) ^ ((n_ >> 1) & 4);
    hp = __builtin_bit_cast(u32x4, trfrag(s2_ + pc_ * 16, 1024)); hq = __builtin_bit_cast(u32x4, trfrag(s2_ + (pc_ ^ 4) * 16, 1024)); }
  st16(glu8(acc3, gsc, sd), ws, l16, WS_H + (tile * 112 + 4 * p2 + 2 * cg) * 1024);
  LANE(ln); const int r3 = ln & 31, hf = ln >> 5;
  const int tb = xcd * 4 + (bi & 3), n64 = bi >> 2;
  lptr kp = lds + kh * 32768 + l16, kq = lds + (kh ^ 1) * 32768 + l16;
  const bool vrow = r3 < 16;
  int vstep = vrow ? 4096 : 0;
  lptr vp = lds + (vrow ? L_V + kh * 32768 + hf * 256 : L_ONES - 256) + r3 * 16;
  int idx = 0xDDDDDDDD;
  float aone = hf ? 0.f : 1.f;
  PIN(kp); PIN(kq); PIN(vstep); PIN(vp); PIN(idx); PIN(aone);
  f32x16 nm0, nm1, o0, o1, sA, sB;
  const f32x16 z = {};
  { u32x4 zz = {}; PIN(zz); o0 = MFMA(__builtin_bit_cast(bf16x8, zz), __builtin_bit_cast(bf16x8, zz), z); PIN(o0); o1 = MFMA(__builtin_bit_cast(bf16x8, zz), __builtin_bit_cast(bf16x8, zz), o0); PIN(o1); }
  WAITBAR(1);
  PIN(qf0); PIN(qf1);
  const bf16x8 q0 = __builtin_bit_cast(bf16x8, qf0), q1 = __builtin_bit_cast(bf16x8, qf1);
  auto hmax = [&](float m) { auto rr = __builtin_amdgcn_permlane32_swap(__float_as_uint(m), __float_as_uint(m), false, false);
    return __builtin_fmaxf(__uint_as_float(rr[0]), __uint_as_float(rr[1])); };
  auto max16 = [&](float m, const f32x16& s) {
#pragma unroll
    for (int i = 0; i < 16; i += 2) m = __builtin_fmaxf(__builtin_fmaxf(m, s[i]), s[i + 1]);
    return m; };
  __builtin_amdgcn_s_setprio(1);
  bf16x8 k1 = LDS8(kp), va, vb;
  {
    const bf16x8 k0 = LDS8(kq);
    va = LDS8(vp); SBAR();
    if (ks2 < 144) { st16(hp, ws, l16, WS_W2 + (2 * n2 * 144 + ks2) * 1024); st16(hq, ws, l16, WS_W2 + ((2 * n2 + 1) * 144 + ks2) * 1024); }
    hp[0] = glu2(acc4, 0); PIN(hp); SBAR();
    const f32x16 a2 = MFMA(k1, q0, z), a0 = MFMA(k0, q0, z); SBAR();
    hp[1] = glu2(acc4, 2); PIN(hp); SBAR();
    const f32x16 a3 = MFMA(k1, q1, z), a1 = MFMA(k0, q1, z); SBAR();
    float x0 = a0[0], x2 = a2[0];
#pragma unroll
    for (int i = 1; i < 15; i += 2) { x2 = max3(x2, a2[i], a2[i + 1]); x0 = max3(x0, a0[i], a0[i + 1]); }
    nm0 = __builtin_amdgcn_mfma_f32_32x32x2f32(aone, -REF_OFF - hmax(max3(max3(x0, x2, a0[15]), a2[15], a2[15])), z, 0, 0, 0); SBAR();
    float x1 = a1[0], x3 = a3[0];
#pragma unroll
    for (int i = 1; i < 15; i += 2) { x3 = max3(x3, a3[i], a3[i + 1]); x1 = max3(x1, a1[i], a1[i + 1]); }
    float m1 = -REF_OFF - hmax(max3(max3(x1, x3, a1[15]), a3[15], a3[15])); PIN(m1); SBAR();
    sA = MFMA(k1, q0, nm0); SBAR();
    hp[2] = glu2(acc4, 4); PIN(hp); SBAR();
    nm1 = __builtin_amdgcn_mfma_f32_32x32x2f32(aone, m1, z, 0, 0, 0); SBAR();
    hp[3] = glu2(acc4, 6); PIN(hp);
    st16(hp, ws, l16, WS_H + (tile * 112 + 4 * p2 + 2 * cg + 1) * 1024); SBAR();
  }
  f32x8 pa, pb;
  f32x16 g00, g01, g10, g11;
  bf16x8 fa[4], fb[4];
  unsigned sw0 = WS_W2 + (2 * n64 * 144 + 14 * w) * 1024, sw1 = sw0 + 144 * 1024, sh0 = WS_H + ((2 * tb + kh) * 112 + 14 * w) * 1024, sh1 = WS_H + ((2 * tb + 1 - kh) * 112 + 14 * w - 1) * 1024;
#define KADV() do { sw0 += 2048; sw1 += 2048; sh0 += 2048; sh1 += 2048; asm volatile("" : "+s"(sw0), "+s"(sw1), "+s"(sh0), "+s"(sh1)); } while (0)
#define EX(P, S, i) P[(i) & 7] = __builtin_amdgcn_exp2f(S[i])
  unsigned so_;
#define UN_(...) __VA_ARGS__
#define AP_(ph, f, ...) f(ph, __VA_ARGS__)
#define AP2_(...) AP_(__VA_ARGS__)
#define MS(slot, M) do { AP2_(0, UN_ slot); SBAR(); M; SBAR(); AP2_(1, UN_ slot); SBAR(); } while (0)
#define NO(ph, x) do {} while (0)
#define DO(ph, x) do { if (ph) { x; } } while (0)
#define LD(ph, dst, base, imm) do { if (ph) dst = ld16c(ws, l16 + (imm), base); } while (0)
#define DM(ph, base, imm, off) do { if (ph) asm volatile("buffer_load_dwordx4 %0, %1, %2 offen sc1 lds" :: "v"(l16), "s"(ws), "s"(so_) : "memory"); \
    else { so_ = (off); asm volatile("s_add_i32 m0, %1, %2" :: "s"(so_), "s"(base), "i"(imm) : "memory", "scc"); } } while (0)
#define KV(ph, i) DM(ph, mk_, (i) * 8192 + ((i) >= 8 ? L_V - 65536 : 0), skv + (i) * 8192)
#define N_ (NO, 0)
#define T2(u, ...) T2X(o1 = SMF(vb, pb, o1, idx), u, __VA_ARGS__)
#define T2X(FIN, u, A3, MA, A4, B1, B2, MB, B4) do { \
    EX(pa, sA, 0); EX(pa, sA, 1); SBAR(); FIN; SBAR(); vb = LDS8(vp + (u) * 1024 + 512); SBAR(); \
    EX(pa, sA, 2); EX(pa, sA, 3); EX(pa, sA, 4); EX(pa, sA, 5); SBAR(); sB = MFMA(k1, q1, nm1); SBAR(); k1 = LDS8(kp + ((u) + 1) * 1024); SBAR(); \
    EX(pa, sA, 6); EX(pa, sA, 7); SBAR(); EX(pb, sA, 8); EX(pb, sA, 9); MS(A3, o0 = SMF(va, pa, o0, idx)); \
    EX(pb, sA, 10); EX(pb, sA, 11); EX(pb, sA, 12); EX(pb, sA, 13); MS(A4, MA); EX(pb, sA, 14); EX(pb, sA, 15); SBAR(); \
    EX(pa, sB, 0); EX(pa, sB, 1); MS(B1, o0 = SMF(vb, pb, o0, idx)); \
    EX(pa, sB, 2); EX(pa, sB, 3); EX(pa, sB, 4); EX(pa, sB, 5); MS(B2, sA = MFMA(k1, q0, nm0)); \
    EX(pa, sB, 6); EX(pa, sB, 7); SBAR(); EX(pb, sB, 8); EX(pb, sB, 9); SBAR(); o1 = SMF(va, pa, o1, idx); SBAR(); va = LDS8(vp + ((u) + 1) * 1024); SBAR(); \
    EX(pb, sB, 10); EX(pb, sB, 11); EX(pb, sB, 12); EX(pb, sB, 13); MS(B4, MB); EX(pb, sB, 14); EX(pb, sB, 15); SBAR(); } while (0)
#define NOP_ do {} while (0)
#define ADV() do { kp += 4096; vp += vstep; } while (0)
#define GM(g, wf, hf_) do { g = MFMA(hf_, wf, g); PIN(g); } while (0)
#define G2P(u, C, D, X0, X1, X2, X3, X4, X5) do { \
    T2(u, X0, GM(g00, C[0], C[2]), (LD, D[3], sh1, (u) * 512), X1, X2, GM(g10, C[1], C[2]), (LD, C[2], sh0, (u) * 512)); \
    T2(u + 1, X3, GM(g11, C[1], C[3]), (LD, C[1], sw1, (u) * 512), X4, X5, GM(g01, C[0], C[3]), (LD, C[0], sw0, (u) * 512)); } while (0)
#define PRIO_AT(k, n) asm volatile("s_cmp_lg_u32 %0, " #k "\n\ts_cbranch_scc1 1f\n\ts_setprio " #n "\n1:" :: "s"(it) : "scc")
#define PRIO(n) __builtin_amdgcn_s_setprio(n)
  T2X(NOP_, 0, (KV, 1), NOP_, N_, (KV, 5), (KV, 9), NOP_, N_); PRIO(0); T2(1, (KV, 13), NOP_, N_, (KV, 2), (KV, 6), NOP_, N_);
  asm volatile("s_waitcnt vmcnt(6)\n\ts_barrier" ::: "memory");
  FLAG(WS_F2);
  auto side0 = [&]() { vf0 = WS_F2 + ((l16 & 240) + __builtin_amdgcn_ubfe(n64 * 0x02020200u + 0x01010000u + (tb >> 1), (l16 >> 5) & 24, 4)) * FS; PIN(vf0); };
  auto side1 = [&]() { hp = (u32x4){~0u, ~0u, ~0u, ~0u}; PIN(hp); };
  bf16x8 zz;
  auto side2 = [&]() { zz = (bf16x8){}; PIN(zz); };
#define EPOCH() PUT((unsigned long long)e1 << 32 | (e0 + 1), WS_EPOCH, w | b)
#define KPIN() asm volatile("" : "+s"(sw0), "+s"(sw1), "+s"(sh0), "+s"(sh1))
  PRIO(2); T2(2, (KV, 10), EPOCH(), N_, (KV, 14), (KV, 3), side0(), N_); T2(3, (KV, 7), KPIN(), N_, (KV, 11), (KV, 15), side1(), N_); ADV();
  PRIO(1); T2(0, (DO, st16(hp, ws, l16, WS_O + (qown * 32 + ah) * 1024)), side2(), N_, N_, (DO, POLL(vf0)), NOP_, N_); PRIO(0); T2(1, N_, NOP_, N_, N_, N_, NOP_, N_);
  WAITF(0);
  asm volatile("s_waitcnt vmcnt(0)\n\ts_barrier" ::: "memory");
  auto lwo_of = [](int g) { return (unsigned)(g < 3 ? g : g < 6 ? g + 1 : g + 3) * 8192; };
  unsigned swo = WS_W2 + (2 * n64 * 144 + 112 + 4 * w) * 1024, lwo = lds0 + lwo_of(w);
  PRIO(3);
  {
#define WPIN(ph, x) do { if (ph) { vf0 = WS_F3 + xcd * 32 * FS + (l16 & 496) * (FS / 16); PIN(vf0); asm volatile("" : "+s"(swo), "+s"(lwo)); } } while (0)
#define GZ(g) do { PIN(zz); g = MFMA(zz, zz, z); PIN(g); } while (0)
    T2(2, (LD, fa[2], sh0, 0), GZ(g00), (LD, fa[0], sw0, 0), (LD, fa[1], sw1, 0), (LD, fa[3], sh1, 1024), GZ(g10), (DO, PUT(tag, WS_F3 + b * FS, w)));
    T2(3, (LD, fb[2], sh0, 1024), GZ(g11), (WPIN, 0), (LD, fb[1], sw1, 1024), N_, GZ(g01), (LD, fb[0], sw0, 1024)); ADV(); KADV(); }
#pragma unroll 1
  for (int it = 0; it < 4; ++it) { PRIO_AT(2, 2); PRIO_AT(3, 1); G2P(0, fa, fb, N_, N_, N_, N_, N_, N_); PRIO_AT(3, 0); G2P(2, fb, fa, N_, N_, N_, N_, N_, N_); ADV(); KADV(); }
#define WO(ph, i) do { if (ph) asm volatile("buffer_load_dwordx4 %0, %1, %2 offen offset:%3 sc1 lds" :: "v"(l16), "s"(ws), "s"(so_), "i"(((i) & 3) * 1024) : "memory"); \
    else if (((i) & 3) == 0) DM(0, lwo, (i) * 1024, swo + ((i) >> 2) * (144 * 1024)); } while (0)
  asm volatile("s_barrier" ::: "memory");
  PRIO(3);
  G2P(0, fa, fb, (WO, 0), (DO, POLL(vf0)), (WO, 1), (WO, 2), N_, (WO, 3)); G2P(2, fb, fa, (WO, 4), N_, (WO, 5), (WO, 6), N_, (WO, 7)); ADV(); KADV();
  PRIO(2);
  bf16x8 fc[4];
  typedef __attribute__((address_space(3))) f32x4* l4p;
  typedef __attribute__((address_space(3))) float* l1p;
  lptr xw = lds + L_PRIV + w * 2048 + l16, xl = lds + 65536 + w * 256 + l16 / 4; PIN(xw); PIN(xl);
  G2P(0, fa, fb, (LD, fc[0], sw0, 1024), N_, (LD, fc[2], sh0, 1024), (LD, fc[1], sw1, 1024), N_, (LD, fc[3], sh1, 2048));
  PRIO(1);
  T2(2, (LD, fa[3], sh1, 1024), NOP_, N_, N_, N_, NOP_, N_);
  PRIO(0);
  auto xchg = [&]() { PIN(o0); *(l4p)(xw) = (f32x4){o0[0], o0[1], o0[2], o0[3]}; *(l4p)(xw + 1024) = (f32x4){o0[4], o0[5], o0[6], o0[7]}; *(l1p)(xl) = o0[8]; };
  const int hs = (w + n64) & 3;
  lptr wl0, wl1, yw, yl;
  auto side3 = [&]() { yw = lxor(xw - L_PRIV, 8192) + L_PRIV; yl = lxor(xl, 1024); wl0 = lds + lwo_of(2 * hs) + l16; wl1 = lds + lwo_of(2 * hs + 1) + l16; PIN(yw); PIN(yl); PIN(wl0); PIN(wl1); };
  T2(3, N_, NOP_, N_, N_, (DO, side3()), xchg(), N_);
#undef GM
#define GM(g, wf, hf_) do { g = MFMA(hf_, wf, g); PIN(g); SBAR(); } while (0)
  o1 = SMF(vb, pb, o1, idx);
  SBAR();
  LBAR();
  float oo[9];
  {
    const f32x4 a = *(l4p)(yw), c = *(l4p)(yw + 1024); const float lp = *(l1p)(yl);
    SBAR(); asm volatile("" :: "v"(o1));
    oo[8] = o1[8] + lp;
    float sm = 0.f;
#pragma unroll
    for (int i = 0; i < 4; ++i) { oo[i] = o1[i] + a[i]; oo[4 + i] = o1[4 + i] + c[i]; }
#pragma unroll
    for (int i = 0; i < 8; ++i) sm += __builtin_fabsf(oo[i]);
    const bool ok = __builtin_amdgcn_fmed3f(oo[8], 0x1p-104f, 0x1p120f) == oo[8] && __builtin_amdgcn_fmed3f(sm, 0x1p-106f, 0x1p127f) == sm;
    if (__builtin_expect(__builtin_amdgcn_ballot_w64(!ok) != 0, 0)) {
      const unsigned sk = WS_KV + ah * 131072, vv = l16 & 256 ? 0x7fffff00u : (l16 & 512) / 2 + (l16 & 240), one = (l16 & 432) == 256 ? 0x3f803f80u : 0u;
      float m = -INFINITY;
#pragma unroll 1
      for (int t = 0; t < 64; ++t) m = max16(m, MFMA(ld16c(ws, l16, sk + t * 1024), q1, z));
      m = -hmax(m);
#pragma unroll
      for (int i = 0; i < 16; ++i) nm0[i] = m;
      o0 = z;
#pragma unroll 1
      for (int t = 0; t < 64; ++t) {
        const f32x16 s_ = MFMA(ld16c(ws, l16, sk + t * 1024), q1, nm0);
        const u32x4 v0 = __builtin_bit_cast(u32x4, ld16c(ws, vv, sk + 65536 + t * 1024)) | one, v1 = __builtin_bit_cast(u32x4, ld16c(ws, vv, sk + 65536 + t * 1024 + 512)) | one;
#pragma unroll
        for (int i = 0; i < 8; ++i) { pa[i] = __builtin_amdgcn_exp2f(s_[i]); pb[i] = __builtin_amdgcn_exp2f(s_[8 + i]); }
        o0 = SMF(__builtin_bit_cast(bf16x8, v0), pa, o0, idx); o0 = SMF(__builtin_bit_cast(bf16x8, v1), pb, o0, idx);
      }
      float unit = 1.f; PIN(unit);
#pragma unroll
      for (int i = 0; i < 9; ++i) oo[i] = o0[i] * unit;
    }
  }
  { const float rl = __builtin_amdgcn_rcpf(oo[8]);
    u32x4 ov = {cvtpa(oo[0] * rl, oo[1] * rl), cvtpa(oo[2] * rl, oo[3] * rl), cvtpa(oo[4] * rl, oo[5] * rl), cvtpa(oo[6] * rl, oo[7] * rl)};
#pragma unroll
    for (int i = 0; i < 4; ++i) ov[i] = ov[i] < 0xfffffffeu ? ov[i] : 0xfffffffeu;
#pragma unroll
    for (int i = 0; i < 4; ++i) { PIN(fa[i]); PIN(fb[i]); PIN(fc[i]); }
    WAITV(0);
    st16(ov, ws, l16, WS_O + (qown * 32 + ah) * 1024); }
  WAITF(1);
  asm volatile("s_barrier" ::: "memory");
  unsigned vout; u32x2 xr0, xr1; lptr rs;
  f32x4 ra, rc;
#define PUTG(g, o) asm volatile("ds_write_addtid_b32 %0 offset:%c16\n\tds_write_addtid_b32 %1 offset:%c16+256\n\tds_write_addtid_b32 %2 offset:%c16+512\n\tds_write_addtid_b32 %3 offset:%c16+768\n\t" \
      "ds_write_addtid_b32 %4 offset:%c16+1024\n\tds_write_addtid_b32 %5 offset:%c16+1280\n\tds_write_addtid_b32 %6 offset:%c16+1536\n\tds_write_addtid_b32 %7 offset:%c16+1792\n\t" \
      "ds_write_addtid_b32 %8 offset:%c16+2048\n\tds_write_addtid_b32 %9 offset:%c16+2304\n\tds_write_addtid_b32 %10 offset:%c16+2560\n\tds_write_addtid_b32 %11 offset:%c16+2816\n\t" \
      "ds_write_addtid_b32 %12 offset:%c16+3072\n\tds_write_addtid_b32 %13 offset:%c16+3328\n\tds_write_addtid_b32 %14 offset:%c16+3584\n\tds_write_addtid_b32 %15 offset:%c16+3840" \
      :: "v"(g[0]), "v"(g[1]), "v"(g[2]), "v"(g[3]), "v"(g[4]), "v"(g[5]), "v"(g[6]), "v"(g[7]), "v"(g[8]), "v"(g[9]), "v"(g[10]), "v"(g[11]), "v"(g[12]), "v"(g[13]), "v"(g[14]), "v"(g[15]), "i"(o) : "memory")
  f32x4 pa_[4], pc_[4];
#define RD4(p) do { _Pragma("unroll") for (int i = 0; i < 4; ++i) { pa_[i] = *(l4p)((p) + i * 8192); pc_[i] = *(l4p)((p) + i * 8192 + 512); } SBAR(); } while (0)
#define SUM4() do { _Pragma("unroll") for (int i = 0; i < 4; ++i) { ra += pa_[i]; rc += pc_[i]; PIN(ra); PIN(rc); } } while (0)
  {
    const unsigned soo = WS_O + ((2 * tb + kh) * 32 + 8 * hs) * 1024; unsigned spins; unsigned long long ret; u32x16 ofa, ofb;
#define OL0 "buffer_load_dwordx4 v[0:3], %[l], %[r], %[o0] offen sc1\n\t"
#define OL1 "buffer_load_dwordx4 v[4:7], %[l], %[r], %[o0] offen offset:1024 sc1\n\t"
#define OL2 "buffer_load_dwordx4 v[8:11], %[l], %[r], %[o0] offen offset:2048 sc1\n\t"
#define OL3 "buffer_load_dwordx4 v[12:15], %[l], %[r], %[o0] offen offset:3072 sc1\n\t"
#define OL4 "buffer_load_dwordx4 v[16:19], %[l], %[r], %[o1] offen sc1\n\t"
#define OL5 "buffer_load_dwordx4 v[20:23], %[l], %[r], %[o1] offen offset:1024 sc1\n\t"
#define OL6 "buffer_load_dwordx4 v[24:27], %[l], %[r], %[o1] offen offset:2048 sc1\n\t"
#define OL7 "buffer_load_dwordx4 v[28:31], %[l], %[r], %[o1] offen offset:3072 sc1\n\t"
#define OLOADS(br, c) asm volatile("s_cmp_lt_u32 %[w], 4\n\t" br " 1f\n\t" OL0 OL1 OL2 OL3 OL4 OL5 OL6 OL7 "1:" : c "{v[0:15]}"(ofa), c "{v[16:31]}"(ofb) \
      : [l] "v"(l16), [r] "s"(ws), [o0] "s"(soo), [o1] "s"(soo + 4096), [w] "s"(w) : "memory", "scc")
    bf16x8 wa_[8], wb_[8];
#define WRD(i) do { wa_[i] = LDS8(((i) < 4 ? wl0 : wl1) + ((i) & 3) * 1024); wb_[i] = LDS8(((i) < 4 ? wl0 : wl1) + ((i) & 3) * 1024 + 4096); SBAR(); } while (0)
    SBAR(); GM(g01, fb[0], fb[3]); WRD(0); WRD(1); GM(g11, fb[1], fb[3]); WRD(2); WRD(3); GM(g01, fa[0], fa[3]); WRD(4); GM(g11, fa[1], fa[3]); WRD(5); GM(g01, fc[0], fc[3]); WRD(6); GM(g11, fc[1], fc[3]); WRD(7);
    vout = (tb * 64 + w * 8) * 1024 + n64 * 128 + (l16 & 256) * 16 + (l16 & 512) * 2 + (l16 & 240) / 2;
    xr0 = __builtin_amdgcn_raw_buffer_load_b64(rx, vout, 0, 0); xr1 = __builtin_amdgcn_raw_buffer_load_b64(rx, vout + 2048, 0, 0);
    rs = lds + (kh ^ 1) * 32768 + (l16 & 128) * 32 + (w & 3) * 1024 + (l16 & 512) / 2 + (l16 & 256) / 2 + (l16 & 112); PIN(rs); SBAR();
    asm volatile("s_mov_b32 m0, %0\n\ts_nop 7\n\ts_waitcnt lgkmcnt(0)\n\ts_barrier" :: "s"(lds0 + w * 8192) : "memory");
    GM(g00, fb[0], fb[2]); PUTG(g01, 0); GM(g10, fb[1], fb[2]); GM(g00, fa[0], fa[2]); PUTG(g11, 4096); GM(g10, fa[1], fa[2]);
    OLOADS("s_cbranch_scc0", "=&");
    GM(g00, fc[0], fc[2]); GM(g10, fc[1], fc[2]);
    LBAR();
    OLOADS("s_cbranch_scc1", "+");
    RD4(rs);
#define OCHK(r0, r1, r2, r3) "v_max3_u32 v32, v" #r0 ", v" #r1 ", v" #r2 "\n\tv_max_u32 v32, v32, v" #r3 "\n\tv_cmp_eq_u32 vcc, -1, v32\n\t"
#define OT1(n, r0, r1, r2, r3) "s_waitcnt vmcnt(" #n ")\n\tv_max3_u32 v32, v" #r0 ", v" #r1 ", v" #r2 "\n\tv_max_u32 v32, v32, v" #r3 "\n\t"
#define OT2 "v_cmp_eq_u32 vcc, -1, v32\n\ts_getpc_b64 %[ret]\n\ts_cbranch_vccnz 3f\n\t"
#define OM2(a, b, c, d) "v_max3_u32 v32, v32, v" #a ", v" #b "\n\tv_max3_u32 v33, v33, v" #c ", v" #d "\n\t"
#define OMA(i, r0, r3) "v_mfma_f32_32x32x16_bf16 %[g0], v[" #r0 ":" #r3 "], %[a" #i "], %[g0]\n\t"
#define OMB(i, r0, r3) "v_mfma_f32_32x32x16_bf16 %[g1], v[" #r0 ":" #r3 "], %[b" #i "], %[g1]\n\t"
#define ORE(r0, r1, r2, r3, load) OCHK(r0, r1, r2, r3) "s_cbranch_vccz 4f\n\t" load "4:\n\t"
    static_assert(SPIN_CAP == 0x8000, "literal below");
    asm volatile("s_mov_b32 %[sp], 0\n\t"
                 OT1(7, 0, 1, 2, 3) OT2 OMA(0, 0, 3) OT1(6, 4, 5, 6, 7) OMB(0, 0, 3) OT2 OMA(1, 4, 7) OT1(5, 8, 9, 10, 11) OMB(1, 4, 7) OT2 OMA(2, 8, 11) OT1(4, 12, 13, 14, 15) OMB(2, 8, 11) OT2
                 OMA(3, 12, 15) OT1(3, 16, 17, 18, 19) OMB(3, 12, 15) OT2 OMA(4, 16, 19) OT1(2, 20, 21, 22, 23) OMB(4, 16, 19) OT2 OMA(5, 20, 23) OT1(1, 24, 25, 26, 27) OMB(5, 20, 23) OT2
                 OMA(6, 24, 27) OT1(0, 28, 29, 30, 31) OMB(6, 24, 27) OT2 OMA(7, 28, 31) OMB(7, 28, 31)
                 "s_branch 2f\n"
                 "3:\n\ts_add_u32 %[sp], %[sp], 1\n\ts_cmp_gt_u32 %[sp], 0x8000\n\ts_cbranch_scc1 2f\n\ts_waitcnt vmcnt(0)\n\t"
                 ORE(0, 1, 2, 3, OL0) ORE(4, 5, 6, 7, OL1) ORE(8, 9, 10, 11, OL2) ORE(12, 13, 14, 15, OL3)
                 ORE(16, 17, 18, 19, OL4) ORE(20, 21, 22, 23, OL5) ORE(24, 25, 26, 27, OL6) ORE(28, 29, 30, 31, OL7)
                 "s_waitcnt vmcnt(0)\n\tv_max3_u32 v32, v0, v1, v2\n\tv_max3_u32 v33, v3, v4, v5\n\t" OM2(6, 7, 8, 9) OM2(10, 11, 12, 13) OM2(14, 15, 16, 17) OM2(18, 19, 20, 21)
                 OM2(22, 23, 24, 25) OM2(26, 27, 28, 29) "v_max3_u32 v32, v32, v30, v31\n\tv_max_u32 v32, v32, v33\n\tv_cmp_eq_u32 vcc, -1, v32\n\ts_setpc_b64 %[ret]\n"
                 "2:\n\ts_waitcnt vmcnt(0)\n\ts_nop 11"
                 : [g0] "+v"(g00), [g1] "+v"(g10), "+{v[0:15]}"(ofa), "+{v[16:31]}"(ofb), [sp] "=&s"(spins), [ret] "=&s"(ret)
                 : [l] "v"(l16), [r] "s"(ws), [o0] "s"(soo), [o1] "s"(soo + 4096),
                   [a0] "v"(wa_[0]), [a1] "v"(wa_[1]), [a2] "v"(wa_[2]), [a3] "v"(wa_[3]), [a4] "v"(wa_[4]), [a5] "v"(wa_[5]), [a6] "v"(wa_[6]), [a7] "v"(wa_[7]),
                   [b0] "v"(wb_[0]), [b1] "v"(wb_[1]), [b2] "v"(wb_[2]), [b3] "v"(wb_[3]), [b4] "v"(wb_[4]), [b5] "v"(wb_[5]), [b6] "v"(wb_[6]), [b7] "v"(wb_[7])
                 : "memory", "scc", "vcc", "v32", "v33");
  }
#pragma unroll
  for (int i = 0; i < 4; ++i) { PIN(pa_[i]); PIN(pc_[i]); }
  asm volatile("s_mov_b32 m0, %0\n\ts_nop 0" :: "s"(lds0 + w * 8192 + 8188) : "memory");
  PUTG(g00, 65536 - 8188); PUTG(g10, 65536 - 8188 + 4096);
  ra = (f32x4){bflo(xr0[0]), bfhi(xr0[0]), bflo(xr0[1]), bfhi(xr0[1])}; rc = (f32x4){bflo(xr1[0]), bfhi(xr1[0]), bflo(xr1[1]), bfhi(xr1[1])}; PIN(ra); PIN(rc);
  SUM4();
  rs = lxor(rs, 98304); PIN(rs);
  LBAR();
  RD4(rs); SUM4();
  { const rsrc_t ro = mk(pout, 2048 * 1024);
    __builtin_amdgcn_raw_buffer_store_b64((u32x2){cvtpk(ra[0], ra[1]), cvtpk(ra[2], ra[3])}, ro, vout, 0, 16);
    __builtin_amdgcn_raw_buffer_store_b64((u32x2){cvtpk(rc[0], rc[1]), cvtpk(rc[2], rc[3])}, ro, vout + 2048, 0, 16); }
  asm volatile(".Lfb_end:");
}

extern "C" void kernel_launch(void* const* d_in, const int* in_sizes, int n_in, void* d_out, int out_size, void* d_ws, size_t ws_size, hipStream_t stream) {
  (void)hipFuncSetAttribute((const void*)fused_block, hipFuncAttributeMaxDynamicSharedMemorySize, LDS_BYTES);
  hipLaunchKernelGGL(fused_block, dim3(256), dim3(512), LDS_BYTES, stream, d_in[0], d_in[1], d_in[2], d_in[4], d_in[5], d_in[6], d_out, d_ws);
}
```
